```python
import torch

def _softmax(x, axis=-1):
    return torch.softmax(x, dim=axis)
def _gelu(x):
    return torch.nn.functional.gelu(x, approximate="tanh")
def _silu(x):
    return torch.nn.functional.silu(x)
def _sigmoid(x):
    return torch.sigmoid(x)
def _layernorm(x, axis=-1, eps=1e-5):
    m = x.mean(dim=axis, keepdim=True)
    v = x.var(dim=axis, keepdim=True, unbiased=False)
    return (x - m) / torch.sqrt(v + eps)
def _scatter_add(tgt, idx, src, axis=0):
    return tgt.clone().index_add_(axis, idx, src)
def _segment_sum(src, seg, n):
    out = torch.zeros((n,) + src.shape[1:], dtype=src.dtype)
    return out.index_add_(0, seg, src)
def _pad(x, pw):
    flat = tuple(v for pair in reversed(pw) for v in pair)
    return torch.nn.functional.pad(x, flat)

kernel_name = 'interleave_layers__linear_kernel_hedgehog__mixer_s_mlp__prompt'

def setup_inputs():

    _w = (lambda a: (lambda t: t.to(torch.bfloat16) if t.is_floating_point() else t)(torch.as_tensor(a)))
    def _A_setup():
        B, S, D, H = 1, 2048, 512, 32
        Dh = D // H
        return {
            "x": _w((lambda *s: torch.randn(*s, dtype=torch.bfloat16) * 0.05)(B, S, D)),
            "wqkv": _w((lambda *s: torch.randn(*s, dtype=torch.bfloat16) * 0.2)(D, 3 * D)),
            "w_phi": _w((lambda *s: torch.randn(*s, dtype=torch.bfloat16) * 0.2)(H, Dh, Dh) * (Dh ** -0.5)),
            "wo": _w((lambda *s: torch.randn(*s, dtype=torch.bfloat16) * 0.05)(D, D)),
        }
    _A_in = _A_setup()

    def _B_setup():
        B, S, D, F = 1, 2048, 512, 1376
        return {
            "x":  _w((lambda *s: torch.randn(*s, dtype=torch.bfloat16) * 0.05)(B, S, D)),
            "w1": _w((lambda *s: torch.randn(*s, dtype=torch.bfloat16) * 3.2)(D, D) * (D ** -0.5)),
            "w2": _w((lambda *s: torch.randn(*s, dtype=torch.bfloat16) * 3.2)(D, D) * (D ** -0.5)),
            "w3": _w((lambda *s: torch.randn(*s, dtype=torch.bfloat16) * 3.2)(D, F) * (D ** -0.5)),
            "w4": _w((lambda *s: torch.randn(*s, dtype=torch.bfloat16) * 3.2)(F, D) * (F ** -0.5)),
        }
    _B_in = _B_setup()

    return {
        "A_x": _A_in["x"],
        "A_wqkv": _A_in["wqkv"],
        "A_w_phi": _A_in["w_phi"],
        "A_wo": _A_in["wo"],
        "B_x": _B_in["x"],
        "B_w1": _B_in["w1"],
        "B_w2": _B_in["w2"],
        "B_w3": _B_in["w3"],
        "B_w4": _B_in["w4"],
    }

def reference(A_x, A_wqkv, A_w_phi, A_wo, B_x, B_w1, B_w2, B_w3, B_w4):
    def _A_ref(x, wqkv, w_phi, wo):
        B, S, D = x.shape
        H = 32; Dh = D // H
        qkv = (lambda x, *d: x.permute(*d))((x @ wqkv).reshape(B, S, 3, H, Dh), 2, 0, 3, 1, 4)
        q, k, v = qkv[0], qkv[1], qkv[2]
        pq = torch.einsum("bhsd,hdf->bhsf", q, w_phi)
        pk = torch.einsum("bhsd,hdf->bhsf", k, w_phi)
        phi_q = torch.concatenate([_softmax(pq, axis=-1), _softmax(-pq, axis=-1)], axis=-1)
        phi_k = torch.concatenate([_softmax(pk, axis=-1), _softmax(-pk, axis=-1)], axis=-1)
        kv = torch.einsum("bhsf,bhsd->bhfd", phi_k, v)
        z  = torch.einsum("bhsf->bhf", phi_k)
        num = torch.einsum("bhqf,bhfd->bhqd", phi_q, kv)
        den = torch.einsum("bhqf,bhf->bhq", phi_q, z)[..., None] + 1e-6
        o = num / den
        return (lambda x, *d: x.permute(*d))(o, 0, 2, 1, 3).reshape(B, S, D) @ wo

    def _B_ref(x, w1, w2, w3, w4):
        B, S, D = x.shape
        h = _gelu(_layernorm(x, axis=-1) @ w1)
        c = D // 4
        g0 = _pad(h[:, :, 0*c:1*c], ((0, 0), (1, 0), (0, 0)))[:, :S, :]
        g1 = _pad(h[:, :, 1*c:2*c], ((0, 0), (0, 1), (0, 0)))[:, 1:, :]
        g2 = _pad(h[:, :, 2*c:3*c], ((0, 0), (2, 0), (0, 0)))[:, :S, :]
        g3 = _pad(h[:, :, 3*c:   ], ((0, 0), (0, 2), (0, 0)))[:, 2:, :]
        hs = torch.concatenate([g0, g1, g2, g3], axis=-1) @ w2
        x1 = x + hs
        h2 = _gelu(_layernorm(x1, axis=-1) @ w3) @ w4
        return x1 + h2

    x = A_x
    x = x + _A_ref(_layernorm(x, axis=-1), A_wqkv, A_w_phi, A_wo)
    x = x + _B_ref(_layernorm(x, axis=-1), B_w1, B_w2, B_w3, B_w4)
    x = x + _A_ref(_layernorm(x, axis=-1), A_wqkv, A_w_phi, A_wo)
    x = x + _B_ref(_layernorm(x, axis=-1), B_w1, B_w2, B_w3, B_w4)
    return x
```

```cpp
#include <hip/hip_runtime.h>
#include <hip/hip_bf16.h>
#include <stdint.h>

typedef unsigned short u16;
typedef short s16x4 __attribute__((ext_vector_type(4)));
typedef short s16x8 __attribute__((ext_vector_type(8)));
typedef __bf16 bf16x8 __attribute__((ext_vector_type(8)));
typedef __bf16 bf16x2 __attribute__((ext_vector_type(2)));
typedef float f32x4 __attribute__((ext_vector_type(4)));
typedef float f32x2 __attribute__((ext_vector_type(2)));
typedef unsigned u32x4 __attribute__((ext_vector_type(4)));
typedef unsigned u32x2 __attribute__((ext_vector_type(2)));
typedef __attribute__((address_space(3))) s16x4* lds_s16x4_p;

constexpr int S_ = 2048, D_ = 512, F_ = 1376, FP_ = 1408;

#define DEVI __device__ __forceinline__

DEVI unsigned pack2(float a, float b) {
  bf16x2 v = {(__bf16)a, (__bf16)b};
  return __builtin_bit_cast(unsigned, v);
}
DEVI float bfr(float a) { return (float)(__bf16)a; }
DEVI float bflo(unsigned u) { return __uint_as_float(u << 16); }
DEVI float bfhi(unsigned u) { return __uint_as_float(u & 0xffff0000u); }

template <int N> DEVI void wait_vm() { asm volatile("s_waitcnt vmcnt(%0)" ::"n"(N) : "memory"); }
DEVI void wait_lgkm0() { asm volatile("s_waitcnt lgkmcnt(0)" ::: "memory"); }
DEVI void bar() { __builtin_amdgcn_s_barrier(); }

DEVI void glds16(const void* g, char* l) {
  __builtin_amdgcn_global_load_lds((const unsigned*)g, (unsigned*)l, 16, 0, 0);
}
DEVI s16x4 trread(const char* l) { return __builtin_amdgcn_ds_read_tr16_b64_v4i16((lds_s16x4_p)l); }
DEVI f32x4 mfma32(s16x8 a, s16x8 b, f32x4 c) {
  return __builtin_amdgcn_mfma_f32_16x16x32_bf16(__builtin_bit_cast(bf16x8, a), __builtin_bit_cast(bf16x8, b), c, 0, 0, 0);
}
DEVI f32x4 mfma16(s16x4 a, s16x4 b, f32x4 c) { return __builtin_amdgcn_mfma_f32_16x16x16bf16_1k(a, b, c, 0, 0, 0); }
DEVI s16x8 cat(s16x4 a, s16x4 b) { return __builtin_shufflevector(a, b, 0, 1, 2, 3, 4, 5, 6, 7); }
DEVI s16x4 pack4(float a, float b, float c, float d) {
  u32x2 v = {pack2(a, b), pack2(c, d)};
  return __builtin_bit_cast(s16x4, v);
}
DEVI float gelu_t(float x) {
  float u = 0.7978845608028654f * (x + 0.044715f * x * x * x);
  float e = __expf(-2.0f * u);
  return x * __builtin_amdgcn_rcpf(1.0f + e);
}
DEVI float xsum_g(float v) {
  v += __shfl_xor(v, 16);
  v += __shfl_xor(v, 32);
  return v;
}

struct StatRaw { f32x4 a, b; };
DEVI StatRaw stat_load(const float* stats, int row, int g) {
  const f32x4* p = (const f32x4*)(stats + (size_t)row * 32 + g * 8);
  StatRaw r; r.a = p[0]; r.b = p[1];
  return r;
}
DEVI void stat_fin(const StatRaw& r, float& mean, float& var) {
  float s = xsum_g(r.a[0] + r.a[2] + r.b[0] + r.b[2]);
  mean = s * (1.0f / 16.0f);
  float d0 = r.a[0] - mean, d1 = r.a[2] - mean, d2 = r.b[0] - mean, d3 = r.b[2] - mean;
  float m2 = (r.a[1] + r.a[3] + r.b[1] + r.b[3]) + 32.0f * (d0 * d0 + d1 * d1 + d2 * d2 + d3 * d3);
  m2 = xsum_g(m2);
  var = m2 * (1.0f / 512.0f);
}
DEVI void stat_emit(float* stats, int row, int cg, const float (&v)[8], int g) {
  float s = ((v[0] + v[1]) + (v[2] + v[3])) + ((v[4] + v[5]) + (v[6] + v[7]));
  s = xsum_g(s);
  float mean = s * (1.0f / 32.0f);
  float q = 0.f;
#pragma unroll
  for (int i = 0; i < 8; ++i) { float d = v[i] - mean; q += d * d; }
  q = xsum_g(q);
  if (g == 0) { f32x2 o = {mean, q}; *(f32x2*)(stats + (size_t)row * 32 + cg * 2) = o; }
}

__global__ __launch_bounds__(256) void k0_prep(const u16* __restrict__ x, const u16* __restrict__ wqkv,
                                               const u16* __restrict__ w1, const u16* __restrict__ w3,
                                               float* __restrict__ kvacc, u16* __restrict__ hpad,
                                               float* __restrict__ cs_qkv, float* __restrict__ cs_w1,
                                               float* __restrict__ cs_w3, float* __restrict__ stats) {
  __shared__ float red[4][32];
  const int b = blockIdx.x, tid = threadIdx.x, lane = tid & 63, wave = tid >> 6;
  if (tid < 36) { f32x4 z = {0, 0, 0, 0}; ((f32x4*)kvacc)[b * 36 + tid] = z; }
  if (b == 255) {
    u32x4 z = {0, 0, 0, 0};
    int r = tid >> 6, c = tid & 63;
    int row = (r < 2) ? r : (2048 + r);
    *(u32x4*)(hpad + (size_t)row * 512 + c * 8) = z;
  }
#pragma unroll
  for (int i = 0; i < 2; ++i) {
    int row = b * 8 + wave * 2 + i;
    u32x4 v = *(const u32x4*)(x + (size_t)row * 512 + lane * 8);
    float f[8];
#pragma unroll
    for (int j = 0; j < 4; ++j) { f[2 * j] = bflo(v[j]); f[2 * j + 1] = bfhi(v[j]); }
    float s = ((f[0] + f[1]) + (f[2] + f[3])) + ((f[4] + f[5]) + (f[6] + f[7]));
    s += __shfl_xor(s, 1); s += __shfl_xor(s, 2);
    float mean = s * (1.0f / 32.0f);
    float q = 0.f;
#pragma unroll
    for (int j = 0; j < 8; ++j) { float d = f[j] - mean; q += d * d; }
    q += __shfl_xor(q, 1); q += __shfl_xor(q, 2);
    if ((lane & 3) == 0) { f32x2 o = {mean, q}; *(f32x2*)(stats + (size_t)row * 32 + (lane >> 2) * 2) = o; }
  }
  if (b < 107) {
    const u16* W; int ld, c0; float* out;
    if (b < 48) { W = wqkv; ld = 1536; c0 = b * 32; out = cs_qkv; }
    else if (b < 64) { W = w1; ld = 512; c0 = (b - 48) * 32; out = cs_w1; }
    else { W = w3; ld = F_; c0 = (b - 64) * 32; out = cs_w3; }
    int rg = tid >> 2, cq = tid & 3;
    float a[8] = {0, 0, 0, 0, 0, 0, 0, 0};
    u32x4 v[8];
#pragma unroll
    for (int i = 0; i < 8; ++i) v[i] = *(const u32x4*)(W + (size_t)(rg + 64 * i) * ld + c0 + cq * 8);
#pragma unroll
    for (int i = 0; i < 8; ++i)
#pragma unroll
      for (int j = 0; j < 4; ++j) { a[2 * j] += bflo(v[i][j]); a[2 * j + 1] += bfhi(v[i][j]); }
#pragma unroll
    for (int j = 0; j < 8; ++j) {
      a[j] += __shfl_xor(a[j], 4); a[j] += __shfl_xor(a[j], 8);
      a[j] += __shfl_xor(a[j], 16); a[j] += __shfl_xor(a[j], 32);
    }
    if (lane < 4) {
#pragma unroll
      for (int j = 0; j < 8; ++j) red[wave][lane * 8 + j] = a[j];
    }
    __syncthreads();
    if (tid < 32) out[c0 + tid] = (red[0][tid] + red[1][tid]) + (red[2][tid] + red[3][tid]);
  }
}

constexpr int BIG_SLOT = 28672;
template <int NCH, int RING, bool FLIP2>
DEVI void core_big(char* lds, const u16* aptr  , size_t lda, const u16* bp0, const u16* bp1,
                   const u16* bp2, size_t ldw, f32x4 (&acc)[4][3], int wave, int lane) {
  const int wm = wave >> 1, wn = wave & 1, g = lane >> 4, q = (lane & 15) >> 2, p = lane & 3;
  auto issue = [&](int c) {
    char* s = lds + (c % RING) * BIG_SLOT;
#pragma unroll
    for (int i = 0; i < 4; ++i) glds16(aptr + (size_t)(8 * i) * lda + c * 64, s + (wave * 4 + i) * 1024);
    glds16(bp0 + (size_t)(c * 64) * ldw, s + 16384 + wave * 1024);
    glds16(bp1 + (size_t)(c * 64) * ldw, s + 16384 + 4096 + wave * 1024);
    glds16(bp2 + (size_t)(c * 64) * ldw, s + 16384 + 8192 + wave * 1024);
  };
  const int xoff = (64 * wm + (lane & 15)) * 128;
  const int woff = 16384 + (8 * g + q) * 64 + ((wn ^ (g & 1)) * 32) + p * 8;
#pragma unroll
  for (int c = 0; c < RING && c < NCH; ++c) issue(c);
#pragma unroll
  for (int c = 0; c < NCH; ++c) {
    constexpr int PER = 7;
    {
      int last = (c == 0) ? (RING - 1) : (c - 2 + RING);
      if (last > NCH - 1) last = NCH - 1;
      int allow = (last - c) * PER;
      switch (allow) {
        case 0: wait_vm<0>(); break;
        case 7: wait_vm<7>(); break;
        case 14: wait_vm<14>(); break;
        case 21: wait_vm<21>(); break;
        case 28: wait_vm<28>(); break;
        default: wait_vm<0>(); break;
      }
    }
    wait_lgkm0();
    bar();
    if (c >= 1 && c - 1 + RING < NCH) issue(c - 1 + RING);
    const char* s = lds + (c % RING) * BIG_SLOT;
#pragma unroll
    for (int ks = 0; ks < 2; ++ks) {
      s16x8 xf[4], wf[3];
#pragma unroll
      for (int mt = 0; mt < 4; ++mt)
        xf[mt] = *(const s16x8*)(s + xoff + mt * 2048 + (((4 * ks + g) ^ (lane & 7)) * 16));
#pragma unroll
      for (int j = 0; j < 3; ++j) {
        s16x4 lo = trread(s + woff + j * 4096 + ks * 2048);
        s16x4 hi = trread(s + woff + j * 4096 + ks * 2048 + 256);
        wf[j] = cat(lo, hi);
      }
#pragma unroll
      for (int mt = 0; mt < 4; ++mt)
#pragma unroll
        for (int j = 0; j < 3; ++j) {
          if (FLIP2 && j == 2) acc[mt][j] = mfma32(xf[mt], wf[j], acc[mt][j]);
          else acc[mt][j] = mfma32(wf[j], xf[mt], acc[mt][j]);
        }
    }
  }
}

template <int NCH, int RING, class AF, class BF>
DEVI void core_small(char* lds, AF af, size_t lda, BF bf, f32x4 (&acc)[2][2], int wave, int lane) {
  const int wm = wave >> 1, wn = wave & 1, g = lane >> 4, q = (lane & 15) >> 2, p = lane & 3;
  auto issue = [&](int c) {
    char* s = lds + (c % RING) * 16384;
    const u16* a = af(c);
    glds16(a, s + (wave * 2) * 1024);
    glds16(a + 8 * lda, s + (wave * 2 + 1) * 1024);
    glds16(bf(c, 0), s + 8192 + (wave * 2) * 1024);
    glds16(bf(c, 1), s + 8192 + (wave * 2 + 1) * 1024);
  };
  const int xoff = (32 * wm + (lane & 15)) * 128;
  const int fsw = (q >> 1) | ((g & 1) << 1);
  const int woff = 8192 + (8 * g + q) * 128 + p * 8;
#pragma unroll
  for (int c = 0; c < RING && c < NCH; ++c) issue(c);
#pragma unroll
  for (int c = 0; c < NCH; ++c) {
    {
      int last = (c == 0) ? (RING - 1) : (c - 2 + RING);
      if (last > NCH - 1) last = NCH - 1;
      int allow = (last - c) * 4;
      switch (allow) {
        case 0: wait_vm<0>(); break;
        case 4: wait_vm<4>(); break;
        case 8: wait_vm<8>(); break;
        case 12: wait_vm<12>(); break;
        case 16: wait_vm<16>(); break;
        case 20: wait_vm<20>(); break;
        case 24: wait_vm<24>(); break;
        case 28: wait_vm<28>(); break;
        case 32: wait_vm<32>(); break;
        default: wait_vm<0>(); break;
      }
    }
    wait_lgkm0();
    bar();
    if (c >= 1 && c - 1 + RING < NCH) issue(c - 1 + RING);
    const char* s = lds + (c % RING) * 16384;
#pragma unroll
    for (int ks = 0; ks < 2; ++ks) {
      s16x8 xf[2], wf[2];
#pragma unroll
      for (int mt = 0; mt < 2; ++mt)
        xf[mt] = *(const s16x8*)(s + xoff + mt * 2048 + (((4 * ks + g) ^ (lane & 7)) * 16));
#pragma unroll
      for (int nt = 0; nt < 2; ++nt) {
        const char* w = s + woff + ks * 4096 + (((2 * wn + nt) ^ fsw) * 32);
        wf[nt] = cat(trread(w), trread(w + 512));
      }
#pragma unroll
      for (int mt = 0; mt < 2; ++mt)
#pragma unroll
        for (int nt = 0; nt < 2; ++nt) acc[mt][nt] = mfma32(wf[nt], xf[mt], acc[mt][nt]);
    }
  }
}

__global__ __launch_bounds__(256, 1) void k1_qkv(const u16* __restrict__ x, const u16* __restrict__ wqkv,
                                                 const u16* __restrict__ wphi, const float* __restrict__ cs,
                                                 const float* __restrict__ stats, u16* __restrict__ phiq,
                                                 float* __restrict__ kvacc) {
  __shared__ __attribute__((aligned(16))) char lds[5 * BIG_SLOT];
  const int tid = threadIdx.x, lane = tid & 63, wave = __builtin_amdgcn_readfirstlane(tid >> 6);
  const int xcd = blockIdx.x & 7, idx = blockIdx.x >> 3;
  const int rb = xcd * 2 + (idx & 1), hp = idx >> 1;
  const int wm = wave >> 1, wn = wave & 1, g = lane >> 4, m = lane & 15;
  const int row0 = rb * 128, head = hp * 2 + wn;
  StatRaw sr[4];
#pragma unroll
  for (int mt = 0; mt < 4; ++mt) sr[mt] = stat_load(stats, row0 + 64 * wm + 16 * mt + m, g);
  f32x4 csq = *(const f32x4*)(cs + head * 16 + 4 * g);
  f32x4 csk = *(const f32x4*)(cs + 512 + head * 16 + 4 * g);
  float csv = cs[1024 + head * 16 + m];
  s16x4 wp;
#pragma unroll
  for (int j = 0; j < 4; ++j) wp[j] = (short)wphi[head * 256 + (4 * g + j) * 16 + m];

  const u16* aptr = x + (size_t)(row0 + 32 * wave + (lane >> 3)) * 512 + (((lane & 7) ^ (lane >> 3)) * 8);
  const int bch = ((lane & 3) ^ (((lane >> 5) & 1) << 1)) * 8;
  const u16* bp = wqkv + (size_t)(16 * wave + (lane >> 2)) * 1536 + hp * 32 + bch;
  f32x4 acc[4][3];
#pragma unroll
  for (int i = 0; i < 4; ++i)
#pragma unroll
    for (int j = 0; j < 3; ++j) acc[i][j] = f32x4{0, 0, 0, 0};
  core_big<8, 5, true>(lds, aptr, 512, bp, bp + 512, bp + 1024, 1536, acc, wave, lane);

  f32x4 kvp = {0, 0, 0, 0}, kvm = {0, 0, 0, 0};
  float zp = 0.f, zm = 0.f;
#pragma unroll
  for (int mt = 0; mt < 4; ++mt) {
    float mean, var;
    stat_fin(sr[mt], mean, var);
    float rstd = __builtin_amdgcn_rsqf(var + 1e-5f);
    float qv[4], kv_[4], vv[4];
#pragma unroll
    for (int r = 0; r < 4; ++r) {
      qv[r] = rstd * (acc[mt][0][r] - mean * csq[r]);
      kv_[r] = rstd * (acc[mt][1][r] - mean * csk[r]);
      float mr = __shfl(mean, 4 * g + r), rr = __shfl(rstd, 4 * g + r);
      vv[r] = rr * (acc[mt][2][r] - mr * csv);
    }
    s16x4 qf = pack4(qv[0], qv[1], qv[2], qv[3]);
    s16x4 kf = pack4(kv_[0], kv_[1], kv_[2], kv_[3]);
    s16x4 vf = pack4(vv[0], vv[1], vv[2], vv[3]);
    f32x4 zero = {0, 0, 0, 0};
    f32x4 pq = mfma16(wp, qf, zero);
    f32x4 pk = mfma16(kf, wp, zero);
    {
      float a[4];
#pragma unroll
      for (int r = 0; r < 4; ++r) a[r] = bfr(pq[r]);
      float mx = fmaxf(fmaxf(a[0], a[1]), fmaxf(a[2], a[3]));
      float mn = fminf(fminf(a[0], a[1]), fminf(a[2], a[3]));
      mx = fmaxf(mx, __shfl_xor(mx, 16)); mx = fmaxf(mx, __shfl_xor(mx, 32));
      mn = fminf(mn, __shfl_xor(mn, 16)); mn = fminf(mn, __shfl_xor(mn, 32));
      float ep[4], em[4], sp = 0.f, sm = 0.f;
#pragma unroll
      for (int r = 0; r < 4; ++r) { ep[r] = __expf(a[r] - mx); em[r] = __expf(mn - a[r]); sp += ep[r]; sm += em[r]; }
      sp = xsum_g(sp); sm = xsum_g(sm);
      float ip = __builtin_amdgcn_rcpf(sp), im = __builtin_amdgcn_rcpf(sm);
      u32x4 o = {pack2(ep[0] * ip, ep[1] * ip), pack2(ep[2] * ip, ep[3] * ip), pack2(em[0] * im, em[1] * im),
                 pack2(em[2] * im, em[3] * im)};
      *(u32x4*)(phiq + (size_t)(row0 + 64 * wm + 16 * mt + m) * 1024 + head * 32 + 8 * g) = o;
    }
    {
      float fp[4], fm[4];
#pragma unroll
      for (int r = 0; r < 4; ++r) {
        float a = bfr(pk[r]);
        float mx = a, mn = a;
#pragma unroll
        for (int o = 1; o < 16; o <<= 1) { mx = fmaxf(mx, __shfl_xor(mx, o)); mn = fminf(mn, __shfl_xor(mn, o)); }
        float ep = __expf(a - mx), em = __expf(mn - a);
        float sp = ep, sm = em;
#pragma unroll
        for (int o = 1; o < 16; o <<= 1) { sp += __shfl_xor(sp, o); sm += __shfl_xor(sm, o); }
        fp[r] = bfr(ep * __builtin_amdgcn_rcpf(sp));
        fm[r] = bfr(em * __builtin_amdgcn_rcpf(sm));
        zp += fp[r]; zm += fm[r];
      }
      s16x4 pf = pack4(fp[0], fp[1], fp[2], fp[3]);
      s16x4 mf = pack4(fm[0], fm[1], fm[2], fm[3]);
      kvp = mfma16(pf, vf, kvp);
      kvm = mfma16(mf, vf, kvm);
    }
  }
  zp = xsum_g(zp); zm = xsum_g(zm);
  __syncthreads();
  float* red = (float*)lds;
  if (wm == 1) {
    float* r = red + wn * 640;
#pragma unroll
    for (int i = 0; i < 4; ++i) { r[i * 64 + lane] = kvp[i]; r[(4 + i) * 64 + lane] = kvm[i]; }
    r[512 + lane] = zp; r[576 + lane] = zm;
  }
  __syncthreads();
  if (wm == 0) {
    const float* r = red + wn * 640;
    float* dst = kvacc + head * 576;
#pragma unroll
    for (int i = 0; i < 4; ++i) {
      atomicAdd(dst + i * 64 + lane, kvp[i] + r[i * 64 + lane]);
      atomicAdd(dst + (4 + i) * 64 + lane, kvm[i] + r[(4 + i) * 64 + lane]);
    }
    zp += r[512 + lane]; zm += r[576 + lane];
    if (lane < 32) {
      int f = lane & 15, mi = lane >> 4;
      atomicAdd(dst + 512 + 8 * (f >> 2) + (f & 3) + 4 * mi, mi ? zm : zp);
    }
  }
}

__global__ __launch_bounds__(256, 1) void k2_wo(const u16* __restrict__ x, const u16* __restrict__ phiq,
                                                const float* __restrict__ kvacc, const u16* __restrict__ wo,
                                                u16* __restrict__ xo, float* __restrict__ stats) {
  constexpr int RING = 7;
  __shared__ __attribute__((aligned(16))) char lds[RING * 16384 + 32768];
  char* oL = lds + RING * 16384;
  const int tid = threadIdx.x, lane = tid & 63, wave = __builtin_amdgcn_readfirstlane(tid >> 6);
  const int xcd = blockIdx.x & 7, idx = blockIdx.x >> 3;
  const int rb = xcd * 8 + (idx & 7), cb = idx >> 3;
  const int g = lane >> 4, m = lane & 15, q = m >> 2, p = lane & 3;
  const int row0 = rb * 32, col0 = cb * 128;

  u32x2 xr[2][2];
#pragma unroll
  for (int mt = 0; mt < 2; ++mt)
#pragma unroll
    for (int nt = 0; nt < 2; ++nt)
      xr[mt][nt] = *(const u32x2*)(x + (size_t)(row0 + 16 * mt + m) * 512 + col0 + 32 * wave + 16 * nt + 4 * g);

  float kvr[8][8];
  f32x4 zr[8][2];
  s16x8 pf[8][2];
#pragma unroll
  for (int hh = 0; hh < 8; ++hh) {
    const int h = wave * 8 + hh;
    const float* kp = kvacc + h * 576;
#pragma unroll
    for (int i = 0; i < 8; ++i) kvr[hh][i] = kp[i * 64 + lane];
    zr[hh][0] = *(const f32x4*)(kp + 512 + 8 * g);
    zr[hh][1] = *(const f32x4*)(kp + 512 + 8 * g + 4);
#pragma unroll
    for (int mt = 0; mt < 2; ++mt)
      pf[hh][mt] = *(const s16x8*)(phiq + (size_t)(row0 + 16 * mt + m) * 1024 + h * 32 + 8 * g);
  }
  const int krow = 16 * wave + (lane >> 3);
  auto issue = [&](int c) {
    char* s = lds + (c % RING) * 16384;
#pragma unroll
    for (int i = 0; i < 2; ++i) {
      int kr = krow + 8 * i;
      int ch = (lane & 7) ^ (((kr >> 1) & 3) << 1);
      const u16* src = wo + (size_t)(c * 64 + kr) * 512 + col0 + ch * 8;
      glds16(src, s + (wave * 2 + i) * 1024);
      glds16(src + 64, s + 8192 + (wave * 2 + i) * 1024);
    }
  };
#pragma unroll
  for (int c = 0; c < RING; ++c) issue(c);

#pragma unroll
  for (int hp = 0; hp < 4; ++hp) {
    float ov[2][8];
#pragma unroll
    for (int hs = 0; hs < 2; ++hs) {
      const int hh = hp * 2 + hs;
      u32x4 kq = {pack2(kvr[hh][0], kvr[hh][1]), pack2(kvr[hh][2], kvr[hh][3]), pack2(kvr[hh][4], kvr[hh][5]),
                  pack2(kvr[hh][6], kvr[hh][7])};
      u32x4 zq = {pack2(zr[hh][0][0], zr[hh][0][1]), pack2(zr[hh][0][2], zr[hh][0][3]),
                  pack2(zr[hh][1][0], zr[hh][1][1]), pack2(zr[hh][1][2], zr[hh][1][3])};
      s16x8 kf = __builtin_bit_cast(s16x8, kq), zf = __builtin_bit_cast(s16x8, zq);
      f32x4 zero = {0, 0, 0, 0};
#pragma unroll
      for (int mt = 0; mt < 2; ++mt) {
        f32x4 num = mfma32(kf, pf[hh][mt], zero);
        f32x4 den = mfma32(zf, pf[hh][mt], zero);
        float inv = __builtin_amdgcn_rcpf(den[0] + 1e-6f);
#pragma unroll
        for (int r = 0; r < 4; ++r) ov[mt][hs * 4 + r] = num[r] * inv;
      }
    }
#pragma unroll
    for (int mt = 0; mt < 2; ++mt) {
      u32x4 o = {pack2(ov[mt][0], ov[mt][1]), pack2(ov[mt][2], ov[mt][3]), pack2(ov[mt][4], ov[mt][5]),
                 pack2(ov[mt][6], ov[mt][7])};
      *(u32x4*)(oL + ((mt * 16 + wave * 4 + hp) * 64 + lane) * 16) = o;
    }
  }
  f32x4 acc[2][2];
#pragma unroll
  for (int i = 0; i < 2; ++i)
#pragma unroll
    for (int j = 0; j < 2; ++j) acc[i][j] = f32x4{0, 0, 0, 0};
  const int fsw = (q >> 1) | ((g & 1) << 1);
  const int woff = (wave >> 1) * 8192 + (4 * g + q) * 128 + p * 8;
#pragma unroll
  for (int c = 0; c < 8; ++c) {
    {
      int last = (c == 0) ? (RING - 1) : (c - 2 + RING);
      if (last > 7) last = 7;
      int allow = (last - c) * 4;
      switch (allow) {
        case 0: wait_vm<0>(); break;
        case 4: wait_vm<4>(); break;
        case 8: wait_vm<8>(); break;
        case 12: wait_vm<12>(); break;
        case 16: wait_vm<16>(); break;
        case 20: wait_vm<20>(); break;
        case 24: wait_vm<24>(); break;
        default: wait_vm<0>(); break;
      }
    }
    wait_lgkm0();
    bar();
    if (c >= 1 && c - 1 + RING < 8) issue(c - 1 + RING);
    const char* s = lds + (c % RING) * 16384;
#pragma unroll
    for (int ks = 0; ks < 2; ++ks) {
      s16x8 xf[2], wf[2];
#pragma unroll
      for (int mt = 0; mt < 2; ++mt) xf[mt] = *(const s16x8*)(oL + ((mt * 16 + c * 2 + ks) * 64 + lane) * 16);
#pragma unroll
      for (int nt = 0; nt < 2; ++nt) {
        const char* w = s + woff + ks * 4096 + (((2 * (wave & 1) + nt) ^ fsw) * 32);
        wf[nt] = cat(trread(w), trread(w + 2048));
      }
#pragma unroll
      for (int mt = 0; mt < 2; ++mt)
#pragma unroll
        for (int nt = 0; nt < 2; ++nt) acc[mt][nt] = mfma32(wf[nt], xf[mt], acc[mt][nt]);
    }
  }
#pragma unroll
  for (int mt = 0; mt < 2; ++mt) {
    const int row = row0 + 16 * mt + m;
    float v[8];
#pragma unroll
    for (int nt = 0; nt < 2; ++nt) {
      float x0 = bflo(xr[mt][nt][0]), x1 = bfhi(xr[mt][nt][0]), x2 = bflo(xr[mt][nt][1]), x3 = bfhi(xr[mt][nt][1]);
      v[nt * 4 + 0] = bfr(x0 + bfr(acc[mt][nt][0]));
      v[nt * 4 + 1] = bfr(x1 + bfr(acc[mt][nt][1]));
      v[nt * 4 + 2] = bfr(x2 + bfr(acc[mt][nt][2]));
      v[nt * 4 + 3] = bfr(x3 + bfr(acc[mt][nt][3]));
      u32x2 o = {pack2(v[nt * 4], v[nt * 4 + 1]), pack2(v[nt * 4 + 2], v[nt * 4 + 3])};
      *(u32x2*)(xo + (size_t)row * 512 + col0 + 32 * wave + 16 * nt + 4 * g) = o;
    }
    stat_emit(stats, row, cb * 4 + wave, v, g);
  }
}

__global__ __launch_bounds__(256, 1) void k3_w1(const u16* __restrict__ xp, const u16* __restrict__ w1,
                                                const float* __restrict__ cs, const float* __restrict__ stats,
                                                u16* __restrict__ hpad) {
  __shared__ __attribute__((aligned(16))) char lds[8 * 16384];
  const int tid = threadIdx.x, lane = tid & 63, wave = __builtin_amdgcn_readfirstlane(tid >> 6);
  const int xcd = blockIdx.x & 7, idx = blockIdx.x >> 3;
  const int rb = xcd * 4 + (idx & 3), cb = idx >> 2;
  const int wm = wave >> 1, wn = wave & 1, g = lane >> 4, m = lane & 15;
  const int row0 = rb * 64, col0 = cb * 64;
  StatRaw sr[2];
  f32x4 csr[2];
#pragma unroll
  for (int mt = 0; mt < 2; ++mt) sr[mt] = stat_load(stats, row0 + 32 * wm + 16 * mt + m, g);
#pragma unroll
  for (int nt = 0; nt < 2; ++nt) csr[nt] = *(const f32x4*)(cs + col0 + 32 * wn + 16 * nt + 4 * g);
  const u16* ap = xp + (size_t)(row0 + 16 * wave + (lane >> 3)) * 512 + (((lane & 7) ^ (lane >> 3)) * 8);
  const int kr0 = 16 * wave + (lane >> 3);
  auto af = [&](int c) { return ap + c * 64; };
  auto bf = [&](int c, int i) {
    int kr = kr0 + 8 * i;
    int ch = (lane & 7) ^ ((((kr >> 1) & 1) | (((kr >> 3) & 1) << 1)) << 1);
    return w1 + (size_t)(c * 64 + kr) * 512 + col0 + ch * 8;
  };
  f32x4 acc[2][2];
#pragma unroll
  for (int i = 0; i < 2; ++i)
#pragma unroll
    for (int j = 0; j < 2; ++j) acc[i][j] = f32x4{0, 0, 0, 0};
  core_small<8, 8>(lds, af, 512, bf, acc, wave, lane);
#pragma unroll
  for (int mt = 0; mt < 2; ++mt) {
    float mean, var;
    stat_fin(sr[mt], mean, var);
    float r1 = __builtin_amdgcn_rsqf(var + 1e-5f);
    float r2 = __builtin_amdgcn_rsqf(var * r1 * r1 + 1e-5f);
    float sc = r1 * r2;
    const int row = row0 + 32 * wm + 16 * mt + m;
#pragma unroll
    for (int nt = 0; nt < 2; ++nt) {
      float y[4];
#pragma unroll
      for (int r = 0; r < 4; ++r) y[r] = gelu_t(bfr(sc * (acc[mt][nt][r] - mean * csr[nt][r])));
      u32x2 o = {pack2(y[0], y[1]), pack2(y[2], y[3])};
      *(u32x2*)(hpad + (size_t)(row + 2) * 512 + col0 + 32 * wn + 16 * nt + 4 * g) = o;
    }
  }
}

__global__ __launch_bounds__(256, 1) void k4_w2(const u16* __restrict__ hpad, const u16* __restrict__ w2,
                                                const u16* __restrict__ xp, const float* __restrict__ stats_in,
                                                u16* __restrict__ x1, float* __restrict__ stats_out) {
  __shared__ __attribute__((aligned(16))) char lds[8 * 16384];
  const int tid = threadIdx.x, lane = tid & 63, wave = __builtin_amdgcn_readfirstlane(tid >> 6);
  const int xcd = blockIdx.x & 7, idx = blockIdx.x >> 3;
  const int rb = xcd * 4 + (idx & 3), cb = idx >> 2;
  const int wm = wave >> 1, wn = wave & 1, g = lane >> 4, m = lane & 15;
  const int row0 = rb * 64, col0 = cb * 64;
  StatRaw sr[2];
  u32x2 xr[2][2];
#pragma unroll
  for (int mt = 0; mt < 2; ++mt) {
    sr[mt] = stat_load(stats_in, row0 + 32 * wm + 16 * mt + m, g);
#pragma unroll
    for (int nt = 0; nt < 2; ++nt)
      xr[mt][nt] = *(const u32x2*)(xp + (size_t)(row0 + 32 * wm + 16 * mt + m) * 512 + col0 + 32 * wn + 16 * nt + 4 * g);
  }
  const u16* ap = hpad + (size_t)(row0 + 2 + 16 * wave + (lane >> 3)) * 512 + (((lane & 7) ^ (lane >> 3)) * 8);
  const int kr0 = 16 * wave + (lane >> 3);
  auto af = [&](int c) {
    int grp = c >> 1;
    int sh = (grp == 0) ? -1 : (grp == 1) ? 1 : (grp == 2) ? -2 : 2;
    return ap + sh * 512 + c * 64;
  };
  auto bf = [&](int c, int i) {
    int kr = kr0 + 8 * i;
    int ch = (lane & 7) ^ ((((kr >> 1) & 1) | (((kr >> 3) & 1) << 1)) << 1);
    return w2 + (size_t)(c * 64 + kr) * 512 + col0 + ch * 8;
  };
  f32x4 acc[2][2];
#pragma unroll
  for (int i = 0; i < 2; ++i)
#pragma unroll
    for (int j = 0; j < 2; ++j) acc[i][j] = f32x4{0, 0, 0, 0};
  core_small<8, 8>(lds, af, 512, bf, acc, wave, lane);
#pragma unroll
  for (int mt = 0; mt < 2; ++mt) {
    float mean, var;
    stat_fin(sr[mt], mean, var);
    float r1 = __builtin_amdgcn_rsqf(var + 1e-5f);
    const int row = row0 + 32 * wm + 16 * mt + m;
    float v[8];
#pragma unroll
    for (int nt = 0; nt < 2; ++nt) {
      float xx[4] = {bflo(xr[mt][nt][0]), bfhi(xr[mt][nt][0]), bflo(xr[mt][nt][1]), bfhi(xr[mt][nt][1])};
#pragma unroll
      for (int r = 0; r < 4; ++r) v[nt * 4 + r] = bfr(bfr((xx[r] - mean) * r1) + bfr(acc[mt][nt][r]));
      u32x2 o = {pack2(v[nt * 4], v[nt * 4 + 1]), pack2(v[nt * 4 + 2], v[nt * 4 + 3])};
      *(u32x2*)(x1 + (size_t)row * 512 + col0 + 32 * wn + 16 * nt + 4 * g) = o;
    }
    stat_emit(stats_out, row, cb * 2 + wn, v, g);
  }
}

__global__ __launch_bounds__(256, 1) void k5_w3(const u16* __restrict__ x1, const u16* __restrict__ w3,
                                                const float* __restrict__ cs, const float* __restrict__ stats,
                                                u16* __restrict__ h2) {
  __shared__ __attribute__((aligned(16))) char lds[5 * BIG_SLOT];
  const int tid = threadIdx.x, lane = tid & 63, wave = __builtin_amdgcn_readfirstlane(tid >> 6);
  const int xcd = blockIdx.x & 7, idx = blockIdx.x >> 3;
  const int rb = xcd * 2 + (idx & 1), cb = idx >> 1;
  const int wm = wave >> 1, wn = wave & 1, g = lane >> 4, m = lane & 15;
  const int row0 = rb * 128, col0 = cb * 96;
  StatRaw sr[4];
  f32x4 csr[3];
#pragma unroll
  for (int mt = 0; mt < 4; ++mt) sr[mt] = stat_load(stats, row0 + 64 * wm + 16 * mt + m, g);
#pragma unroll
  for (int j = 0; j < 3; ++j) {
    int c = col0 + 32 * j + 16 * wn + 4 * g;
    csr[j] = *(const f32x4*)(cs + (c < F_ ? c : 0));
  }
  const u16* aptr = x1 + (size_t)(row0 + 32 * wave + (lane >> 3)) * 512 + (((lane & 7) ^ (lane >> 3)) * 8);
  const int bch = ((lane & 3) ^ (((lane >> 5) & 1) << 1)) * 8;
  const u16* bpb = w3 + (size_t)(16 * wave + (lane >> 2)) * F_ + bch;
  const int c1 = (col0 + 32 < F_) ? col0 + 32 : col0, c2 = (col0 + 64 < F_) ? col0 + 64 : col0;
  f32x4 acc[4][3];
#pragma unroll
  for (int i = 0; i < 4; ++i)
#pragma unroll
    for (int j = 0; j < 3; ++j) acc[i][j] = f32x4{0, 0, 0, 0};
  core_big<8, 5, false>(lds, aptr, 512, bpb + col0, bpb + c1, bpb + c2, F_, acc, wave, lane);
#pragma unroll
  for (int mt = 0; mt < 4; ++mt) {
    float mean, var;
    stat_fin(sr[mt], mean, var);
    float r1 = __builtin_amdgcn_rsqf(var + 1e-5f);
    const int row = row0 + 64 * wm + 16 * mt + m;
#pragma unroll
    for (int j = 0; j < 3; ++j) {
      const int c = col0 + 32 * j + 16 * wn + 4 * g;
      float y[4];
#pragma unroll
      for (int r = 0; r < 4; ++r) y[r] = gelu_t(bfr(r1 * (acc[mt][j][r] - mean * csr[j][r])));
      u32x2 o = {pack2(y[0], y[1]), pack2(y[2], y[3])};
      if (c >= F_) o = u32x2{0, 0};
      if (c < FP_) *(u32x2*)(h2 + (size_t)row * FP_ + c) = o;
    }
  }
}

__global__ __launch_bounds__(256, 1) void k6_w4(const u16* __restrict__ h2, const u16* __restrict__ w4,
                                                const u16* __restrict__ xp, const u16* __restrict__ x1,
                                                u16* __restrict__ xo, float* __restrict__ stats_out) {
  __shared__ __attribute__((aligned(16))) char lds[9 * 16384];
  const int tid = threadIdx.x, lane = tid & 63, wave = __builtin_amdgcn_readfirstlane(tid >> 6);
  const int xcd = blockIdx.x & 7, idx = blockIdx.x >> 3;
  const int rb = xcd * 4 + (idx & 3), cb = idx >> 2;
  const int wm = wave >> 1, wn = wave & 1, g = lane >> 4, m = lane & 15;
  const int row0 = rb * 64, col0 = cb * 64;
  u32x2 xr[2][2], yr[2][2];
#pragma unroll
  for (int mt = 0; mt < 2; ++mt)
#pragma unroll
    for (int nt = 0; nt < 2; ++nt) {
      size_t o = (size_t)(row0 + 32 * wm + 16 * mt + m) * 512 + col0 + 32 * wn + 16 * nt + 4 * g;
      xr[mt][nt] = *(const u32x2*)(xp + o);
      yr[mt][nt] = *(const u32x2*)(x1 + o);
    }
  const u16* ap = h2 + (size_t)(row0 + 16 * wave + (lane >> 3)) * FP_ + (((lane & 7) ^ (lane >> 3)) * 8);
  const int kr0 = 16 * wave + (lane >> 3);
  auto af = [&](int c) { return ap + c * 64; };
  auto bf = [&](int c, int i) {
    int kr = kr0 + 8 * i;
    int ch = (lane & 7) ^ ((((kr >> 1) & 1) | (((kr >> 3) & 1) << 1)) << 1);
    int k = c * 64 + kr;
    if (k > F_ - 1) k = F_ - 1;
    return w4 + (size_t)k * 512 + col0 + ch * 8;
  };
  f32x4 acc[2][2];
#pragma unroll
  for (int i = 0; i < 2; ++i)
#pragma unroll
    for (int j = 0; j < 2; ++j) acc[i][j] = f32x4{0, 0, 0, 0};
  core_small<22, 9>(lds, af, FP_, bf, acc, wave, lane);
#pragma unroll
  for (int mt = 0; mt < 2; ++mt) {
    const int row = row0 + 32 * wm + 16 * mt + m;
    float v[8];
#pragma unroll
    for (int nt = 0; nt < 2; ++nt) {
      float xx[4] = {bflo(xr[mt][nt][0]), bfhi(xr[mt][nt][0]), bflo(xr[mt][nt][1]), bfhi(xr[mt][nt][1])};
      float yy[4] = {bflo(yr[mt][nt][0]), bfhi(yr[mt][nt][0]), bflo(yr[mt][nt][1]), bfhi(yr[mt][nt][1])};
#pragma unroll
      for (int r = 0; r < 4; ++r) v[nt * 4 + r] = bfr(xx[r] + bfr(yy[r] + bfr(acc[mt][nt][r])));
      u32x2 o = {pack2(v[nt * 4], v[nt * 4 + 1]), pack2(v[nt * 4 + 2], v[nt * 4 + 3])};
      *(u32x2*)(xo + (size_t)row * 512 + col0 + 32 * wn + 16 * nt + 4 * g) = o;
    }
    stat_emit(stats_out, row, cb * 2 + wn, v, g);
  }
}

extern "C" void kernel_launch(void* const* d_in, const int* in_sizes, int n_in, void* d_out, int out_size,
                              void* d_ws, size_t ws_size, hipStream_t stream) {
  const u16* A_x = (const u16*)d_in[0];
  const u16* wqkv = (const u16*)d_in[1];
  const u16* wphi = (const u16*)d_in[2];
  const u16* wo = (const u16*)d_in[3];
  const u16* w1 = (const u16*)d_in[5];
  const u16* w2 = (const u16*)d_in[6];
  const u16* w3 = (const u16*)d_in[7];
  const u16* w4 = (const u16*)d_in[8];
  char* ws = (char*)d_ws;
  size_t off = 0;
  auto take = [&](size_t bytes) { char* p = ws + off; off += (bytes + 255) & ~(size_t)255; return p; };
  float* kvacc = (float*)take(2 * 32 * 576 * 4);
  float* cs_qkv = (float*)take(1536 * 4);
  float* cs_w1 = (float*)take(512 * 4);
  float* cs_w3 = (float*)take(1440 * 4);
  float* st_x = (float*)take(2048 * 32 * 4);
  float* st_xp = (float*)take(2048 * 32 * 4);
  float* st_x1 = (float*)take(2048 * 32 * 4);
  u16* phiq = (u16*)take((size_t)2048 * 1024 * 2);
  u16* xp = (u16*)take((size_t)2048 * 512 * 2);
  u16* hpad = (u16*)take((size_t)2052 * 512 * 2);
  u16* x1 = (u16*)take((size_t)2048 * 512 * 2);
  u16* h2 = (u16*)take((size_t)2048 * FP_ * 2);
  u16* x2 = (u16*)take((size_t)2048 * 512 * 2);
  if (off > ws_size) return;

  k0_prep<<<256, 256, 0, stream>>>(A_x, wqkv, w1, w3, kvacc, hpad, cs_qkv, cs_w1, cs_w3, st_x);
  for (int it = 0; it < 2; ++it) {
    const u16* xin = it == 0 ? A_x : x2;
    u16* xout = it == 0 ? x2 : (u16*)d_out;
    float* kva = kvacc + it * 32 * 576;
    k1_qkv<<<256, 256, 0, stream>>>(xin, wqkv, wphi, cs_qkv, st_x, phiq, kva);
    k2_wo<<<256, 256, 0, stream>>>(xin, phiq, kva, wo, xp, st_xp);
    k3_w1<<<256, 256, 0, stream>>>(xp, w1, cs_w1, st_xp, hpad);
    k4_w2<<<256, 256, 0, stream>>>(hpad, w2, xp, st_xp, x1, st_x1);
    k5_w3<<<240, 256, 0, stream>>>(x1, w3, cs_w3, st_x1, h2);
    k6_w4<<<256, 256, 0, stream>>>(h2, w4, xp, x1, xout, st_x);
  }
}
```

```cpp
#include <hip/hip_runtime.h>
#include <hip/hip_bf16.h>
#include <stdint.h>

typedef unsigned short u16;
typedef short s16x4 __attribute__((ext_vector_type(4)));
typedef short s16x8 __attribute__((ext_vector_type(8)));
typedef __bf16 bf16x8 __attribute__((ext_vector_type(8)));
typedef __bf16 bf16x2 __attribute__((ext_vector_type(2)));
typedef float f32x4 __attribute__((ext_vector_type(4)));
typedef float f32x2 __attribute__((ext_vector_type(2)));
typedef unsigned u32x4 __attribute__((ext_vector_type(4)));
typedef unsigned u32x2 __attribute__((ext_vector_type(2)));
typedef __attribute__((address_space(3))) s16x4* lds_s16x4_p;
typedef __amdgpu_buffer_rsrc_t rsrc_t;

constexpr int F_ = 1376;
constexpr int CSN = 3456;

#define DEVI __device__ __forceinline__

DEVI unsigned pack2(float a, float b) {
  bf16x2 v = {(__bf16)a, (__bf16)b};
  return __builtin_bit_cast(unsigned, v);
}
DEVI float bfr(float a) { return (float)(__bf16)a; }
DEVI float bflo(unsigned u) { return __uint_as_float(u << 16); }
DEVI float bfhi(unsigned u) { return __uint_as_float(u & 0xffff0000u); }

template <int N> DEVI void wait_vm() { asm volatile("s_waitcnt vmcnt(%0)" ::"n"(N) : "memory"); }
DEVI void wait_lgkm0() { asm volatile("s_waitcnt lgkmcnt(0)" ::: "memory"); }
DEVI void bar() { __builtin_amdgcn_s_barrier(); }
template <class T> DEVI void pin(T& v) { asm volatile("" : "+v"(v)); }

DEVI rsrc_t mk(const void* p, unsigned n) { return __builtin_amdgcn_make_buffer_rsrc((void*)p, 0, n, 0x00020000); }
DEVI void dma(rsrc_t r, unsigned voff, unsigned soff, unsigned lb) {
  asm volatile("s_mov_b32 m0, %0\n\ts_nop 0\n\tbuffer_load_dwordx4 %1, %2, %3 offen lds" ::"s"(lb), "v"(voff), "s"(r),
               "s"(soff)
               : "memory");
}
DEVI u32x4 aload16(rsrc_t r, unsigned voff, unsigned soff) {
  u32x4 v;
  asm volatile("buffer_load_dwordx4 %0, %1, %2, %3 offen" : "=v"(v) : "v"(voff), "s"(r), "s"(soff) : "memory");
  return v;
}
DEVI u32x2 aload8(rsrc_t r, unsigned voff, unsigned soff) {
  u32x2 v;
  asm volatile("buffer_load_dwordx2 %0, %1, %2, %3 offen" : "=v"(v) : "v"(voff), "s"(r), "s"(soff) : "memory");
  return v;
}
DEVI f32x4 asf(u32x4 v) { return __builtin_bit_cast(f32x4, v); }

DEVI s16x4 trread(const char* l) { return __builtin_amdgcn_ds_read_tr16_b64_v4i16((lds_s16x4_p)l); }
DEVI f32x4 mfma32(s16x8 a, s16x8 b, f32x4 c) {
  return __builtin_amdgcn_mfma_f32_16x16x32_bf16(__builtin_bit_cast(bf16x8, a), __builtin_bit_cast(bf16x8, b), c, 0, 0, 0);
}
DEVI f32x4 mfma16(s16x4 a, s16x4 b, f32x4 c) { return __builtin_amdgcn_mfma_f32_16x16x16bf16_1k(a, b, c, 0, 0, 0); }
DEVI s16x8 cat(s16x4 a, s16x4 b) { return __builtin_shufflevector(a, b, 0, 1, 2, 3, 4, 5, 6, 7); }
DEVI s16x4 pack4(float a, float b, float c, float d) {
  u32x2 v = {pack2(a, b), pack2(c, d)};
  return __builtin_bit_cast(s16x4, v);
}
DEVI float gelu_t(float x) {
  float t = __builtin_fmaf(x * x, -0.10294324f, -2.30220819f);
  float e = __builtin_amdgcn_exp2f(x * t);
  return x * __builtin_amdgcn_rcpf(1.0f + e);
}
DEVI float rsum_g(float v) {
  unsigned u = __float_as_uint(v);
  auto a = __builtin_amdgcn_permlane16_swap(u, u, false, false);
  v = __uint_as_float(a[0]) + __uint_as_float(a[1]);
  u = __float_as_uint(v);
  auto b = __builtin_amdgcn_permlane32_swap(u, u, false, false);
  return __uint_as_float(b[0]) + __uint_as_float(b[1]);
}
DEVI float rmax_g(float v) {
  unsigned u = __float_as_uint(v);
  auto a = __builtin_amdgcn_permlane16_swap(u, u, false, false);
  v = fmaxf(__uint_as_float(a[0]), __uint_as_float(a[1]));
  u = __float_as_uint(v);
  auto b = __builtin_amdgcn_permlane32_swap(u, u, false, false);
  return fmaxf(__uint_as_float(b[0]), __uint_as_float(b[1]));
}
DEVI float rmin_g(float v) {
  unsigned u = __float_as_uint(v);
  auto a = __builtin_amdgcn_permlane16_swap(u, u, false, false);
  v = fminf(__uint_as_float(a[0]), __uint_as_float(a[1]));
  u = __float_as_uint(v);
  auto b = __builtin_amdgcn_permlane32_swap(u, u, false, false);
  return fminf(__uint_as_float(b[0]), __uint_as_float(b[1]));
}
DEVI u32x4 swz8(u32x2 p0, u32x2 p1) {
  auto a = __builtin_amdgcn_permlane16_swap(p0[0], p1[0], false, false);
  auto b = __builtin_amdgcn_permlane16_swap(p0[1], p1[1], false, false);
  return u32x4{a[0], b[0], a[1], b[1]};
}
DEVI void unswz8(u32x4 v, u32x2& p0, u32x2& p1) {
  auto a = __builtin_amdgcn_permlane16_swap(v[0], v[2], false, false);
  auto b = __builtin_amdgcn_permlane16_swap(v[1], v[3], false, false);
  p0 = u32x2{a[0], b[0]};
  p1 = u32x2{a[1], b[1]};
}
DEVI int swz_col(int g) { return (g & 1) * 16 + (g >> 1) * 8; }

DEVI void stat_fin(f32x4 a, f32x4 b, float& mean, float& var) {
  float s = rsum_g((a[0] + a[2]) + (b[0] + b[2]));
  mean = s * (1.0f / 16.0f);
  float d0 = a[0] - mean, d1 = a[2] - mean, d2 = b[0] - mean, d3 = b[2] - mean;
  float m2 = ((a[1] + a[3]) + (b[1] + b[3])) + 32.0f * ((d0 * d0 + d1 * d1) + (d2 * d2 + d3 * d3));
  m2 = rsum_g(m2);
  var = m2 * (1.0f / 512.0f);
}
DEVI void stat_emit(float* stats, int row, int cg, const float (&v)[8], int g) {
  float s = ((v[0] + v[1]) + (v[2] + v[3])) + ((v[4] + v[5]) + (v[6] + v[7]));
  s = rsum_g(s);
  float mean = s * (1.0f / 32.0f);
  float q = 0.f;
#pragma unroll
  for (int i = 0; i < 8; ++i) { float d = v[i] - mean; q = __builtin_fmaf(d, d, q); }
  q = rsum_g(q);
  if (g == 0) { f32x2 o = {mean, q}; *(f32x2*)(stats + (size_t)row * 32 + cg * 2) = o; }
}

template <int N> DEVI void wait_vm_n(int n) {
  if constexpr (N > 0) {
    if (n >= N) wait_vm<N>(); else wait_vm_n<N - 1>(n);
  } else {
    wait_vm<0>();
  }
}

__global__ __launch_bounds__(256) void k0_prep(const u16* __restrict__ x, const u16* __restrict__ wqkv,
                                               const u16* __restrict__ w1, const u16* __restrict__ w3,
                                               float* __restrict__ kvacc, u16* __restrict__ hpad,
                                               float* __restrict__ cs, float* __restrict__ stats) {
  __shared__ float red[4][64];
  const int b = blockIdx.x, tid = threadIdx.x, lane = tid & 63, wave = tid >> 6;
  const int cbk = b >> 2, rq = b & 3;
  const u16* W; int ld, c0, oc;
  if (cbk < 24) { W = wqkv; ld = 1536; c0 = cbk * 64; oc = c0; }
  else if (cbk < 32) { W = w1; ld = 512; c0 = (cbk - 24) * 64; oc = 1536 + c0; }
  else { W = w3; ld = F_; c0 = (cbk - 32) * 64; oc = 2048 + c0; }
  const int r = tid >> 3, c8 = tid & 7;
  const bool wact = (cbk < 54) && (c0 + c8 * 8 < ld);
  u32x4 wv[4];
#pragma unroll
  for (int i = 0; i < 4; ++i) {
    wv[i] = u32x4{0, 0, 0, 0};
    if (wact) wv[i] = *(const u32x4*)(W + (size_t)(rq * 128 + r + 32 * i) * ld + c0 + c8 * 8);
  }
  u32x4 xv[2];
#pragma unroll
  for (int i = 0; i < 2; ++i) xv[i] = *(const u32x4*)(x + (size_t)(b * 8 + wave * 2 + i) * 512 + lane * 8);
  if (tid < 34) { f32x4 z = {0, 0, 0, 0}; ((f32x4*)kvacc)[b * 34 + tid] = z; }
  if (b == 255) {
    u32x4 z = {0, 0, 0, 0};
    int rr = tid >> 6, c = tid & 63;
    int row = (rr < 2) ? rr : (2048 + rr);
    *(u32x4*)(hpad + (size_t)row * 512 + c * 8) = z;
  }
#pragma unroll
  for (int i = 0; i < 2; ++i) {
    int row = b * 8 + wave * 2 + i;
    float f[8];
#pragma unroll
    for (int j = 0; j < 4; ++j) { f[2 * j] = bflo(xv[i][j]); f[2 * j + 1] = bfhi(xv[i][j]); }
    float s = ((f[0] + f[1]) + (f[2] + f[3])) + ((f[4] + f[5]) + (f[6] + f[7]));
    s += __shfl_xor(s, 1); s += __shfl_xor(s, 2);
    float mean = s * (1.0f / 32.0f);
    float q = 0.f;
#pragma unroll
    for (int j = 0; j < 8; ++j) { float d = f[j] - mean; q += d * d; }
    q += __shfl_xor(q, 1); q += __shfl_xor(q, 2);
    if ((lane & 3) == 0) { f32x2 o = {mean, q}; *(f32x2*)(stats + (size_t)row * 32 + (lane >> 2) * 2) = o; }
  }
  if (cbk < 54) {
    float a[8];
#pragma unroll
    for (int j = 0; j < 4; ++j) {
      a[2 * j] = (bflo(wv[0][j]) + bflo(wv[1][j])) + (bflo(wv[2][j]) + bflo(wv[3][j]));
      a[2 * j + 1] = (bfhi(wv[0][j]) + bfhi(wv[1][j])) + (bfhi(wv[2][j]) + bfhi(wv[3][j]));
    }
#pragma unroll
    for (int j = 0; j < 8; ++j) { a[j] += __shfl_xor(a[j], 8); a[j] = rsum_g(a[j]); }
    if (lane < 8) {
#pragma unroll
      for (int j = 0; j < 8; ++j) red[wave][lane * 8 + j] = a[j];
    }
    __syncthreads();
    if (tid < 64) cs[rq * CSN + oc + tid] = (red[0][tid] + red[1][tid]) + (red[2][tid] + red[3][tid]);
  }
}

template <int NCH, int RING, int P, bool HALF_LAST, class ISS, class HOOK>
DEVI void core64(const char* lds, ISS issue, HOOK hook, f32x4 (&acc)[2], int wave, int lane) {
  const int wm = wave >> 1, wn = wave & 1, g = lane >> 4, q = (lane & 15) >> 2, p = lane & 3;
  const int xoff = (16 * wm + (lane & 15)) * 128;
  const int fsw = (q >> 1) | ((g & 1) << 1);
  const int woff = 8192 + (8 * g + q) * 128 + p * 8;
#pragma unroll
  for (int c = 0; c < P; ++c) issue(c);
#pragma unroll
  for (int c = 0; c < NCH; ++c) {
    int last = c + P - 1;
    if (last > NCH - 1) last = NCH - 1;
    wait_vm_n<2 * (P - 1)>((last - c) * 2);
    wait_lgkm0();
    bar();
    if (c + P < NCH) issue(c + P);
    if (c == 0) hook();
    const char* s = lds + (c % RING) * 16384;
#pragma unroll
    for (int ks = 0; ks < 2; ++ks) {
      if (HALF_LAST && c == NCH - 1 && ks == 1) break;
      s16x8 xf = *(const s16x8*)(s + xoff + (((4 * ks + g) ^ (lane & 7)) * 16));
#pragma unroll
      for (int nt = 0; nt < 2; ++nt) {
        const char* w = s + woff + ks * 4096 + (((2 * wn + nt) ^ fsw) * 32);
        s16x8 wf = cat(trread(w), trread(w + 512));
        acc[nt] = mfma32(wf, xf, acc[nt]);
      }
    }
  }
}

constexpr int BIG_SLOT = 28672;
template <int NCH, int RING, int P, class ISS, class HOOK>
DEVI void core_big(const char* lds, ISS issue, HOOK hook, f32x4 (&acc)[2][3], int wave, int lane) {
  const int wm = wave >> 1, wn = wave & 1, g = lane >> 4, q = (lane & 15) >> 2, p = lane & 3;
  const int xoff = (32 * wm + (lane & 15)) * 128;
  const int woff = 16384 + (8 * g + q) * 64 + ((wn ^ (g & 1)) * 32) + p * 8;
#pragma unroll
  for (int c = 0; c < P; ++c) issue(c);
#pragma unroll
  for (int c = 0; c < NCH; ++c) {
    int last = c + P - 1;
    if (last > NCH - 1) last = NCH - 1;
    if (wave < 4) wait_vm_n<4 * (P - 1)>((last - c) * 4);
    else wait_vm_n<3 * (P - 1)>((last - c) * 3);
    wait_lgkm0();
    bar();
    if (c + P < NCH) issue(c + P);
    if (c == 0) hook();
    const char* s = lds + (c % RING) * BIG_SLOT;
#pragma unroll
    for (int ks = 0; ks < 2; ++ks) {
      s16x8 xf[2], wf[3];
#pragma unroll
      for (int mt = 0; mt < 2; ++mt)
        xf[mt] = *(const s16x8*)(s + xoff + mt * 2048 + (((4 * ks + g) ^ (lane & 7)) * 16));
#pragma unroll
      for (int j = 0; j < 3; ++j)
        wf[j] = cat(trread(s + woff + j * 4096 + ks * 2048), trread(s + woff + j * 4096 + ks * 2048 + 256));
#pragma unroll
      for (int mt = 0; mt < 2; ++mt)
#pragma unroll
        for (int j = 0; j < 3; ++j) acc[mt][j] = mfma32(wf[j], xf[mt], acc[mt][j]);
    }
  }
}

DEVI unsigned lds_base(const char* p) {
  return (unsigned)(uintptr_t)(__attribute__((address_space(3))) const char*)p;
}
DEVI f32x4 sum4(u32x4 a, u32x4 b, u32x4 c, u32x4 d) { return (asf(a) + asf(b)) + (asf(c) + asf(d)); }

DEVI u32x4 dual_softmax(f32x4 p) {
  float a[4];
#pragma unroll
  for (int r = 0; r < 4; ++r) a[r] = bfr(p[r]) * 1.44269504f;
  float mx = rmax_g(fmaxf(fmaxf(a[0], a[1]), fmaxf(a[2], a[3])));
  float mn = rmin_g(fminf(fminf(a[0], a[1]), fminf(a[2], a[3])));
  float ep[4], em[4];
#pragma unroll
  for (int r = 0; r < 4; ++r) { ep[r] = __builtin_amdgcn_exp2f(a[r] - mx); em[r] = __builtin_amdgcn_exp2f(mn - a[r]); }
  float sp = rsum_g((ep[0] + ep[1]) + (ep[2] + ep[3]));
  float sm = rsum_g((em[0] + em[1]) + (em[2] + em[3]));
  float ip = __builtin_amdgcn_rcpf(sp), im = __builtin_amdgcn_rcpf(sm);
  return u32x4{pack2(ep[0] * ip, ep[1] * ip), pack2(ep[2] * ip, ep[3] * ip), pack2(em[0] * im, em[1] * im),
               pack2(em[2] * im, em[3] * im)};
}

__global__ __launch_bounds__(512, 1) void k1_qkv(const u16* __restrict__ x, const u16* __restrict__ wqkv,
                                                 const u16* __restrict__ wphi, const float* __restrict__ cs,
                                                 const float* __restrict__ stats, u16* __restrict__ phiq,
                                                 float* __restrict__ kvacc) {
  __shared__ __attribute__((aligned(16))) char lds[5 * BIG_SLOT];
  const int tid = threadIdx.x, lane = tid & 63, wave = __builtin_amdgcn_readfirstlane(tid >> 6);
  const int xcd = blockIdx.x & 7, idx = blockIdx.x >> 3;
  const int rb = xcd * 2 + (idx & 1), hp = idx >> 1;
  const int wm = wave >> 1, wn = wave & 1, g = lane >> 4, m = lane & 15, q = m >> 2, p = lane & 3;
  const int row0 = rb * 128, head = hp * 2 + wn;
  const unsigned lb = lds_base(lds);
  const rsrc_t rx = mk(x, 2048 * 1024), rw = mk(wqkv, 512 * 1536 * 2), rs = mk(stats, 2048 * 128),
               rc = mk(cs, 4 * CSN * 4);
  u32x4 sa[2][2], cq[3][4];
#pragma unroll
  for (int mt = 0; mt < 2; ++mt) {
    unsigned vo = (row0 + 32 * wm + 16 * mt + m) * 128 + g * 32;
    sa[mt][0] = aload16(rs, vo, 0);
    sa[mt][1] = aload16(rs, vo + 16, 0);
  }
#pragma unroll
  for (int j = 0; j < 3; ++j)
#pragma unroll
    for (int i = 0; i < 4; ++i) cq[j][i] = aload16(rc, (head * 16 + 4 * g) * 4, (i * CSN + j * 512) * 4);
  s16x4 wp;
#pragma unroll
  for (int j = 0; j < 4; ++j) wp[j] = (short)wphi[head * 256 + (4 * g + j) * 16 + m];

  const unsigned va = ((row0 + 16 * wave + (lane >> 3)) * 512 + (((lane & 7) ^ (lane >> 3)) * 8)) * 2;
  const int bch = ((lane & 3) ^ (((lane >> 5) & 1) << 1)) * 8;
  const unsigned vb = ((16 * (wave & 3) + (lane >> 2)) * 1536 + hp * 32 + bch) * 2;
  auto issue = [&](int c) {
    unsigned s = lb + (c % 5) * BIG_SLOT;
    dma(rx, va, c * 128, s + wave * 2048);
    dma(rx, va, c * 128 + 8192, s + wave * 2048 + 1024);
    if (wave < 4) {
      dma(rw, vb, c * 64 * 3072, s + 16384 + wave * 1024);
      dma(rw, vb, c * 64 * 3072 + 2048, s + 16384 + 8192 + wave * 1024);
    } else {
      dma(rw, vb, c * 64 * 3072 + 1024, s + 16384 + 4096 + (wave - 4) * 1024);
    }
  };
  float mean[2], rstd[2];
  f32x4 csv[3];
  auto hook = [&]() {
#pragma unroll
    for (int mt = 0; mt < 2; ++mt) {
      pin(sa[mt][0]); pin(sa[mt][1]);
      float var;
      stat_fin(asf(sa[mt][0]), asf(sa[mt][1]), mean[mt], var);
      rstd[mt] = __builtin_amdgcn_rsqf(var + 1e-5f);
    }
#pragma unroll
    for (int j = 0; j < 3; ++j) {
#pragma unroll
      for (int i = 0; i < 4; ++i) pin(cq[j][i]);
      csv[j] = sum4(cq[j][0], cq[j][1], cq[j][2], cq[j][3]);
    }
  };
  f32x4 acc[2][3];
#pragma unroll
  for (int i = 0; i < 2; ++i)
#pragma unroll
    for (int j = 0; j < 3; ++j) acc[i][j] = f32x4{0, 0, 0, 0};
  core_big<8, 5, 4>(lds, issue, hook, acc, wave, lane);

  char* sk = lds + wave * 3072;
  char* sv = sk + 2048;
  const f32x4 zero = {0, 0, 0, 0};
  s16x4 qf[2];
#pragma unroll
  for (int mt = 0; mt < 2; ++mt) {
    float y[3][4];
#pragma unroll
    for (int j = 0; j < 3; ++j)
#pragma unroll
      for (int r = 0; r < 4; ++r) y[j][r] = rstd[mt] * __builtin_fmaf(-mean[mt], csv[j][r], acc[mt][j][r]);
    qf[mt] = pack4(y[0][0], y[0][1], y[0][2], y[0][3]);
    s16x4 kf = pack4(y[1][0], y[1][1], y[1][2], y[1][3]);
    s16x4 vf = pack4(y[2][0], y[2][1], y[2][2], y[2][3]);
    f32x4 pk = mfma16(wp, kf, zero);
    u32x4 fk = dual_softmax(pk);
    *(u32x4*)(sk + (16 * mt + m) * 64 + g * 16) = fk;
    *(s16x4*)(sv + (16 * mt + m) * 32 + g * 8) = vf;
  }
  wait_lgkm0();
  s16x8 vt = cat(trread(sv + (4 * g + q) * 32 + p * 8), trread(sv + (16 + 4 * g + q) * 32 + p * 8));
  const s16x8 ones = {0x3f80, 0x3f80, 0x3f80, 0x3f80, 0x3f80, 0x3f80, 0x3f80, 0x3f80};
  float* red = (float*)(lds + 3 * BIG_SLOT) + wave * 544;
#pragma unroll
  for (int b = 0; b < 2; ++b) {
    s16x8 kt = cat(trread(sk + (4 * g + q) * 64 + b * 32 + p * 8), trread(sk + (16 + 4 * g + q) * 64 + b * 32 + p * 8));
    f32x4 kv = mfma32(kt, vt, zero);
    f32x4 zz = mfma32(kt, ones, zero);
    *(f32x4*)(red + m * 32 + 16 * b + 4 * g) = kv;
    if (m == 0) *(f32x4*)(red + 512 + 16 * b + 4 * g) = zz;
  }
  wait_lgkm0();
  bar();
  {
    const float* r0 = (const float*)(lds + 3 * BIG_SLOT);
#pragma unroll
    for (int i = 0; i < 3; ++i) {
      if (i == 2 && wave != 0) break;
      int hh, e;
      if (i < 2) { int sl = 2 * wave + i; hh = sl >> 3; e = (sl & 7) * 64 + lane; }
      else { hh = lane >> 5; e = 512 + (lane & 31); }
      const float* rr = r0 + hh * 544 + e;
      float v = (rr[0] + rr[2 * 544]) + (rr[4 * 544] + rr[6 * 544]);
      atomicAdd(kvacc + (hp * 2 + hh) * 544 + e, v);
    }
  }
#pragma unroll
  for (int mt = 0; mt < 2; ++mt) {
    f32x4 pq = mfma16(wp, qf[mt], zero);
    u32x4 o = dual_softmax(pq);
    *(u32x4*)(phiq + (size_t)(row0 + 32 * wm + 16 * mt + m) * 1024 + head * 32 + 8 * g) = o;
  }
}

__global__ __launch_bounds__(512, 1) void k2_wo(const u16* __restrict__ x, const u16* __restrict__ phiq,
                                                const float* __restrict__ kvacc, const u16* __restrict__ wo,
                                                u16* __restrict__ xo, float* __restrict__ stats) {
  constexpr int RING = 7, P = 5;
  __shared__ __attribute__((aligned(16))) char lds[RING * 16384 + 32768];
  char* oL = lds + RING * 16384;
  const int tid = threadIdx.x, lane = tid & 63, wave = __builtin_amdgcn_readfirstlane(tid >> 6);
  const int xcd = blockIdx.x & 7, idx = blockIdx.x >> 3;
  const int rb = xcd * 8 + (idx & 7), cb = idx >> 3;
  const int wm = wave >> 2, wn = wave & 3, g = lane >> 4, m = lane & 15, q = m >> 2, p = lane & 3;
  const int row0 = rb * 32, col0 = cb * 128;
  const unsigned lb = lds_base(lds);
  const rsrc_t rx = mk(x, 2048 * 1024), rp = mk(phiq, 2048 * 2048), rk = mk(kvacc, 32 * 544 * 4),
               rw = mk(wo, 512 * 1024);
  u32x4 kv[4][2], zz[4][2], pf[4][2];
#pragma unroll
  for (int hh = 0; hh < 4; ++hh) {
    const int h = wave * 4 + hh;
    kv[hh][0] = aload16(rk, (m * 32 + 8 * g) * 4, h * 2176);
    kv[hh][1] = aload16(rk, (m * 32 + 8 * g) * 4 + 16, h * 2176);
    zz[hh][0] = aload16(rk, (512 + 8 * g) * 4, h * 2176);
    zz[hh][1] = aload16(rk, (512 + 8 * g) * 4 + 16, h * 2176);
#pragma unroll
    for (int mt = 0; mt < 2; ++mt) pf[hh][mt] = aload16(rp, (row0 + 16 * mt + m) * 2048 + 16 * g, h * 64);
  }
  u32x4 xr = aload16(rx, ((row0 + 16 * wm + m) * 512 + col0 + 32 * wn + swz_col(g)) * 2, 0);
  const int kr = 8 * wave + (lane >> 3);
  const unsigned vb = (kr * 512 + col0 + ((lane & 7) ^ (((kr >> 1) & 3) << 1)) * 8) * 2;
  auto issue = [&](int c) {
    unsigned s = lb + (c % RING) * 16384 + wave * 1024;
    dma(rw, vb, c * 65536, s);
    dma(rw, vb, c * 65536 + 128, s + 8192);
  };
#pragma unroll
  for (int c = 0; c < P; ++c) issue(c);
  wait_vm<2 * P>();
  const f32x4 zero = {0, 0, 0, 0};
#pragma unroll
  for (int hp = 0; hp < 2; ++hp) {
    float ov[2][8];
#pragma unroll
    for (int hs = 0; hs < 2; ++hs) {
      const int hh = hp * 2 + hs;
      pin(kv[hh][0]); pin(kv[hh][1]); pin(zz[hh][0]); pin(zz[hh][1]); pin(pf[hh][0]); pin(pf[hh][1]);
      f32x4 k0 = asf(kv[hh][0]), k1 = asf(kv[hh][1]), z0 = asf(zz[hh][0]), z1 = asf(zz[hh][1]);
      u32x4 kq = {pack2(k0[0], k0[1]), pack2(k0[2], k0[3]), pack2(k1[0], k1[1]), pack2(k1[2], k1[3])};
      u32x4 zq = {pack2(z0[0], z0[1]), pack2(z0[2], z0[3]), pack2(z1[0], z1[1]), pack2(z1[2], z1[3])};
      s16x8 kf = __builtin_bit_cast(s16x8, kq), zf = __builtin_bit_cast(s16x8, zq);
#pragma unroll
      for (int mt = 0; mt < 2; ++mt) {
        s16x8 pp = __builtin_bit_cast(s16x8, pf[hh][mt]);
        f32x4 num = mfma32(kf, pp, zero);
        f32x4 den = mfma32(zf, pp, zero);
        float inv = __builtin_amdgcn_rcpf(den[0] + 1e-6f);
#pragma unroll
        for (int r = 0; r < 4; ++r) ov[mt][hs * 4 + r] = num[r] * inv;
      }
    }
#pragma unroll
    for (int mt = 0; mt < 2; ++mt) {
      u32x4 o = {pack2(ov[mt][0], ov[mt][1]), pack2(ov[mt][2], ov[mt][3]), pack2(ov[mt][4], ov[mt][5]),
                 pack2(ov[mt][6], ov[mt][7])};
      *(u32x4*)(oL + ((mt * 16 + wave * 2 + hp) * 64 + lane) * 16) = o;
    }
  }
  f32x4 acc[2] = {zero, zero};
  const int fsw = (q >> 1) | ((g & 1) << 1);
  const int woff = (wn >> 1) * 8192 + (4 * g + q) * 128 + p * 8;
#pragma unroll
  for (int c = 0; c < 8; ++c) {
    int last = c + P - 1;
    if (last > 7) last = 7;
    wait_vm_n<2 * (P - 1)>((last - c) * 2);
    wait_lgkm0();
    bar();
    if (c + P < 8) issue(c + P);
    const char* s = lds + (c % RING) * 16384;
#pragma unroll
    for (int ks = 0; ks < 2; ++ks) {
      s16x8 xf = *(const s16x8*)(oL + ((wm * 16 + c * 2 + ks) * 64 + lane) * 16);
#pragma unroll
      for (int nt = 0; nt < 2; ++nt) {
        const char* w = s + woff + ks * 4096 + (((2 * (wn & 1) + nt) ^ fsw) * 32);
        s16x8 wf = cat(trread(w), trread(w + 2048));
        acc[nt] = mfma32(wf, xf, acc[nt]);
      }
    }
  }
  pin(xr);
  u32x2 xp_[2];
  unswz8(xr, xp_[0], xp_[1]);
  const int row = row0 + 16 * wm + m;
  float v[8];
  u32x2 o[2];
#pragma unroll
  for (int nt = 0; nt < 2; ++nt) {
    float xx[4] = {bflo(xp_[nt][0]), bfhi(xp_[nt][0]), bflo(xp_[nt][1]), bfhi(xp_[nt][1])};
#pragma unroll
    for (int r = 0; r < 4; ++r) v[nt * 4 + r] = bfr(xx[r] + bfr(acc[nt][r]));
    o[nt] = u32x2{pack2(v[nt * 4], v[nt * 4 + 1]), pack2(v[nt * 4 + 2], v[nt * 4 + 3])};
  }
  *(u32x4*)(xo + (size_t)row * 512 + col0 + 32 * wn + swz_col(g)) = swz8(o[0], o[1]);
  stat_emit(stats, row, cb * 4 + wn, v, g);
}

__global__ __launch_bounds__(512, 1) void k3_w1(const u16* __restrict__ xp, const u16* __restrict__ w1,
                                                const float* __restrict__ cs, const float* __restrict__ stats,
                                                u16* __restrict__ hpad) {
  __shared__ __attribute__((aligned(16))) char lds[8 * 16384];
  const int tid = threadIdx.x, lane = tid & 63, wave = __builtin_amdgcn_readfirstlane(tid >> 6);
  const int xcd = blockIdx.x & 7, idx = blockIdx.x >> 3;
  const int rb = xcd * 4 + (idx & 3), cb = idx >> 2;
  const int wm = wave >> 1, wn = wave & 1, g = lane >> 4, m = lane & 15;
  const int row0 = rb * 64, col0 = cb * 64;
  const unsigned lb = lds_base(lds);
  const rsrc_t rx = mk(xp, 2048 * 1024), rw = mk(w1, 512 * 1024), rs = mk(stats, 2048 * 128),
               rc = mk(cs, 4 * CSN * 4);
  const int row = row0 + 16 * wm + m;
  u32x4 sa[2], cq[2][4];
  sa[0] = aload16(rs, row * 128 + g * 32, 0);
  sa[1] = aload16(rs, row * 128 + g * 32 + 16, 0);
#pragma unroll
  for (int nt = 0; nt < 2; ++nt)
#pragma unroll
    for (int i = 0; i < 4; ++i)
      cq[nt][i] = aload16(rc, (col0 + 32 * wn + 16 * nt + 4 * g) * 4, (i * CSN + 1536) * 4);
  const unsigned va = ((row0 + 8 * wave + (lane >> 3)) * 512 + (((lane & 7) ^ (lane >> 3)) * 8)) * 2;
  const unsigned vb =
      ((8 * wave + (lane >> 3)) * 512 + col0 + ((lane & 7) ^ ((((lane >> 4) & 1) | ((wave & 1) << 1)) << 1)) * 8) * 2;
  auto issue = [&](int c) {
    unsigned s = lb + c * 16384 + wave * 1024;
    dma(rx, va, c * 128, s);
    dma(rw, vb, c * 65536, s + 8192);
  };
  float mean, sc;
  f32x4 csv[2];
  auto hook = [&]() {
    pin(sa[0]); pin(sa[1]);
    float var;
    stat_fin(asf(sa[0]), asf(sa[1]), mean, var);
    float r1 = __builtin_amdgcn_rsqf(var + 1e-5f);
    float r2 = __builtin_amdgcn_rsqf(var * r1 * r1 + 1e-5f);
    sc = r1 * r2;
#pragma unroll
    for (int nt = 0; nt < 2; ++nt) {
#pragma unroll
      for (int i = 0; i < 4; ++i) pin(cq[nt][i]);
      csv[nt] = sum4(cq[nt][0], cq[nt][1], cq[nt][2], cq[nt][3]);
    }
  };
  f32x4 acc[2] = {f32x4{0, 0, 0, 0}, f32x4{0, 0, 0, 0}};
  core64<8, 8, 4, false>(lds, issue, hook, acc, wave, lane);
  u32x2 o[2];
#pragma unroll
  for (int nt = 0; nt < 2; ++nt) {
    float y[4];
#pragma unroll
    for (int r = 0; r < 4; ++r) y[r] = gelu_t(bfr(sc * __builtin_fmaf(-mean, csv[nt][r], acc[nt][r])));
    o[nt] = u32x2{pack2(y[0], y[1]), pack2(y[2], y[3])};
  }
  *(u32x4*)(hpad + (size_t)(row + 2) * 512 + col0 + 32 * wn + swz_col(g)) = swz8(o[0], o[1]);
}

__global__ __launch_bounds__(512, 1) void k4_w2(const u16* __restrict__ hpad, const u16* __restrict__ w2,
                                                const u16* __restrict__ xp, const float* __restrict__ stats_in,
                                                u16* __restrict__ x1, float* __restrict__ stats_out) {
  __shared__ __attribute__((aligned(16))) char lds[8 * 16384];
  const int tid = threadIdx.x, lane = tid & 63, wave = __builtin_amdgcn_readfirstlane(tid >> 6);
  const int xcd = blockIdx.x & 7, idx = blockIdx.x >> 3;
  const int rb = xcd * 4 + (idx & 3), cb = idx >> 2;
  const int wm = wave >> 1, wn = wave & 1, g = lane >> 4, m = lane & 15;
  const int row0 = rb * 64, col0 = cb * 64;
  const unsigned lb = lds_base(lds);
  const rsrc_t rh = mk(hpad, 2052 * 1024), rw = mk(w2, 512 * 1024), rs = mk(stats_in, 2048 * 128),
               rx = mk(xp, 2048 * 1024);
  const int row = row0 + 16 * wm + m;
  u32x4 sa[2];
  sa[0] = aload16(rs, row * 128 + g * 32, 0);
  sa[1] = aload16(rs, row * 128 + g * 32 + 16, 0);
  u32x4 xr = aload16(rx, (row * 512 + col0 + 32 * wn + swz_col(g)) * 2, 0);
  const unsigned va = ((row0 + 8 * wave + (lane >> 3)) * 512 + (((lane & 7) ^ (lane >> 3)) * 8)) * 2;
  const unsigned vb =
      ((8 * wave + (lane >> 3)) * 512 + col0 + ((lane & 7) ^ ((((lane >> 4) & 1) | ((wave & 1) << 1)) << 1)) * 8) * 2;
  auto issue = [&](int c) {
    unsigned s = lb + c * 16384 + wave * 1024;
    const int grp = c >> 1;
    const int sh = (grp == 0) ? 1 : (grp == 1) ? 3 : (grp == 2) ? 0 : 4;
    dma(rh, va, c * 128 + sh * 1024, s);
    dma(rw, vb, c * 65536, s + 8192);
  };
  float xn[8];
  auto hook = [&]() {
    pin(sa[0]); pin(sa[1]); pin(xr);
    float mean, var;
    stat_fin(asf(sa[0]), asf(sa[1]), mean, var);
    float r1 = __builtin_amdgcn_rsqf(var + 1e-5f);
    u32x2 xq[2];
    unswz8(xr, xq[0], xq[1]);
#pragma unroll
    for (int nt = 0; nt < 2; ++nt) {
      xn[nt * 4 + 0] = bfr((bflo(xq[nt][0]) - mean) * r1);
      xn[nt * 4 + 1] = bfr((bfhi(xq[nt][0]) - mean) * r1);
      xn[nt * 4 + 2] = bfr((bflo(xq[nt][1]) - mean) * r1);
      xn[nt * 4 + 3] = bfr((bfhi(xq[nt][1]) - mean) * r1);
    }
  };
  f32x4 acc[2] = {f32x4{0, 0, 0, 0}, f32x4{0, 0, 0, 0}};
  core64<8, 8, 4, false>(lds, issue, hook, acc, wave, lane);
  float v[8];
  u32x2 o[2];
#pragma unroll
  for (int nt = 0; nt < 2; ++nt) {
#pragma unroll
    for (int r = 0; r < 4; ++r) v[nt * 4 + r] = bfr(xn[nt * 4 + r] + bfr(acc[nt][r]));
    o[nt] = u32x2{pack2(v[nt * 4], v[nt * 4 + 1]), pack2(v[nt * 4 + 2], v[nt * 4 + 3])};
  }
  *(u32x4*)(x1 + (size_t)row * 512 + col0 + 32 * wn + swz_col(g)) = swz8(o[0], o[1]);
  stat_emit(stats_out, row, cb * 2 + wn, v, g);
}

__global__ __launch_bounds__(512, 1) void k5_w3(const u16* __restrict__ x1, const u16* __restrict__ w3,
                                                const float* __restrict__ cs, const float* __restrict__ stats,
                                                u16* __restrict__ h2) {
  __shared__ __attribute__((aligned(16))) char lds[5 * BIG_SLOT];
  const int tid = threadIdx.x, lane = tid & 63, wave = __builtin_amdgcn_readfirstlane(tid >> 6);
  const int xcd = blockIdx.x & 7, idx = blockIdx.x >> 3;
  const int rb = xcd * 2 + (idx & 1), cb = idx >> 1;
  const int wm = wave >> 1, wn = wave & 1, g = lane >> 4, m = lane & 15;
  const int row0 = rb * 128, col0 = cb * 96;
  const unsigned lb = lds_base(lds);
  const rsrc_t rx = mk(x1, 2048 * 1024), rw = mk(w3, 512 * F_ * 2), rs = mk(stats, 2048 * 128),
               rc = mk(cs, 4 * CSN * 4);
  u32x4 sa[2][2], cq[3][4];
#pragma unroll
  for (int mt = 0; mt < 2; ++mt) {
    unsigned vo = (row0 + 32 * wm + 16 * mt + m) * 128 + g * 32;
    sa[mt][0] = aload16(rs, vo, 0);
    sa[mt][1] = aload16(rs, vo + 16, 0);
  }
  const bool full = (cb < 14);
#pragma unroll
  for (int j = 0; j < 3; ++j)
#pragma unroll
    for (int i = 0; i < 4; ++i)
      cq[j][i] = aload16(rc, (col0 + 16 * wn + 4 * g) * 4, (i * CSN + 2048 + (full ? 32 * j : 0)) * 4);
  const unsigned va = ((row0 + 16 * wave + (lane >> 3)) * 512 + (((lane & 7) ^ (lane >> 3)) * 8)) * 2;
  const int bch = ((lane & 3) ^ (((lane >> 5) & 1) << 1)) * 8;
  const unsigned vb = ((16 * (wave & 3) + (lane >> 2)) * F_ + col0 + bch) * 2;
  const unsigned j1 = full ? 64 : 0, j2 = full ? 128 : 0;
  auto issue = [&](int c) {
    unsigned s = lb + (c % 5) * BIG_SLOT;
    dma(rx, va, c * 128, s + wave * 2048);
    dma(rx, va, c * 128 + 8192, s + wave * 2048 + 1024);
    if (wave < 4) {
      dma(rw, vb, c * 64 * F_ * 2, s + 16384 + wave * 1024);
      dma(rw, vb, c * 64 * F_ * 2 + j2, s + 16384 + 8192 + wave * 1024);
    } else {
      dma(rw, vb, c * 64 * F_ * 2 + j1, s + 16384 + 4096 + (wave - 4) * 1024);
    }
  };
  float mean[2], rstd[2];
  f32x4 csv[3];
  auto hook = [&]() {
#pragma unroll
    for (int mt = 0; mt < 2; ++mt) {
      pin(sa[mt][0]); pin(sa[mt][1]);
      float var;
      stat_fin(asf(sa[mt][0]), asf(sa[mt][1]), mean[mt], var);
      rstd[mt] = __builtin_amdgcn_rsqf(var + 1e-5f);
    }
#pragma unroll
    for (int j = 0; j < 3; ++j) {
#pragma unroll
      for (int i = 0; i < 4; ++i) pin(cq[j][i]);
      csv[j] = sum4(cq[j][0], cq[j][1], cq[j][2], cq[j][3]);
    }
  };
  f32x4 acc[2][3];
#pragma unroll
  for (int i = 0; i < 2; ++i)
#pragma unroll
    for (int j = 0; j < 3; ++j) acc[i][j] = f32x4{0, 0, 0, 0};
  core_big<8, 5, 4>(lds, issue, hook, acc, wave, lane);
  const int srow = row0 + 32 * wm + 16 * (g & 1) + m;
#pragma unroll
  for (int j = 0; j < 3; ++j) {
    if (j > 0 && !full) break;
    u32x2 o[2];
#pragma unroll
    for (int mt = 0; mt < 2; ++mt) {
      float y[4];
#pragma unroll
      for (int r = 0; r < 4; ++r) y[r] = gelu_t(bfr(rstd[mt] * __builtin_fmaf(-mean[mt], csv[j][r], acc[mt][j][r])));
      o[mt] = u32x2{pack2(y[0], y[1]), pack2(y[2], y[3])};
    }
    *(u32x4*)(h2 + (size_t)srow * F_ + col0 + 32 * j + 16 * wn + 8 * (g >> 1)) = swz8(o[0], o[1]);
  }
}

template <bool STATS>
__global__ __launch_bounds__(512, 1) void k6_w4(const u16* __restrict__ h2, const u16* __restrict__ w4,
                                                const u16* __restrict__ xp, const u16* __restrict__ x1,
                                                u16* __restrict__ xo, float* __restrict__ stats_out) {
  constexpr int RING = 9;
  __shared__ __attribute__((aligned(16))) char lds[RING * 16384];
  const int tid = threadIdx.x, lane = tid & 63, wave = __builtin_amdgcn_readfirstlane(tid >> 6);
  const int xcd = blockIdx.x & 7, idx = blockIdx.x >> 3;
  const int rb = xcd * 4 + (idx & 3), cb = idx >> 2;
  const int wm = wave >> 1, wn = wave & 1, g = lane >> 4, m = lane & 15;
  const int row0 = rb * 64, col0 = cb * 64;
  const unsigned lb = lds_base(lds);
  const rsrc_t rh = mk(h2, 2048 * F_ * 2 + 256), rw = mk(w4, F_ * 1024), rx = mk(xp, 2048 * 1024),
               ry = mk(x1, 2048 * 1024);
  const int row = row0 + 16 * wm + m;
  u32x4 xr = aload16(rx, (row * 512 + col0 + 32 * wn + swz_col(g)) * 2, 0);
  u32x4 yr = aload16(ry, (row * 512 + col0 + 32 * wn + swz_col(g)) * 2, 0);
  const unsigned va = ((row0 + 8 * wave + (lane >> 3)) * F_ + (((lane & 7) ^ (lane >> 3)) * 8)) * 2;
  const unsigned vb =
      ((8 * wave + (lane >> 3)) * 512 + col0 + ((lane & 7) ^ ((((lane >> 4) & 1) | ((wave & 1) << 1)) << 1)) * 8) * 2;
  auto issue = [&](int c) {
    unsigned s = lb + (c % RING) * 16384 + wave * 1024;
    dma(rh, va, c * 128, s);
    dma(rw, vb, (c == 21 && wave >= 4) ? c * 65536 - 32768 : c * 65536, s + 8192);
  };
  float xx[8], yy[8];
  auto hook = [&]() {
    pin(xr); pin(yr);
    u32x2 a[2], b[2];
    unswz8(xr, a[0], a[1]);
    unswz8(yr, b[0], b[1]);
#pragma unroll
    for (int nt = 0; nt < 2; ++nt) {
      xx[nt * 4 + 0] = bflo(a[nt][0]); xx[nt * 4 + 1] = bfhi(a[nt][0]);
      xx[nt * 4 + 2] = bflo(a[nt][1]); xx[nt * 4 + 3] = bfhi(a[nt][1]);
      yy[nt * 4 + 0] = bflo(b[nt][0]); yy[nt * 4 + 1] = bfhi(b[nt][0]);
      yy[nt * 4 + 2] = bflo(b[nt][1]); yy[nt * 4 + 3] = bfhi(b[nt][1]);
    }
  };
  f32x4 acc[2] = {f32x4{0, 0, 0, 0}, f32x4{0, 0, 0, 0}};
  core64<22, RING, 5, true>(lds, issue, hook, acc, wave, lane);
  float v[8];
  u32x2 o[2];
#pragma unroll
  for (int nt = 0; nt < 2; ++nt) {
#pragma unroll
    for (int r = 0; r < 4; ++r) v[nt * 4 + r] = bfr(xx[nt * 4 + r] + bfr(yy[nt * 4 + r] + bfr(acc[nt][r])));
    o[nt] = u32x2{pack2(v[nt * 4], v[nt * 4 + 1]), pack2(v[nt * 4 + 2], v[nt * 4 + 3])};
  }
  *(u32x4*)(xo + (size_t)row * 512 + col0 + 32 * wn + swz_col(g)) = swz8(o[0], o[1]);
  if (STATS) stat_emit(stats_out, row, cb * 2 + wn, v, g);
}

extern "C" void kernel_launch(void* const* d_in, const int* in_sizes, int n_in, void* d_out, int out_size,
                              void* d_ws, size_t ws_size, hipStream_t stream) {
  const u16* A_x = (const u16*)d_in[0];
  const u16* wqkv = (const u16*)d_in[1];
  const u16* wphi = (const u16*)d_in[2];
  const u16* wo = (const u16*)d_in[3];
  const u16* w1 = (const u16*)d_in[5];
  const u16* w2 = (const u16*)d_in[6];
  const u16* w3 = (const u16*)d_in[7];
  const u16* w4 = (const u16*)d_in[8];
  char* ws = (char*)d_ws;
  size_t off = 0;
  auto take = [&](size_t bytes) { char* p = ws + off; off += (bytes + 255) & ~(size_t)255; return p; };
  float* kvacc = (float*)take(2 * 32 * 544 * 4);
  float* cs = (float*)take(4 * CSN * 4);
  float* st_x = (float*)take(2048 * 32 * 4);
  float* st_xp = (float*)take(2048 * 32 * 4);
  float* st_x1 = (float*)take(2048 * 32 * 4);
  u16* phiq = (u16*)take((size_t)2048 * 1024 * 2);
  u16* xp = (u16*)take((size_t)2048 * 512 * 2);
  u16* hpad = (u16*)take((size_t)2052 * 512 * 2);
  u16* x1 = (u16*)take((size_t)2048 * 512 * 2);
  u16* h2 = (u16*)take((size_t)2048 * F_ * 2 + 256);
  u16* x2 = (u16*)take((size_t)2048 * 512 * 2);
  if (off > ws_size) return;

  k0_prep<<<256, 256, 0, stream>>>(A_x, wqkv, w1, w3, kvacc, hpad, cs, st_x);
  for (int it = 0; it < 2; ++it) {
    const u16* xin = it == 0 ? A_x : x2;
    float* kva = kvacc + it * 32 * 544;
    k1_qkv<<<256, 512, 0, stream>>>(xin, wqkv, wphi, cs, st_x, phiq, kva);
    k2_wo<<<256, 512, 0, stream>>>(xin, phiq, kva, wo, xp, st_xp);
    k3_w1<<<256, 512, 0, stream>>>(xp, w1, cs, st_xp, hpad);
    k4_w2<<<256, 512, 0, stream>>>(hpad, w2, xp, st_xp, x1, st_x1);
    k5_w3<<<240, 512, 0, stream>>>(x1, w3, cs, st_x1, h2);
    if (it == 0) k6_w4<true><<<256, 512, 0, stream>>>(h2, w4, xp, x1, x2, st_x);
    else k6_w4<false><<<256, 512, 0, stream>>>(h2, w4, xp, x1, (u16*)d_out, st_x);
  }
}
```

```cpp
#include <hip/hip_runtime.h>
#include <hip/hip_bf16.h>
#include <stdint.h>

typedef unsigned short u16;
typedef short s16x4 __attribute__((ext_vector_type(4)));
typedef short s16x8 __attribute__((ext_vector_type(8)));
typedef __bf16 bf16x8 __attribute__((ext_vector_type(8)));
typedef __bf16 bf16x2 __attribute__((ext_vector_type(2)));
typedef float f32x4 __attribute__((ext_vector_type(4)));
typedef float f32x2 __attribute__((ext_vector_type(2)));
typedef unsigned u32x4 __attribute__((ext_vector_type(4)));
typedef unsigned u32x2 __attribute__((ext_vector_type(2)));
typedef __attribute__((address_space(3))) s16x4* lds_s16x4_p;
typedef __amdgpu_buffer_rsrc_t rsrc_t;

constexpr int F_ = 1376;
constexpr int CSN = 3456;

#define DEVI __device__ __forceinline__

#define PARG
#define PPASS(x)
#define TSDECL
#define STAMP(i)
#define TSFLUSH
#define TSP
#define TSA

DEVI unsigned pack2(float a, float b) {
  bf16x2 v = {(__bf16)a, (__bf16)b};
  return __builtin_bit_cast(unsigned, v);
}
DEVI unsigned pack2(f32x2 a) { return pack2(a[0], a[1]); }
DEVI float bflo(unsigned u) { return __uint_as_float(u << 16); }
DEVI float bfhi(unsigned u) { return __uint_as_float(u & 0xffff0000u); }
DEVI f32x2 unpk(unsigned u) { return f32x2{bflo(u), bfhi(u)}; }

template <int N> DEVI void wait_vm() { asm volatile("s_waitcnt vmcnt(%0)" ::"n"(N) : "memory"); }
DEVI void wait_lgkm0() { asm volatile("s_waitcnt lgkmcnt(0)" ::: "memory"); }
DEVI void bar() { __builtin_amdgcn_s_barrier(); }
template <class T> DEVI void pin(T& v) { asm volatile("" : "+v"(v)); }

DEVI rsrc_t mk(const void* p, unsigned n) { return __builtin_amdgcn_make_buffer_rsrc((void*)p, 0, n, 0x00020000); }
DEVI void dma(rsrc_t r, unsigned voff, unsigned soff, unsigned lb) {
  asm volatile("s_mov_b32 m0, %0\n\ts_nop 0\n\tbuffer_load_dwordx4 %1, %2, %3 offen lds" ::"s"(lb), "v"(voff), "s"(r),
               "s"(soff)
               : "memory");
}
DEVI u32x4 aload16(rsrc_t r, unsigned voff, unsigned soff) {
  u32x4 v;
  asm volatile("buffer_load_dwordx4 %0, %1, %2, %3 offen" : "=v"(v) : "v"(voff), "s"(r), "s"(soff) : "memory");
  return v;
}
DEVI u32x2 aload8(rsrc_t r, unsigned voff, unsigned soff) {
  u32x2 v;
  asm volatile("buffer_load_dwordx2 %0, %1, %2, %3 offen" : "=v"(v) : "v"(voff), "s"(r), "s"(soff) : "memory");
  return v;
}
DEVI f32x4 asf(u32x4 v) { return __builtin_bit_cast(f32x4, v); }

DEVI s16x4 trread(const char* l) { return __builtin_amdgcn_ds_read_tr16_b64_v4i16((lds_s16x4_p)l); }
DEVI f32x4 mfma32(s16x8 a, s16x8 b, f32x4 c) {
  return __builtin_amdgcn_mfma_f32_16x16x32_bf16(__builtin_bit_cast(bf16x8, a), __builtin_bit_cast(bf16x8, b), c, 0, 0, 0);
}
DEVI f32x4 mfma16(s16x4 a, s16x4 b, f32x4 c) { return __builtin_amdgcn_mfma_f32_16x16x16bf16_1k(a, b, c, 0, 0, 0); }
DEVI s16x8 cat(s16x4 a, s16x4 b) { return __builtin_shufflevector(a, b, 0, 1, 2, 3, 4, 5, 6, 7); }
DEVI f32x2 lo2(f32x4 v) { return f32x2{v[0], v[1]}; }
DEVI f32x2 hi2(f32x4 v) { return f32x2{v[2], v[3]}; }
DEVI f32x2 fma2(f32x2 a, f32x2 b, f32x2 c) { return __builtin_elementwise_fma(a, b, c); }
DEVI f32x2 bc2(float a) { return f32x2{a, a}; }
DEVI f32x2 gelu2(f32x2 x) {
  f32x2 t = fma2(x * x, bc2(-0.10294324f), bc2(-2.30220819f));
  t = x * t;
  f32x2 e = {__builtin_amdgcn_exp2f(t[0]), __builtin_amdgcn_exp2f(t[1])};
  e = e + bc2(1.0f);
  f32x2 r = {__builtin_amdgcn_rcpf(e[0]), __builtin_amdgcn_rcpf(e[1])};
  return x * r;
}
DEVI void sw16(float& a, float& b) {
  auto r = __builtin_amdgcn_permlane16_swap(__float_as_uint(a), __float_as_uint(b), false, false);
  a = __uint_as_float(r[0]); b = __uint_as_float(r[1]);
}
DEVI void sw32(float& a, float& b) {
  auto r = __builtin_amdgcn_permlane32_swap(__float_as_uint(a), __float_as_uint(b), false, false);
  a = __uint_as_float(r[0]); b = __uint_as_float(r[1]);
}
DEVI float rsum_g(float v) {
  float a = v, b = v;
  sw16(a, b);
  a = a + b; b = a;
  sw32(a, b);
  return a + b;
}
template <bool MAX> DEVI float red4(float a, float b, float c, float d) {
  sw16(a, b); sw16(c, d);
  float e = MAX ? fmaxf(a, b) : a + b, f = MAX ? fmaxf(c, d) : c + d;
  sw32(e, f);
  return MAX ? fmaxf(e, f) : e + f;
}
DEVI void bcast4(float h, float& a, float& b, float& c, float& d) {
  float h1 = h, h2 = h;
  sw16(h1, h2);
  a = h1; c = h1; b = h2; d = h2;
  sw32(a, c); sw32(b, d);
}
DEVI u32x4 swz8(u32x4 p) {
  auto a = __builtin_amdgcn_permlane16_swap(p[0], p[2], false, false);
  auto b = __builtin_amdgcn_permlane16_swap(p[1], p[3], false, false);
  return u32x4{a[0], b[0], a[1], b[1]};
}
DEVI int swz_col(int g) { return (g & 1) * 16 + (g >> 1) * 8; }

DEVI void stat_fin(f32x4 a, f32x4 b, float& mean, float& var) {
  float s = rsum_g((a[0] + a[2]) + (b[0] + b[2]));
  mean = s * (1.0f / 16.0f);
  float d0 = a[0] - mean, d1 = a[2] - mean, d2 = b[0] - mean, d3 = b[2] - mean;
  float m2 = ((a[1] + a[3]) + (b[1] + b[3])) + 32.0f * ((d0 * d0 + d1 * d1) + (d2 * d2 + d3 * d3));
  m2 = rsum_g(m2);
  var = m2 * (1.0f / 512.0f);
}
DEVI void stat_emit(float* stats, int rb, int cg, u32x4 o, int lane) {
  f32x2 v0 = unpk(o[0]), v1 = unpk(o[1]), v2 = unpk(o[2]), v3 = unpk(o[3]);
  f32x2 s2 = (v0 + v1) + (v2 + v3);
  float mean = rsum_g(s2[0] + s2[1]) * (1.0f / 32.0f);
  f32x2 mm = bc2(mean);
  v0 -= mm; v1 -= mm; v2 -= mm; v3 -= mm;
  f32x2 q2 = fma2(v1, v1, v0 * v0) + fma2(v3, v3, v2 * v2);
  float q = rsum_g(q2[0] + q2[1]);
  if (lane < 16) {
    f32x2 r = {mean, q};
    *(f32x2*)(stats + rb * 512 + ((cg >> 1) & 1) * 256 + ((cg >> 2) * 16 + lane) * 4 + (cg & 1) * 2) = r;
  }
}

template <int N> DEVI void wait_vm_n(int n) {
  if constexpr (N > 0) {
    if (n >= N) wait_vm<N>(); else wait_vm_n<N - 1>(n);
  } else {
    wait_vm<0>();
  }
}
DEVI unsigned lds_base(const char* p) {
  return (unsigned)(uintptr_t)(__attribute__((address_space(3))) const char*)p;
}

__global__ __launch_bounds__(512) void k0_prep(const u16* __restrict__ x, const u16* __restrict__ wqkv,
                                               const u16* __restrict__ w1, const u16* __restrict__ w3,
                                               float* __restrict__ kvacc, u16* __restrict__ hF,
                                               float* __restrict__ cs, float* __restrict__ stats,
                                               u16* __restrict__ xF PARG) {
  __shared__ __attribute__((aligned(16))) char lds[16384];
  TSDECL STAMP(0);
  const int b = blockIdx.x, tid = threadIdx.x, lane = tid & 63, wave = tid >> 6;
  if (b < 128) {
    u32x4 xv[2];
#pragma unroll
    for (int i = 0; i < 2; ++i) xv[i] = *(const u32x4*)(x + (size_t)(b * 16 + wave * 2 + i) * 512 + lane * 8);
    if (tid < 68) { f32x4 z = {0, 0, 0, 0}; ((f32x4*)kvacc)[b * 68 + tid] = z; }
    if (b < 4) {
      u32x4 z = {0, 0, 0, 0};
      *(u32x4*)((char*)hF + (size_t)(b >> 1) * 129 * 16384 + (b & 1) * 8192 + tid * 16) = z;
    }
#pragma unroll
    for (int i = 0; i < 2; ++i) {
      const int m = wave * 2 + i;
      char* d = lds + (lane >> 2) * 1024 + ((lane & 1) * 32 + m) * 16 + ((lane >> 1) & 1) * 8;
      *(u32x2*)d = u32x2{xv[i][0], xv[i][1]};
      *(u32x2*)(d + 256) = u32x2{xv[i][2], xv[i][3]};
      f32x2 v0 = unpk(xv[i][0]), v1 = unpk(xv[i][1]), v2 = unpk(xv[i][2]), v3 = unpk(xv[i][3]);
      f32x2 s2 = (v0 + v1) + (v2 + v3);
      float s = s2[0] + s2[1];
      s += __shfl_xor(s, 1); s += __shfl_xor(s, 2);
      float mean = s * (1.0f / 32.0f);
      f32x2 mm = bc2(mean);
      v0 -= mm; v1 -= mm; v2 -= mm; v3 -= mm;
      f32x2 q2 = fma2(v1, v1, v0 * v0) + fma2(v3, v3, v2 * v2);
      float q = q2[0] + q2[1];
      q += __shfl_xor(q, 1); q += __shfl_xor(q, 2);
      if ((lane & 3) == 0) {
        const int cg = lane >> 2;
        f32x2 r = {mean, q};
        *(f32x2*)(stats + b * 512 + ((cg >> 1) & 1) * 256 + ((cg >> 2) * 16 + m) * 4 + (cg & 1) * 2) = r;
      }
    }
    __syncthreads();
#pragma unroll
    for (int i = 0; i < 2; ++i)
      *(u32x4*)((char*)xF + (size_t)b * 16384 + i * 8192 + tid * 16) = *(const u32x4*)(lds + i * 8192 + tid * 16);
  } else {
    const int cbk = (b - 128) >> 1, rh = b & 1;
    const u16* W; int ld, c0, oc;
    if (cbk < 24) { W = wqkv; ld = 1536; c0 = cbk * 64; oc = c0; }
    else if (cbk < 32) { W = w1; ld = 512; c0 = (cbk - 24) * 64; oc = 1536 + c0; }
    else { W = w3; ld = F_; c0 = (cbk - 32) * 64; oc = 2048 + c0; }
    const int r = tid >> 3, c8 = tid & 7;
    const bool wact = (c0 + c8 * 8 < ld);
    u32x4 wv[4];
#pragma unroll
    for (int i = 0; i < 4; ++i) {
      wv[i] = u32x4{0, 0, 0, 0};
      if (wact) wv[i] = *(const u32x4*)(W + (size_t)(rh * 256 + r + 64 * i) * ld + c0 + c8 * 8);
    }
    float* red = (float*)lds;
    float a[8];
#pragma unroll
    for (int j = 0; j < 4; ++j) {
      f32x2 t = (unpk(wv[0][j]) + unpk(wv[1][j])) + (unpk(wv[2][j]) + unpk(wv[3][j]));
      a[2 * j] = t[0]; a[2 * j + 1] = t[1];
    }
#pragma unroll
    for (int j = 0; j < 8; ++j) { a[j] += __shfl_xor(a[j], 8); a[j] = rsum_g(a[j]); }
    if (lane < 8) {
#pragma unroll
      for (int j = 0; j < 8; ++j) red[wave * 64 + lane * 8 + j] = a[j];
    }
    __syncthreads();
    if (tid < 64) {
      float s = 0.f;
#pragma unroll
      for (int w = 0; w < 8; ++w) s += red[w * 64 + tid];
      cs[rh * CSN + oc + tid] = s;
    }
  }
  TSFLUSH;
}

struct Frag64 { s16x8 x, w0, w1; };
template <int NCH, int RING, int P, bool HALF_LAST, class ISS, class HOOK>
DEVI void core64(const char* lds, ISS issue, HOOK hook, f32x4 (&acc)[2], int wave, int lane TSP) {
  const int wm = wave >> 1, wn = wave & 1, g = lane >> 4, q = (lane & 15) >> 2, p = lane & 3;
  const int xoff = wm * 2048 + lane * 16;
  const int fsw = (q >> 1) | ((g & 1) << 1);
  const int woff0 = 8192 + (4 * g + q) * 128 + p * 8 + (((2 * wn) ^ fsw) * 32);
  const int woff1 = 8192 + (4 * g + q) * 128 + p * 8 + (((2 * wn + 1) ^ fsw) * 32);
  auto rd = [&](int c, int ks) {
    const char* s = lds + (c % RING) * 16384;
    Frag64 f;
    f.x = *(const s16x8*)(s + xoff + ks * 1024);
    f.w0 = cat(trread(s + woff0 + ks * 4096), trread(s + woff0 + ks * 4096 + 2048));
    f.w1 = cat(trread(s + woff1 + ks * 4096), trread(s + woff1 + ks * 4096 + 2048));
    return f;
  };
  auto mm = [&](const Frag64& f) {
    acc[0] = mfma32(f.w0, f.x, acc[0]);
    acc[1] = mfma32(f.w1, f.x, acc[1]);
  };
#pragma unroll
  for (int c = 0; c < P; ++c) issue(c);
  STAMP(1);
  Frag64 f1;
#pragma unroll
  for (int c = 0; c < NCH; ++c) {
    int last = c + P - 1;
    if (last > NCH - 1) last = NCH - 1;
    wait_vm_n<2 * (P - 1)>((last - c) * 2);
    if (RING < NCH) wait_lgkm0();
    bar();
    STAMP(4 + c);
    Frag64 f0 = rd(c, 0);
    if (c + P < NCH) issue(c + P);
    if (c > 0) mm(f1);
    if (!(HALF_LAST && c == NCH - 1)) f1 = rd(c, 1);
    if (c == 0) hook();
    mm(f0);
  }
  if (!HALF_LAST) mm(f1);
}

constexpr int BIG_SLOT = 28672;
struct FragB { s16x8 x[2], w[3]; };
template <int NCH, int RING, int P, class ISS, class HOOK>
DEVI void core_big(const char* lds, ISS issue, HOOK hook, f32x4 (&acc)[2][3], const int (&woff)[3], int wave,
                   int lane TSP) {
  const int wm = wave >> 1;
  const int xoff = wm * 4096 + lane * 16;
  auto rd = [&](int c, int ks) {
    const char* s = lds + (c % RING) * BIG_SLOT;
    FragB f;
#pragma unroll
    for (int mt = 0; mt < 2; ++mt) f.x[mt] = *(const s16x8*)(s + xoff + mt * 2048 + ks * 1024);
#pragma unroll
    for (int j = 0; j < 3; ++j) f.w[j] = cat(trread(s + woff[j] + ks * 2048), trread(s + woff[j] + ks * 2048 + 1024));
    return f;
  };
  auto mm = [&](const FragB& f) {
#pragma unroll
    for (int j = 0; j < 3; ++j)
#pragma unroll
      for (int mt = 0; mt < 2; ++mt) acc[mt][j] = mfma32(f.w[j], f.x[mt], acc[mt][j]);
  };
#pragma unroll
  for (int c = 0; c < P; ++c) issue(c);
  STAMP(1);
  FragB f1;
#pragma unroll
  for (int c = 0; c < NCH; ++c) {
    int last = c + P - 1;
    if (last > NCH - 1) last = NCH - 1;
    if (wave < 4) wait_vm_n<4 * (P - 1)>((last - c) * 4);
    else wait_vm_n<3 * (P - 1)>((last - c) * 3);
    wait_lgkm0();
    bar();
    STAMP(4 + c);
    FragB f0 = rd(c, 0);
    if (c + P < NCH) issue(c + P);
    if (c > 0) mm(f1);
    f1 = rd(c, 1);
    if (c == 0) hook();
    mm(f0);
  }
  mm(f1);
}

DEVI void dual_softmax2(f32x4 p0, f32x4 p1, float c0, float c1, float csel, u32x4& o0, u32x4& o1) {
  float mx0 = fmaxf(fmaxf(p0[0], p0[1]), fmaxf(p0[2], p0[3]));
  float nm0 = -fminf(fminf(p0[0], p0[1]), fminf(p0[2], p0[3]));
  float mx1 = fmaxf(fmaxf(p1[0], p1[1]), fmaxf(p1[2], p1[3]));
  float nm1 = -fminf(fminf(p1[0], p1[1]), fminf(p1[2], p1[3]));
  float h = red4<true>(mx0, nm0, mx1, nm1) * csel;
  bcast4(h, mx0, nm0, mx1, nm1);
  float ep0[4], em0[4], ep1[4], em1[4];
#pragma unroll
  for (int r = 0; r < 4; ++r) {
    ep0[r] = __builtin_amdgcn_exp2f(__builtin_fmaf(p0[r], c0, -mx0));
    em0[r] = __builtin_amdgcn_exp2f(__builtin_fmaf(p0[r], -c0, -nm0));
    ep1[r] = __builtin_amdgcn_exp2f(__builtin_fmaf(p1[r], c1, -mx1));
    em1[r] = __builtin_amdgcn_exp2f(__builtin_fmaf(p1[r], -c1, -nm1));
  }
  float s0 = (ep0[0] + ep0[1]) + (ep0[2] + ep0[3]), s1 = (em0[0] + em0[1]) + (em0[2] + em0[3]);
  float s2 = (ep1[0] + ep1[1]) + (ep1[2] + ep1[3]), s3 = (em1[0] + em1[1]) + (em1[2] + em1[3]);
  h = __builtin_amdgcn_rcpf(red4<false>(s0, s1, s2, s3));
  bcast4(h, s0, s1, s2, s3);
  o0 = u32x4{pack2(f32x2{ep0[0], ep0[1]} * bc2(s0)), pack2(f32x2{ep0[2], ep0[3]} * bc2(s0)),
             pack2(f32x2{em0[0], em0[1]} * bc2(s1)), pack2(f32x2{em0[2], em0[3]} * bc2(s1))};
  o1 = u32x4{pack2(f32x2{ep1[0], ep1[1]} * bc2(s2)), pack2(f32x2{ep1[2], ep1[3]} * bc2(s2)),
             pack2(f32x2{em1[0], em1[1]} * bc2(s3)), pack2(f32x2{em1[2], em1[3]} * bc2(s3))};
}

constexpr int AUX_BIG = 5 * BIG_SLOT;
__global__ __launch_bounds__(512, 1) void k1_qkv(const u16* __restrict__ xF, const u16* __restrict__ wqkv,
                                                 const u16* __restrict__ wphi, const float* __restrict__ cs,
                                                 const float* __restrict__ stats, u16* __restrict__ phiq,
                                                 float* __restrict__ kvacc PARG) {
  __shared__ __attribute__((aligned(16))) char lds[AUX_BIG + 16384 + 1024];
  TSDECL STAMP(0);
  const int tid = threadIdx.x, lane = tid & 63, wave = __builtin_amdgcn_readfirstlane(tid >> 6);
  const int xcd = blockIdx.x & 7, idx = blockIdx.x >> 3;
  const int rb = xcd * 2 + (idx & 1), hp = idx >> 1;
  const int wm = wave >> 1, wn = wave & 1, g = lane >> 4, m = lane & 15, q = m >> 2, p = lane & 3;
  const int head = hp * 2 + wn;
  const unsigned lb = lds_base(lds);
  const rsrc_t rx = mk(xF, 2048 * 1024), rw = mk(wqkv, 512 * 1536 * 2), rs = mk(stats, 2048 * 128),
               rc = mk(cs, 2 * CSN * 4);
  const unsigned l16 = lane * 16;
  dma(rs, l16, (rb * 8 + wave) * 2048, lb + AUX_BIG + wave * 2048);
  dma(rs, l16, (rb * 8 + wave) * 2048 + 1024, lb + AUX_BIG + wave * 2048 + 1024);
  if (wave == 0) {
    const int i = lane >> 3;
    unsigned vo = (((i >= 3) ? CSN : 0) + (i % 3) * 512 + hp * 32 + (lane & 7) * 4) * 4;
    if (lane < 48) dma(rc, vo, 0, lb + AUX_BIG + 16384);
  }
  s16x4 wp;
#pragma unroll
  for (int j = 0; j < 4; ++j) wp[j] = (short)wphi[head * 256 + (4 * g + j) * 16 + m];

  const int bch = ((lane & 3) ^ (((lane >> 4) & 1) << 1)) * 8;
  const unsigned vb = ((16 * (wave & 3) + (lane >> 2)) * 1536 + hp * 32 + bch) * 2;
  const unsigned sa = (rb * 8 + wave) * 16384;
  auto issue = [&](int c) {
    unsigned s = lb + (c % 5) * BIG_SLOT;
    dma(rx, l16, sa + c * 2048, s + wave * 2048);
    dma(rx, l16, sa + c * 2048 + 1024, s + wave * 2048 + 1024);
    if (wave < 4) {
      dma(rw, vb, c * 64 * 3072, s + 16384 + wave * 1024);
      dma(rw, vb, c * 64 * 3072 + 2048, s + 16384 + 8192 + wave * 1024);
    } else {
      dma(rw, vb, c * 64 * 3072 + 1024, s + 16384 + 4096 + (wave - 4) * 1024);
    }
  };
  float mean[2], rstd[2];
  f32x4 csv[3];
  auto hook = [&]() {
    const char* ax = lds + AUX_BIG;
#pragma unroll
    for (int mt = 0; mt < 2; ++mt) {
      f32x4 a = *(const f32x4*)(ax + (2 * wm + mt) * 2048 + l16), b = *(const f32x4*)(ax + (2 * wm + mt) * 2048 + 1024 + l16);
      float var;
      stat_fin(a, b, mean[mt], var);
      rstd[mt] = __builtin_amdgcn_rsqf(var + 1e-5f);
    }
#pragma unroll
    for (int j = 0; j < 3; ++j)
      csv[j] = *(const f32x4*)(ax + 16384 + j * 128 + wn * 64 + g * 16) +
               *(const f32x4*)(ax + 16384 + 384 + j * 128 + wn * 64 + g * 16);
  };
  f32x4 acc[2][3];
#pragma unroll
  for (int i = 0; i < 2; ++i)
#pragma unroll
    for (int j = 0; j < 3; ++j) acc[i][j] = f32x4{0, 0, 0, 0};
  const int wo_ = 16384 + (4 * g + q) * 64 + ((wn ^ (g & 1)) * 32) + p * 8;
  const int woff[3] = {wo_, wo_ + 4096, wo_ + 8192};
  core_big<8, 5, 4>(lds, issue, hook, acc, woff, wave, lane TSA);
  STAMP(2);

  char* sk = lds + wave * 3072;
  char* sv = sk + 2048;
  const f32x4 zero = {0, 0, 0, 0};
  const float L2E = 1.44269504f;
  const float c0 = rstd[0] * L2E, c1 = rstd[1] * L2E, csel = (g < 2) ? c0 : c1;
  s16x4 qf[2];
  f32x4 pk[2];
#pragma unroll
  for (int mt = 0; mt < 2; ++mt) {
    const f32x2 nm = bc2(-mean[mt]), rs2 = bc2(rstd[mt]);
    u32x2 t;
    t = u32x2{pack2(fma2(nm, lo2(csv[0]), lo2(acc[mt][0]))), pack2(fma2(nm, hi2(csv[0]), hi2(acc[mt][0])))};
    qf[mt] = __builtin_bit_cast(s16x4, t);
    t = u32x2{pack2(fma2(nm, lo2(csv[1]), lo2(acc[mt][1]))), pack2(fma2(nm, hi2(csv[1]), hi2(acc[mt][1])))};
    pk[mt] = mfma16(wp, __builtin_bit_cast(s16x4, t), zero);
    t = u32x2{pack2(rs2 * fma2(nm, lo2(csv[2]), lo2(acc[mt][2]))), pack2(rs2 * fma2(nm, hi2(csv[2]), hi2(acc[mt][2])))};
    *(u32x2*)(sv + (16 * mt + m) * 32 + g * 8) = t;
  }
  {
    u32x4 f0, f1;
    dual_softmax2(pk[0], pk[1], c0, c1, csel, f0, f1);
    *(u32x4*)(sk + m * 64 + g * 16) = f0;
    *(u32x4*)(sk + (16 + m) * 64 + g * 16) = f1;
  }
  wait_lgkm0();
  s16x8 vt = cat(trread(sv + (4 * g + q) * 32 + p * 8), trread(sv + (16 + 4 * g + q) * 32 + p * 8));
  const s16x8 ones = {0x3f80, 0x3f80, 0x3f80, 0x3f80, 0x3f80, 0x3f80, 0x3f80, 0x3f80};
  float* red = (float*)(lds + 3 * BIG_SLOT) + wave * 544;
#pragma unroll
  for (int b = 0; b < 2; ++b) {
    s16x8 kt = cat(trread(sk + (4 * g + q) * 64 + b * 32 + p * 8), trread(sk + (16 + 4 * g + q) * 64 + b * 32 + p * 8));
    f32x4 kv = mfma32(kt, vt, zero);
    f32x4 zz = mfma32(kt, ones, zero);
    *(f32x4*)(red + ((g & 1) * 64 + (2 * b + (g >> 1)) * 16 + m) * 4) = kv;
    if (m == 0) *(f32x4*)(red + 512 + 16 * b + 4 * g) = zz;
  }
  wait_lgkm0();
  bar();
  {
    const float* r0 = (const float*)(lds + 3 * BIG_SLOT);
#pragma unroll
    for (int i = 0; i < 3; ++i) {
      if (i == 2 && wave != 0) break;
      int hh, e;
      if (i < 2) { int sl = 2 * wave + i; hh = sl >> 3; e = (sl & 7) * 64 + lane; }
      else { hh = lane >> 5; e = 512 + (lane & 31); }
      const float* rr = r0 + hh * 544 + e;
      float v = (rr[0] + rr[2 * 544]) + (rr[4 * 544] + rr[6 * 544]);
      atomicAdd(kvacc + (hp * 2 + hh) * 544 + e, v);
    }
  }
  {
    f32x4 pq0 = mfma16(wp, qf[0], zero), pq1 = mfma16(wp, qf[1], zero);
    u32x4 o0, o1;
    dual_softmax2(pq0, pq1, c0, c1, csel, o0, o1);
    char* dst = (char*)phiq + ((size_t)(rb * 8 + 2 * wm) * 32 + head) * 1024 + l16;
    *(u32x4*)dst = o0;
    *(u32x4*)(dst + 32768) = o1;
  }
  TSFLUSH;
}

__global__ __launch_bounds__(512, 1) void k2_wo(const u16* __restrict__ xF, const u16* __restrict__ phiq,
                                                const float* __restrict__ kvacc, const u16* __restrict__ wo,
                                                u16* __restrict__ xo, float* __restrict__ stats PARG) {
  __shared__ __attribute__((aligned(16))) char lds[8 * 16384 + 32768];
  TSDECL STAMP(0);
  char* oL = lds + 8 * 16384;
  const int tid = threadIdx.x, lane = tid & 63, wave = __builtin_amdgcn_readfirstlane(tid >> 6);
  const int xcd = blockIdx.x & 7, idx = blockIdx.x >> 3;
  const int rb = xcd * 8 + (idx & 7), cb = idx >> 3;
  const int wm = wave >> 2, wn = wave & 3, g = lane >> 4, m = lane & 15, q = m >> 2, p = lane & 3;
  const int col0 = cb * 128;
  const unsigned lb = lds_base(lds);
  const rsrc_t rx = mk(xF, 2048 * 1024), rp = mk(phiq, 2048 * 2048), rk = mk(kvacc, 32 * 544 * 4),
               rw = mk(wo, 512 * 1024);
  const unsigned l16 = lane * 16;
  u32x4 kv[4][2], pf[4][2];
  u32x2 zz = aload8(rk, (lane >> 4) * 2176 + 2048 + (lane & 15) * 8, wave * 4 * 2176);
#pragma unroll
  for (int hh = 0; hh < 4; ++hh) {
    const int h = wave * 4 + hh;
    kv[hh][0] = aload16(rk, l16, h * 2176);
    kv[hh][1] = aload16(rk, l16, h * 2176 + 1024);
#pragma unroll
    for (int mt = 0; mt < 2; ++mt) pf[hh][mt] = aload16(rp, l16, ((rb * 2 + mt) * 32 + h) * 1024);
  }
  const int frag = (rb * 2 + wm) * 16 + cb * 4 + wn;
  u32x4 xr = aload16(rx, l16, frag * 1024);
  const int kr = 8 * wave + (lane >> 3);
  const unsigned vb = (kr * 512 + col0 + ((lane & 7) ^ (((kr >> 1) & 3) << 1)) * 8) * 2;
  auto issue = [&](int c) {
    unsigned s = lb + c * 16384 + wave * 1024;
    dma(rw, vb, c * 65536, s);
    dma(rw, vb, c * 65536 + 128, s + 8192);
  };
#pragma unroll
  for (int c = 0; c < 4; ++c) issue(c);
  STAMP(1);
  const f32x4 zero = {0, 0, 0, 0};
  char* zs = oL + wave * 2048;
  wait_vm<8 + 17>();
  pin(zz);
  *(unsigned*)(zs + lane * 4) = pack2(__uint_as_float(zz[0]), __uint_as_float(zz[1]));
  s16x8 zf[4];
#pragma unroll
  for (int hh = 0; hh < 4; ++hh) zf[hh] = *(const s16x8*)(zs + hh * 64 + g * 16);
  wait_lgkm0();
  STAMP(28);
#pragma unroll
  for (int hp = 0; hp < 2; ++hp) {
    if (hp == 0) wait_vm<8 + 9>(); else wait_vm<8 + 1 + 4>();
    f32x2 ov[2][4];
#pragma unroll
    for (int hs = 0; hs < 2; ++hs) {
      const int hh = hp * 2 + hs;
      pin(kv[hh][0]); pin(kv[hh][1]); pin(pf[hh][0]); pin(pf[hh][1]);
      f32x4 k0 = asf(kv[hh][0]), k1 = asf(kv[hh][1]);
      u32x4 kq = {pack2(k0[0], k0[1]), pack2(k0[2], k0[3]), pack2(k1[0], k1[1]), pack2(k1[2], k1[3])};
      s16x8 kf = __builtin_bit_cast(s16x8, kq);
#pragma unroll
      for (int mt = 0; mt < 2; ++mt) {
        s16x8 pp = __builtin_bit_cast(s16x8, pf[hh][mt]);
        f32x4 num = mfma32(kf, pp, zero);
        f32x4 den = mfma32(zf[hh], pp, zero);
        f32x2 inv = bc2(__builtin_amdgcn_rcpf(den[0] + 1e-6f));
        ov[mt][hs * 2] = lo2(num) * inv;
        ov[mt][hs * 2 + 1] = hi2(num) * inv;
      }
    }
#pragma unroll
    for (int mt = 0; mt < 2; ++mt) {
      u32x4 o = {pack2(ov[mt][0]), pack2(ov[mt][1]), pack2(ov[mt][2]), pack2(ov[mt][3])};
      *(u32x4*)(oL + ((mt * 16 + wave * 2 + hp) * 64 + lane) * 16) = o;
    }
    issue(4 + 2 * hp);
    issue(5 + 2 * hp);
  }
  f32x4 acc[2] = {zero, zero};
  const int fsw = (q >> 1) | ((g & 1) << 1);
  const int woff0 = (wn >> 1) * 8192 + (4 * g + q) * 128 + p * 8 + (((2 * (wn & 1)) ^ fsw) * 32);
  const int woff1 = (wn >> 1) * 8192 + (4 * g + q) * 128 + p * 8 + (((2 * (wn & 1) + 1) ^ fsw) * 32);
  auto rd = [&](int c, int ks) {
    const char* s = lds + c * 16384;
    Frag64 f;
    f.x = *(const s16x8*)(oL + (wm * 16 + c * 2 + ks) * 1024 + l16);
    f.w0 = cat(trread(s + woff0 + ks * 4096), trread(s + woff0 + ks * 4096 + 2048));
    f.w1 = cat(trread(s + woff1 + ks * 4096), trread(s + woff1 + ks * 4096 + 2048));
    return f;
  };
  auto mm = [&](const Frag64& f) {
    acc[0] = mfma32(f.w0, f.x, acc[0]);
    acc[1] = mfma32(f.w1, f.x, acc[1]);
  };
  Frag64 f1;
#pragma unroll
  for (int c = 0; c < 8; c += 2) {
    wait_vm_n<14>((6 - c) * 2);
    if (c == 0) wait_lgkm0();
    bar();
    STAMP(4 + c);
    Frag64 f0 = rd(c, 0);
    if (c > 0) mm(f1);
    f1 = rd(c, 1);
    mm(f0);
    f0 = rd(c + 1, 0);
    mm(f1);
    f1 = rd(c + 1, 1);
    mm(f0);
  }
  mm(f1);
  STAMP(2);
  pin(xr);
  u32x4 o = {pack2(unpk(xr[0]) + lo2(acc[0])), pack2(unpk(xr[1]) + hi2(acc[0])), pack2(unpk(xr[2]) + lo2(acc[1])),
             pack2(unpk(xr[3]) + hi2(acc[1]))};
  *(u32x4*)((char*)xo + (size_t)frag * 1024 + l16) = o;
  stat_emit(stats, rb * 2 + wm, cb * 4 + wn, o, lane);
  TSFLUSH;
}

__global__ __launch_bounds__(512, 1) void k3_w1(const u16* __restrict__ xp, const u16* __restrict__ w1,
                                                const float* __restrict__ cs, const float* __restrict__ stats,
                                                u16* __restrict__ hF PARG) {
  __shared__ __attribute__((aligned(16))) char lds[8 * 16384 + 8192 + 1024];
  TSDECL STAMP(0);
  const int tid = threadIdx.x, lane = tid & 63, wave = __builtin_amdgcn_readfirstlane(tid >> 6);
  const int xcd = blockIdx.x & 7, idx = blockIdx.x >> 3;
  const int rb = xcd * 4 + (idx & 3), cb = idx >> 2;
  const int wm = wave >> 1, wn = wave & 1, g = lane >> 4;
  const int col0 = cb * 64;
  const unsigned lb = lds_base(lds);
  const rsrc_t rx = mk(xp, 2048 * 1024), rw = mk(w1, 512 * 1024), rs = mk(stats, 2048 * 128),
               rc = mk(cs, 2 * CSN * 4);
  const unsigned l16 = lane * 16;
  dma(rs, l16, rb * 8192 + wave * 1024, lb + 131072 + wave * 1024);
  if (wave == 0) {
    unsigned vo = ((lane >> 4) * CSN + 1536 + col0 + (lane & 15) * 4) * 4;
    if (lane < 32) dma(rc, vo, 0, lb + 131072 + 8192);
  }
  const int kr = 8 * wave + (lane >> 3);
  const unsigned vb = (kr * 512 + col0 + ((lane & 7) ^ (((kr >> 1) & 3) << 1)) * 8) * 2;
  const unsigned sa = (rb * 4 + wm) * 16384 + wn * 1024;
  auto issue = [&](int c) {
    unsigned s = lb + c * 16384 + wave * 1024;
    dma(rx, l16, sa + c * 2048, s);
    dma(rw, vb, c * 65536, s + 8192);
  };
  float mean, sc;
  f32x4 csv[2];
  auto hook = [&]() {
    const char* ax = lds + 131072;
    f32x4 a = *(const f32x4*)(ax + wm * 2048 + l16), b = *(const f32x4*)(ax + wm * 2048 + 1024 + l16);
    float var;
    stat_fin(a, b, mean, var);
    float r1 = __builtin_amdgcn_rsqf(var + 1e-5f);
    float r2 = __builtin_amdgcn_rsqf(var * r1 * r1 + 1e-5f);
    sc = r1 * r2;
#pragma unroll
    for (int nt = 0; nt < 2; ++nt)
      csv[nt] = *(const f32x4*)(ax + 8192 + wn * 128 + nt * 64 + g * 16) +
                *(const f32x4*)(ax + 8192 + 256 + wn * 128 + nt * 64 + g * 16);
  };
  f32x4 acc[2] = {f32x4{0, 0, 0, 0}, f32x4{0, 0, 0, 0}};
  core64<8, 8, 4, false>(lds, issue, hook, acc, wave, lane TSA);
  STAMP(2);
  const f32x2 nm = bc2(-mean), s2 = bc2(sc);
  u32x4 o = {pack2(gelu2(s2 * fma2(nm, lo2(csv[0]), lo2(acc[0])))), pack2(gelu2(s2 * fma2(nm, hi2(csv[0]), hi2(acc[0])))),
             pack2(gelu2(s2 * fma2(nm, lo2(csv[1]), lo2(acc[1])))), pack2(gelu2(s2 * fma2(nm, hi2(csv[1]), hi2(acc[1]))))};
  *(u32x4*)((char*)hF + (size_t)((rb * 4 + wm + 1) * 16 + cb * 2 + wn) * 1024 + l16) = o;
  TSFLUSH;
}

__global__ __launch_bounds__(512, 1) void k4_w2(const u16* __restrict__ hF, const u16* __restrict__ w2,
                                                const u16* __restrict__ xp, const float* __restrict__ stats_in,
                                                u16* __restrict__ x1, float* __restrict__ stats_out PARG) {
  __shared__ __attribute__((aligned(16))) char lds[8 * 16384 + 8192];
  TSDECL STAMP(0);
  const int tid = threadIdx.x, lane = tid & 63, wave = __builtin_amdgcn_readfirstlane(tid >> 6);
  const int xcd = blockIdx.x & 7, idx = blockIdx.x >> 3;
  const int rb = xcd * 4 + (idx & 3), cb = idx >> 2;
  const int wm = wave >> 1, wn = wave & 1, g = lane >> 4, m = lane & 15;
  const int col0 = cb * 64;
  const unsigned lb = lds_base(lds);
  const rsrc_t rh = mk(hF, 130 * 16384), rw = mk(w2, 512 * 1024), rs = mk(stats_in, 2048 * 128),
               rx = mk(xp, 2048 * 1024);
  const unsigned l16 = lane * 16;
  dma(rs, l16, rb * 8192 + wave * 1024, lb + 131072 + wave * 1024);
  const int frag = (rb * 4 + wm) * 16 + cb * 2 + wn;
  u32x4 xr = aload16(rx, l16, frag * 1024);
  unsigned va[4];
#pragma unroll
  for (int i = 0; i < 4; ++i) {
    const int r = 16 + m + ((i == 0) ? -1 : (i == 1) ? 1 : (i == 2) ? -2 : 2);
    va[i] = (r >> 4) * 16384 + ((r & 15) + 16 * g) * 16;
  }
  const int kr = 8 * wave + (lane >> 3);
  const unsigned vb = (kr * 512 + col0 + ((lane & 7) ^ (((kr >> 1) & 3) << 1)) * 8) * 2;
  const unsigned sa = (rb * 4 + wm) * 16384 + wn * 1024;
  auto issue = [&](int c) {
    unsigned s = lb + c * 16384 + wave * 1024;
    dma(rh, va[c >> 1], sa + c * 2048, s);
    dma(rw, vb, c * 65536, s + 8192);
  };
  f32x2 xn[4];
  auto hook = [&]() {
    const char* ax = lds + 131072;
    f32x4 a = *(const f32x4*)(ax + wm * 2048 + l16), b = *(const f32x4*)(ax + wm * 2048 + 1024 + l16);
    pin(xr);
    float mean, var;
    stat_fin(a, b, mean, var);
    const f32x2 r1 = bc2(__builtin_amdgcn_rsqf(var + 1e-5f)), mm = bc2(mean);
#pragma unroll
    for (int i = 0; i < 4; ++i) xn[i] = (unpk(xr[i]) - mm) * r1;
  };
  f32x4 acc[2] = {f32x4{0, 0, 0, 0}, f32x4{0, 0, 0, 0}};
  core64<8, 8, 4, false>(lds, issue, hook, acc, wave, lane TSA);
  STAMP(2);
  u32x4 o = {pack2(xn[0] + lo2(acc[0])), pack2(xn[1] + hi2(acc[0])), pack2(xn[2] + lo2(acc[1])),
             pack2(xn[3] + hi2(acc[1]))};
  *(u32x4*)((char*)x1 + (size_t)frag * 1024 + l16) = o;
  stat_emit(stats_out, rb * 4 + wm, cb * 2 + wn, o, lane);
  TSFLUSH;
}

__global__ __launch_bounds__(512, 1) void k5_w3(const u16* __restrict__ x1, const u16* __restrict__ w3,
                                                const float* __restrict__ cs, const float* __restrict__ stats,
                                                u16* __restrict__ h2 PARG) {
  __shared__ __attribute__((aligned(16))) char lds[AUX_BIG + 16384 + 1024];
  TSDECL STAMP(0);
  const int tid = threadIdx.x, lane = tid & 63, wave = __builtin_amdgcn_readfirstlane(tid >> 6);
  const int xcd = blockIdx.x & 7, idx = blockIdx.x >> 3;
  const int rb = xcd * 2 + (idx & 1), cb = idx >> 1;
  const int wm = wave >> 1, wn = wave & 1, g = lane >> 4, m = lane & 15, q = m >> 2, p = lane & 3;
  const int col0 = cb * 96;
  const unsigned lb = lds_base(lds);
  const rsrc_t rx = mk(x1, 2048 * 1024), rw = mk(w3, 512 * F_ * 2), rs = mk(stats, 2048 * 128),
               rc = mk(cs, 2 * CSN * 4);
  const unsigned l16 = lane * 16;
  const bool full = (cb < 14);
  dma(rs, l16, (rb * 8 + wave) * 2048, lb + AUX_BIG + wave * 2048);
  dma(rs, l16, (rb * 8 + wave) * 2048 + 1024, lb + AUX_BIG + wave * 2048 + 1024);
  if (wave == 0) {
    const int i = lane / 24, e = lane % 24;
    unsigned vo = (i * CSN + 2048 + col0 + (full ? e : (e & 7)) * 4) * 4;
    if (lane < 48) dma(rc, vo, 0, lb + AUX_BIG + 16384);
  }
  const int bch = ((lane & 3) ^ (((lane >> 4) & 1) << 1)) * 8;
  const unsigned vb = ((16 * (wave & 3) + (lane >> 2)) * F_ + col0 + bch) * 2;
  const unsigned j1 = full ? 64 : 0, j2 = full ? 128 : 0;
  const unsigned sa = (rb * 8 + wave) * 16384;
  auto issue = [&](int c) {
    unsigned s = lb + (c % 5) * BIG_SLOT;
    dma(rx, l16, sa + c * 2048, s + wave * 2048);
    dma(rx, l16, sa + c * 2048 + 1024, s + wave * 2048 + 1024);
    if (wave < 4) {
      dma(rw, vb, c * 64 * F_ * 2, s + 16384 + wave * 1024);
      dma(rw, vb, c * 64 * F_ * 2 + j2, s + 16384 + 8192 + wave * 1024);
    } else {
      dma(rw, vb, c * 64 * F_ * 2 + j1, s + 16384 + 4096 + (wave - 4) * 1024);
    }
  };
  float mean[2], rstd[2];
  f32x4 csv[3];
  auto hook = [&]() {
    const char* ax = lds + AUX_BIG;
#pragma unroll
    for (int mt = 0; mt < 2; ++mt) {
      f32x4 a = *(const f32x4*)(ax + (2 * wm + mt) * 2048 + l16), b = *(const f32x4*)(ax + (2 * wm + mt) * 2048 + 1024 + l16);
      float var;
      stat_fin(a, b, mean[mt], var);
      rstd[mt] = __builtin_amdgcn_rsqf(var + 1e-5f);
    }
#pragma unroll
    for (int j = 0; j < 3; ++j) {
      const int o = (j < 2) ? wn * 128 + j * 64 + g * 16 : 256 + wn * 64 + g * 16;
      csv[j] = *(const f32x4*)(ax + 16384 + o) + *(const f32x4*)(ax + 16384 + 384 + o);
    }
  };
  f32x4 acc[2][3];
#pragma unroll
  for (int i = 0; i < 2; ++i)
#pragma unroll
    for (int j = 0; j < 3; ++j) acc[i][j] = f32x4{0, 0, 0, 0};
  const int wo_ = 16384 + (4 * g + q) * 64 + p * 8;
  const int woff[3] = {wo_ + wn * 4096 + (g & 1) * 32, wo_ + wn * 4096 + ((g & 1) ^ 1) * 32,
                       wo_ + 8192 + ((wn ^ (g & 1)) * 32)};
  core_big<8, 5, 4>(lds, issue, hook, acc, woff, wave, lane TSA);
  STAMP(2);
  if (full || wn == 0) {
#pragma unroll
    for (int mt = 0; mt < 2; ++mt) {
      const f32x2 nm = bc2(-mean[mt]), s2 = bc2(rstd[mt]);
      char* dst = (char*)h2 + ((size_t)(rb * 8 + 2 * wm + mt) * 43 + cb * 3) * 1024 + l16;
      u32x4 o = {pack2(gelu2(s2 * fma2(nm, lo2(csv[0]), lo2(acc[mt][0])))),
                 pack2(gelu2(s2 * fma2(nm, hi2(csv[0]), hi2(acc[mt][0])))),
                 pack2(gelu2(s2 * fma2(nm, lo2(csv[1]), lo2(acc[mt][1])))),
                 pack2(gelu2(s2 * fma2(nm, hi2(csv[1]), hi2(acc[mt][1]))))};
      *(u32x4*)(dst + wn * 1024) = o;
      if (full) {
        u32x2 o2 = {pack2(gelu2(s2 * fma2(nm, lo2(csv[2]), lo2(acc[mt][2])))),
                    pack2(gelu2(s2 * fma2(nm, hi2(csv[2]), hi2(acc[mt][2]))))};
        *(u32x2*)(dst + 2048 + wn * 8) = o2;
      }
    }
  }
  TSFLUSH;
}

template <bool LAST>
__global__ __launch_bounds__(512, 1) void k6_w4(const u16* __restrict__ h2, const u16* __restrict__ w4,
                                                const u16* __restrict__ xp, const u16* __restrict__ x1,
                                                u16* __restrict__ xo, float* __restrict__ stats_out PARG) {
  constexpr int RING = 9;
  __shared__ __attribute__((aligned(16))) char lds[RING * 16384];
  TSDECL STAMP(0);
  const int tid = threadIdx.x, lane = tid & 63, wave = __builtin_amdgcn_readfirstlane(tid >> 6);
  const int xcd = blockIdx.x & 7, idx = blockIdx.x >> 3;
  const int rb = xcd * 4 + (idx & 3), cb = idx >> 2;
  const int wm = wave >> 1, wn = wave & 1, g = lane >> 4, m = lane & 15;
  const int col0 = cb * 64;
  const unsigned lb = lds_base(lds);
  const rsrc_t rh = mk(h2, 128 * 43 * 1024), rw = mk(w4, F_ * 1024), rx = mk(xp, 2048 * 1024),
               ry = mk(x1, 2048 * 1024);
  const unsigned l16 = lane * 16;
  const int frag = (rb * 4 + wm) * 16 + cb * 2 + wn;
  u32x4 xr = aload16(rx, l16, frag * 1024);
  u32x4 yr = aload16(ry, l16, frag * 1024);
  const int kr = 8 * wave + (lane >> 3);
  const unsigned vb = (kr * 512 + col0 + ((lane & 7) ^ (((kr >> 1) & 3) << 1)) * 8) * 2;
  const unsigned sa = (rb * 4 + wm) * 43 * 1024;
  auto issue = [&](int c) {
    unsigned s = lb + (c % RING) * 16384 + wave * 1024;
    dma(rh, l16, sa + c * 2048 + ((c == 21) ? 0 : wn * 1024), s);
    dma(rw, vb, (c == 21 && wave >= 4) ? c * 65536 - 32768 : c * 65536, s + 8192);
  };
  f32x2 xy[4];
  auto hook = [&]() {
    pin(xr); pin(yr);
#pragma unroll
    for (int i = 0; i < 4; ++i) xy[i] = unpk(xr[i]) + unpk(yr[i]);
  };
  f32x4 acc[2] = {f32x4{0, 0, 0, 0}, f32x4{0, 0, 0, 0}};
  core64<22, RING, 6, true>(lds, issue, hook, acc, wave, lane TSA);
  STAMP(2);
  u32x4 o = {pack2(xy[0] + lo2(acc[0])), pack2(xy[1] + hi2(acc[0])), pack2(xy[2] + lo2(acc[1])),
             pack2(xy[3] + hi2(acc[1]))};
  if (LAST) {
    *(u32x4*)(xo + (size_t)(rb * 64 + 16 * wm + m) * 512 + col0 + 32 * wn + swz_col(g)) = swz8(o);
  } else {
    *(u32x4*)((char*)xo + (size_t)frag * 1024 + l16) = o;
    stat_emit(stats_out, rb * 4 + wm, cb * 2 + wn, o, lane);
  }
  TSFLUSH;
}

extern "C" void kernel_launch(void* const* d_in, const int* in_sizes, int n_in, void* d_out, int out_size,
                              void* d_ws, size_t ws_size, hipStream_t stream) {
  const u16* A_x = (const u16*)d_in[0];
  const u16* wqkv = (const u16*)d_in[1];
  const u16* wphi = (const u16*)d_in[2];
  const u16* wo = (const u16*)d_in[3];
  const u16* w1 = (const u16*)d_in[5];
  const u16* w2 = (const u16*)d_in[6];
  const u16* w3 = (const u16*)d_in[7];
  const u16* w4 = (const u16*)d_in[8];
  char* ws = (char*)d_ws;
  size_t off = 0;
  auto take = [&](size_t bytes) { char* p = ws + off; off += (bytes + 255) & ~(size_t)255; return p; };
  float* kvacc = (float*)take(2 * 32 * 544 * 4);
  float* cs = (float*)take(2 * CSN * 4);
  float* st_x = (float*)take(2048 * 32 * 4);
  float* st_xp = (float*)take(2048 * 32 * 4);
  float* st_x1 = (float*)take(2048 * 32 * 4);
  u16* phiq = (u16*)take((size_t)2048 * 1024 * 2);
  u16* x0 = (u16*)take((size_t)2048 * 512 * 2);
  u16* xp = (u16*)take((size_t)2048 * 512 * 2);
  u16* hF = (u16*)take((size_t)130 * 16384);
  u16* x1 = (u16*)take((size_t)2048 * 512 * 2);
  u16* h2 = (u16*)take((size_t)128 * 43 * 1024);
  u16* x2 = (u16*)take((size_t)2048 * 512 * 2);
  if (off > ws_size) return;

  k0_prep<<<236, 512, 0, stream>>>(A_x, wqkv, w1, w3, kvacc, hF, cs, st_x, x0 PPASS(0));
  for (int it = 0; it < 2; ++it) {
    const u16* xin = it == 0 ? x0 : x2;
    float* kva = kvacc + it * 32 * 544;
    k1_qkv<<<256, 512, 0, stream>>>(xin, wqkv, wphi, cs, st_x, phiq, kva PPASS(1 + 6 * it));
    k2_wo<<<256, 512, 0, stream>>>(xin, phiq, kva, wo, xp, st_xp PPASS(2 + 6 * it));
    k3_w1<<<256, 512, 0, stream>>>(xp, w1, cs, st_xp, hF PPASS(3 + 6 * it));
    k4_w2<<<256, 512, 0, stream>>>(hF, w2, xp, st_xp, x1, st_x1 PPASS(4 + 6 * it));
    k5_w3<<<240, 512, 0, stream>>>(x1, w3, cs, st_x1, h2 PPASS(5 + 6 * it));
    if (it == 0) k6_w4<false><<<256, 512, 0, stream>>>(h2, w4, xp, x1, x2, st_x PPASS(6 + 6 * it));
    else k6_w4<true><<<256, 512, 0, stream>>>(h2, w4, xp, x1, (u16*)d_out, st_x PPASS(6 + 6 * it));
  }
}
```

```cpp
#include <hip/hip_runtime.h>
#include <hip/hip_bf16.h>
#include <stdint.h>
#include <type_traits>

typedef unsigned short u16;
typedef short s16x4 __attribute__((ext_vector_type(4)));
typedef short s16x8 __attribute__((ext_vector_type(8)));
typedef __bf16 bf16x8 __attribute__((ext_vector_type(8)));
typedef __bf16 bf16x2 __attribute__((ext_vector_type(2)));
typedef float f32x4 __attribute__((ext_vector_type(4)));
typedef float f32x2 __attribute__((ext_vector_type(2)));
typedef unsigned u32x4 __attribute__((ext_vector_type(4)));
typedef unsigned u32x2 __attribute__((ext_vector_type(2)));
typedef __amdgpu_buffer_rsrc_t rsrc_t;
#define LAS __attribute__((address_space(3)))

constexpr int F_ = 1376;

#define DEVI __device__ __forceinline__

DEVI unsigned pack2(float a, float b) {
  bf16x2 v = {(__bf16)a, (__bf16)b};
  return __builtin_bit_cast(unsigned, v);
}
DEVI unsigned pack2(f32x2 a) { return pack2(a[0], a[1]); }
DEVI float bflo(unsigned u) { return __uint_as_float(u << 16); }
DEVI float bfhi(unsigned u) { return __uint_as_float(u & 0xffff0000u); }
DEVI f32x2 unpk(unsigned u) { return f32x2{bflo(u), bfhi(u)}; }

DEVI void wgbar() { asm volatile("s_waitcnt lgkmcnt(0)\n\ts_barrier" ::: "memory"); }
DEVI void cfence() { asm volatile("" ::: "memory"); }
DEVI void fence() { __builtin_amdgcn_sched_barrier(0); }
DEVI rsrc_t mk(const void* p, unsigned n) { return __builtin_amdgcn_make_buffer_rsrc((void*)p, 0, n, 0x00020000); }
template <int I, int N, class F> DEVI void sfor(F f) {
  if constexpr (I < N) { f(std::integral_constant<int, I>{}); sfor<I + 1, N>(f); }
}
DEVI unsigned opq(unsigned s) { asm volatile("" : "+s"(s)); return s; }
DEVI unsigned uni(unsigned s) { return __builtin_amdgcn_readfirstlane(s); }
DEVI u32x4 ldw(rsrc_t r, unsigned voff, unsigned soff) { return __builtin_amdgcn_raw_buffer_load_b128(r, voff, soff, 0); }
DEVI u32x4 ldc(rsrc_t r, unsigned voff, unsigned soff) { return __builtin_amdgcn_raw_buffer_load_b128(r, voff, soff, 16); }
DEVI unsigned ldc1(rsrc_t r, unsigned voff, unsigned soff) { return __builtin_amdgcn_raw_buffer_load_b32(r, voff, soff, 16); }
DEVI void hold(u32x4 v) { asm volatile("s_nop 1" ::"v"(v) : "memory"); }
DEVI void stc(u32x4 v, rsrc_t r, unsigned voff, unsigned soff) { __builtin_amdgcn_raw_buffer_store_b128(v, r, voff, soff, 16); hold(v); }
DEVI void sta(u32x4 v, rsrc_t r, unsigned voff, unsigned soff) { __builtin_amdgcn_raw_buffer_store_b128(v, r, voff, soff, 16); }
DEVI void stc(u32x2 v, rsrc_t r, unsigned voff, unsigned soff) { __builtin_amdgcn_raw_buffer_store_b64(v, r, voff, soff, 16); }
DEVI unsigned opv(unsigned v) { asm volatile("" : "+v"(v)); return v; }
template <bool RE, int OFF> DEVI void ldg(u32x4& a, rsrc_t r, unsigned voff, unsigned soff) {
  if constexpr (RE)
    asm volatile("buffer_load_dwordx4 %0, %1, %2, %3 offen offset:%4 sc1" : "+v"(a) : "v"(voff), "s"(r), "s"(soff), "i"(OFF) : "memory");
  else a = ldc(r, voff + OFF, soff);
}
template <bool RE, int OFF> DEVI void ldg(unsigned& a, rsrc_t r, unsigned voff, unsigned soff) {
  if constexpr (RE)
    asm volatile("buffer_load_dword %0, %1, %2, %3 offen offset:%4 sc1" : "+v"(a) : "v"(voff), "s"(r), "s"(soff), "i"(OFF) : "memory");
  else a = ldc1(r, voff + OFF, soff);
}
DEVI void pa_(u32x4& v) { asm("" : "+a"(v)); }
DEVI void pv_(u32x4& v) { asm("" : "+v"(v)); }
template <class T> DEVI void pin(T& v) { asm volatile("" : "+v"(v)); }
DEVI void drain() { asm volatile("s_waitcnt vmcnt(0)" ::: "memory"); }
DEVI unsigned san(unsigned u) { return u < 0xfffffffeu ? u : 0xfffffffeu; }
DEVI u32x4 san(u32x4 v) { return u32x4{san(v[0]), san(v[1]), san(v[2]), san(v[3])}; }
DEVI u32x2 san(u32x2 v) { return u32x2{san(v[0]), san(v[1])}; }
DEVI unsigned umax(unsigned a, unsigned b) { return a > b ? a : b; }
DEVI unsigned mx4(unsigned m, u32x4 v) { return umax(umax(umax(m, v[0]), v[1]), umax(v[2], v[3])); }
DEVI bool any_empty(unsigned m) { return __builtin_amdgcn_ballot_w64(m == 0xffffffffu) != 0; }
DEVI f32x4 asf(u32x4 v) { return __builtin_bit_cast(f32x4, v); }
DEVI u32x4 asu(f32x4 v) { return __builtin_bit_cast(u32x4, v); }

DEVI f32x4 mfma32(u32x4 a, u32x4 b, f32x4 c) {
  return __builtin_amdgcn_mfma_f32_16x16x32_bf16(__builtin_bit_cast(bf16x8, a), __builtin_bit_cast(bf16x8, b), c, 0, 0, 0);
}
DEVI f32x4 mfma16(u32x2 a, u32x2 b, f32x4 c) {
  return __builtin_amdgcn_mfma_f32_16x16x16bf16_1k(__builtin_bit_cast(s16x4, a), __builtin_bit_cast(s16x4, b), c, 0, 0, 0);
}
DEVI f32x2 lo2(f32x4 v) { return f32x2{v[0], v[1]}; }
DEVI f32x2 hi2(f32x4 v) { return f32x2{v[2], v[3]}; }
DEVI f32x4 cat4(f32x2 a, f32x2 b) { return f32x4{a[0], a[1], b[0], b[1]}; }
DEVI u32x4 cat4(u32x2 a, u32x2 b) { return u32x4{a[0], a[1], b[0], b[1]}; }
DEVI u32x2 pk4(f32x4 v) { return u32x2{pack2(lo2(v)), pack2(hi2(v))}; }
DEVI f32x2 fma2(f32x2 a, f32x2 b, f32x2 c) { return __builtin_elementwise_fma(a, b, c); }
DEVI f32x2 bc2(float a) { return f32x2{a, a}; }
DEVI f32x2 gelu2(f32x2 x) {
  f32x2 t = fma2(x * x, bc2(-0.10294324f), bc2(-2.30220819f));
  t = x * t;
  f32x2 e = {__builtin_amdgcn_exp2f(t[0]), __builtin_amdgcn_exp2f(t[1])};
  e = e + bc2(1.0f);
  f32x2 r = {__builtin_amdgcn_rcpf(e[0]), __builtin_amdgcn_rcpf(e[1])};
  return x * r;
}
DEVI f32x4 lnf(f32x4 a, f32x4 cs, float r, float nrm) {
  return cat4(fma2(lo2(cs), bc2(nrm), lo2(a) * bc2(r)), fma2(hi2(cs), bc2(nrm), hi2(a) * bc2(r)));
}
DEVI u32x2 gelu4(f32x4 v) { return u32x2{pack2(gelu2(lo2(v))), pack2(gelu2(hi2(v)))}; }
DEVI void sw16(float& a, float& b) {
  auto r = __builtin_amdgcn_permlane16_swap(__float_as_uint(a), __float_as_uint(b), false, false);
  a = __uint_as_float(r[0]); b = __uint_as_float(r[1]);
}
DEVI void sw32(float& a, float& b) {
  auto r = __builtin_amdgcn_permlane32_swap(__float_as_uint(a), __float_as_uint(b), false, false);
  a = __uint_as_float(r[0]); b = __uint_as_float(r[1]);
}
DEVI float rsum_g(float v) {
  float a = v, b = v;
  sw16(a, b);
  a = a + b; b = a;
  sw32(a, b);
  return a + b;
}
template <bool MAX> DEVI float red4(float a, float b, float c, float d) {
  sw16(a, b); sw16(c, d);
  float e = MAX ? fmaxf(a, b) : a + b, f = MAX ? fmaxf(c, d) : c + d;
  sw32(e, f);
  return MAX ? fmaxf(e, f) : e + f;
}
DEVI void bcast4(float h, float& a, float& b, float& c, float& d) {
  float h1 = h, h2 = h;
  sw16(h1, h2);
  a = h1; c = h1; b = h2; d = h2;
  sw32(a, c); sw32(b, d);
}
template <int C> DEVI float dppf(float v) {
  return __uint_as_float(__builtin_amdgcn_update_dpp(0u, __float_as_uint(v), C, 0xf, 0xf, false));
}
DEVI u32x2 stat_part(u32x4 o) {
  f32x2 v0 = unpk(o[0]), v1 = unpk(o[1]), v2 = unpk(o[2]), v3 = unpk(o[3]);
  f32x2 s2 = (v0 + v1) + (v2 + v3);
  float mean = rsum_g(s2[0] + s2[1]) * (1.0f / 32.0f);
  f32x2 mm = bc2(mean);
  v0 -= mm; v1 -= mm; v2 -= mm; v3 -= mm;
  f32x2 q2 = fma2(v1, v1, v0 * v0) + fma2(v3, v3, v2 * v2);
  float q = rsum_g(q2[0] + q2[1]);
  return san(u32x2{__float_as_uint(mean), __float_as_uint(q)});
}
DEVI f32x2 stat_half(f32x4 a) {
  float mh = rsum_g(a[0] + a[2]) * 0.125f;
  float d0 = a[0] - mh, d1 = a[2] - mh;
  float q = rsum_g((a[1] + a[3]) + 32.0f * (d0 * d0 + d1 * d1));
  return f32x2{mh, q};
}
DEVI void stat_fin(f32x4 a, float& mean, float& var) {
  mean = 0.5f * (a[0] + a[2]);
  float d = a[0] - a[2];
  var = ((a[1] + a[3]) + 128.0f * d * d) * (1.0f / 512.0f);
}

DEVI void dual_softmax2(f32x4 p0, f32x4 p1, u32x4& o0, u32x4& o1) {
  const float c = 1.44269504f;
  float mx0 = fmaxf(fmaxf(p0[0], p0[1]), fmaxf(p0[2], p0[3]));
  float nm0 = -fminf(fminf(p0[0], p0[1]), fminf(p0[2], p0[3]));
  float mx1 = fmaxf(fmaxf(p1[0], p1[1]), fmaxf(p1[2], p1[3]));
  float nm1 = -fminf(fminf(p1[0], p1[1]), fminf(p1[2], p1[3]));
  float h = red4<true>(mx0, nm0, mx1, nm1) * c;
  bcast4(h, mx0, nm0, mx1, nm1);
  float ep0[4], em0[4], ep1[4], em1[4];
#pragma unroll
  for (int r = 0; r < 4; ++r) {
    ep0[r] = __builtin_amdgcn_exp2f(__builtin_fmaf(p0[r], c, -mx0));
    em0[r] = __builtin_amdgcn_exp2f(__builtin_fmaf(p0[r], -c, -nm0));
    ep1[r] = __builtin_amdgcn_exp2f(__builtin_fmaf(p1[r], c, -mx1));
    em1[r] = __builtin_amdgcn_exp2f(__builtin_fmaf(p1[r], -c, -nm1));
  }
  float s0 = (ep0[0] + ep0[1]) + (ep0[2] + ep0[3]), s1 = (em0[0] + em0[1]) + (em0[2] + em0[3]);
  float s2 = (ep1[0] + ep1[1]) + (ep1[2] + ep1[3]), s3 = (em1[0] + em1[1]) + (em1[2] + em1[3]);
  h = __builtin_amdgcn_rcpf(red4<false>(s0, s1, s2, s3));
  bcast4(h, s0, s1, s2, s3);
  o0 = u32x4{pack2(f32x2{ep0[0], ep0[1]} * bc2(s0)), pack2(f32x2{ep0[2], ep0[3]} * bc2(s0)),
             pack2(f32x2{em0[0], em0[1]} * bc2(s1)), pack2(f32x2{em0[2], em0[3]} * bc2(s1))};
  o1 = u32x4{pack2(f32x2{ep1[0], ep1[1]} * bc2(s2)), pack2(f32x2{ep1[2], ep1[3]} * bc2(s2)),
             pack2(f32x2{em1[0], em1[1]} * bc2(s3)), pack2(f32x2{em1[2], em1[3]} * bc2(s3))};
}

constexpr unsigned MB = 1u << 20;
constexpr int NF = 188;
constexpr int I_KV = 0, I_Q = 32, I_WO = 48, I_W1 = 64, I_W2 = 80, I_W3 = 96, I_W4 = 144;
constexpr unsigned O_WT = 0, O_CS = 6 * MB, O_WP = 6 * MB + 0x8000, O_L = 7 * MB, LSZ = 21 * MB;
constexpr unsigned L_X = 0, L_O = 2 * MB, L_XP = 4 * MB, L_X1 = 6 * MB, L_H = 8 * MB, L_H2 = 11 * MB, L_P = 17 * MB,
                   L_R = 19 * MB + 0x40000, L_STX = 19 * MB + 0x80000, L_STP = 19 * MB + 0xC0000, L_ST1 = 20 * MB;
constexpr unsigned PSZ = 8704, RSZ = 4352, H2T = 43 * 1024;
constexpr unsigned WS_BYTES = O_L + 2 * LSZ;
constexpr unsigned OOB = 0x80000000u;

__global__ __launch_bounds__(512) void k0_prep(const u16* __restrict__ x, const u16* __restrict__ wqkv,
                                               const u16* __restrict__ wphi, const u16* __restrict__ wo,
                                               const u16* __restrict__ w1, const u16* __restrict__ w2,
                                               const u16* __restrict__ w3, const u16* __restrict__ w4,
                                               char* __restrict__ ws) {
  __shared__ float red[512];
  const int b = blockIdx.x, tid = threadIdx.x, lane = tid & 63, wave = tid >> 6, m = lane & 15, g = lane >> 4;
  if (b < 94) {
    const u16* W; int ld, c0, mat, ks0 = 2 * wave, nks = 16, oc = -1;
    if (b < 24) { W = wqkv; ld = 1536; c0 = b * 64; mat = 0; oc = c0; }
    else if (b < 32) { W = w1; ld = 512; c0 = (b - 24) * 64; mat = 2; oc = 1536 + c0; }
    else if (b < 54) { W = w3; ld = F_; c0 = (b - 32) * 64; mat = 4; oc = 2048 + c0; }
    else if (b < 62) { W = wo; ld = 512; c0 = (b - 54) * 64; mat = 1; }
    else if (b < 70) { W = w2; ld = 512; c0 = (b - 62) * 64; mat = 3; }
    else { W = w4; ld = 512; c0 = ((b - 70) & 7) * 64; mat = 5; ks0 += ((b - 70) >> 3) * 16; nks = 43; }
    const int col = c0 + 4 * m;
    const bool cok = col < ld;
    const int cc = cok ? col : 0;
    u32x2 v[2][8];
#pragma unroll
    for (int h = 0; h < 2; ++h) {
      const int ks = (ks0 + h < nks) ? ks0 + h : 0;
#pragma unroll
      for (int i = 0; i < 8; ++i) {
        const int r = 32 * ks + (mat == 1 ? 16 * (i >> 2) + 4 * g + (i & 3) : 8 * g + i);
        v[h][i] = *(const u32x2*)(W + (size_t)r * ld + cc);
      }
    }
#pragma unroll
    for (int h = 0; h < 2; ++h) {
      const int ks = ks0 + h;
      int j, w, idx, n0;
      const int U = col >> 5, c4 = (col & 31) >> 2, t = c4 & 1;
      if (mat == 0) {
        const int sec = col >> 9, hd = (col & 511) >> 4;
        j = hd >> 2; w = hd & 3; n0 = col & 15;
        const int p = (ks - 4 * w) & 15;
        idx = sec == 0 ? I_Q + p : I_KV + 2 * p + (sec - 1);
      } else if (mat == 4) {
        n0 = 4 * (c4 >> 1);
        int lu;
        if (U < 18) { j = U / 6; lu = U - 6 * j; } else { j = 3 + (U - 18) / 5; lu = (U - 18) % 5; }
        int slot;
        if (lu < 4) { w = lu; slot = t; } else { w = 2 * (lu - 4) + t; slot = 2; }
        idx = I_W3 + 3 * ((ks - 4 * w) & 15) + slot;
      } else {
        n0 = 4 * (c4 >> 1);
        j = U >> 1;
        int p;
        if (mat == 5) { w = ks / 11; p = ks - 11 * w; idx = I_W4; }
        else { w = ks >> 2; p = ks & 3; idx = I_WO + 16 * (mat - 1); }
        idx += 4 * p + 2 * ((U ^ w) & 1) + t;
      }
      if (cok && ks < nks) {
        char* d = ws + O_WT + (size_t)((j * 4 + w) * NF + idx) * 1024 + (n0 + 16 * g) * 16;
#pragma unroll
        for (int e = 0; e < 4; ++e) {
          u32x4 o;
#pragma unroll
          for (int q = 0; q < 4; ++q)
            o[q] = __builtin_amdgcn_perm(v[h][2 * q + 1][e >> 1], v[h][2 * q][e >> 1], (e & 1) ? 0x07060302u : 0x05040100u);
          *(u32x4*)(d + e * 16) = o;
        }
      }
    }
    if (oc >= 0) {
      f32x2 a0 = {0.f, 0.f}, a1 = {0.f, 0.f};
#pragma unroll
      for (int h = 0; h < 2; ++h)
#pragma unroll
        for (int i = 0; i < 8; ++i) { a0 += unpk(v[h][i][0]); a1 += unpk(v[h][i][1]); }
      const float s = red4<false>(a0[0], a0[1], a1[0], a1[1]);
      red[wave * 64 + 4 * m + g] = cok ? s : 0.f;
      __syncthreads();
      if (tid < 64) {
        float t = 0.f;
#pragma unroll
        for (int w = 0; w < 8; ++w) t += red[w * 64 + tid];
        ((float*)(ws + O_CS))[oc + tid] = t;
      }
    }
  } else if (b < 158) {
    const int RT = 2 * (b - 94) + (wave >> 2), k0 = 4 * (wave & 3);
    u32x4 xv[4];
#pragma unroll
    for (int i = 0; i < 4; ++i) xv[i] = *(const u32x4*)(x + (size_t)(RT * 16 + m) * 512 + (k0 + i) * 32 + g * 8);
#pragma unroll
    for (int i = 0; i < 4; ++i) {
      const int ks = k0 + i;
      *(u32x4*)(ws + O_L + L_X + RT * 16384 + ks * 1024 + lane * 16) = san(xv[i]);
      const u32x2 st = stat_part(xv[i]);
      if (g == 0) *(u32x2*)(ws + O_L + L_STX + RT * 2048 + (ks >> 1) * 256 + m * 16 + (ks & 1) * 8) = st;
    }
  } else if (b == 158) {
#pragma unroll
    for (int q = 0; q < 4; ++q) {
      const int h = wave * 4 + q;
      const u16* p = wphi + h * 256 + g * 64 + m;
      const unsigned lo = p[0] | ((unsigned)p[16] << 16), hi = p[32] | ((unsigned)p[48] << 16);
      *(u32x2*)(ws + O_WP + h * 512 + lane * 8) = u32x2{lo, hi};
    }
  } else {
    const u32x4 e = {~0u, ~0u, ~0u, ~0u}, z = {0u, 0u, 0u, 0u};
    constexpr unsigned N1 = 2 * MB / 16, N2 = N1 + (256 * PSZ) / 16, N3 = N2 + (8 * RSZ) / 16, N4 = N3 + 4 * 1024;
    for (unsigned i = (b - 159) * 512 + tid; i < N4; i += 97 * 512) {
      unsigned o;
      if (i < N1) o = L_O + i * 16;
      else if (i < N2) o = L_P + (i - N1) * 16;
      else if (i < N3) o = L_R + (i - N2) * 16;
      else { const unsigned q = i - N3; o = (q >> 11) * LSZ + L_H + ((q >> 10) & 1) * 129 * 16384 + (q & 1023) * 16; }
      *(u32x4*)(ws + O_L + o) = i < N3 ? e : z;
    }
  }
}

#define STAMP(i)
#define RETRY(i)
#define IC(n) std::integral_constant<int, n>{}
#define T_ std::true_type{}
#define F_T std::false_type{}
#define AWAIT(ID, ISSUE, PINS, MAXEXPR)                                                          \
  if (__builtin_expect(any_empty(MAXEXPR), 0)) {                                                 \
    do { RETRY(ID); ISSUE; drain(); PINS; } while (any_empty(MAXEXPR) && --budget > 0);          \
  }

constexpr unsigned XS = 0, RED = 65536, STL = RED + 49152, CST = STL + 1024, LDS_BYTES = CST + 2048;

template <int N> DEVI unsigned mxa(const u32x4 (&a)[N], unsigned m) {
  sfor<0, N>([&](auto i) { m = mx4(m, a[i]); });
  return m;
}

template <int A, int B, int N> DEVI void cla(u32x4 (&a)[N]) { sfor<A, B>([&](auto i) { pa_(a[i]); }); }
template <int A, int B, int N> DEVI void clv(u32x4 (&a)[N]) { sfor<A, B>([&](auto i) { pv_(a[i]); }); }
template <int N> DEVI void pina(u32x4 (&a)[N]) { sfor<0, N>([&](auto i) { pin(a[i]); }); }

__global__ __launch_bounds__(256, 1) void kp_main(char* __restrict__ ws, u16* __restrict__ out) {
  __shared__ __attribute__((aligned(256))) char lds_[LDS_BYTES];
  LAS char* const lds = (LAS char*)lds_;
  const unsigned tid = threadIdx.x, lane = tid & 63;
  const unsigned w = uni(tid >> 6), wr = w >> 1, wu = w & 1;
  const unsigned bid = blockIdx.x, idx = bid >> 3, c = ((bid & 7) >> 1) * 8 + (idx >> 2), j = (bid & 1) * 4 + (idx & 3);
  const rsrc_t R = mk(ws, WS_BYTES);
  const unsigned l16_ = lane * 16, m16_ = (lane & 15) * 16, g16_ = (lane >> 4) * 16;
  const unsigned g0m = lane < 16 ? l16_ : OOB;
#define l16 opv(l16_)
#define m16 opv(m16_)
#define g16 opv(g16_)
  const unsigned wb = O_WT + (j * 4 + w) * NF * 1024;
  const unsigned us = j < 3 ? 6 * j : 3 + 5 * j;
  const unsigned no3 = j < 3 || w < 2 ? 0 : OOB;
  const unsigned noe = w < 3 ? 0 : OOB;
#define l16c (l16 | no3)
#define l16e (l16 | noe)
  int budget = 1 << 16;
  const f32x4 zero = {0.f, 0.f, 0.f, 0.f};

  auto ldW = [&](auto& dst, auto base, auto i0, auto n, rsrc_t r) {
    fence();
    const unsigned wbo = opq(wb), va = l16, vc = l16c, ve = l16e;
    sfor<0, decltype(n)::value>([&](auto i) {
      constexpr int k = decltype(i0)::value + i, f = decltype(base)::value + k;
      constexpr bool third = decltype(base)::value == I_W3 && k % 3 == 2, last = decltype(base)::value == I_W4 && k >= 40;
      dst[k] = ldw(r, (third ? vc : last ? ve : va) + (f & 3) * 1024, wbo + (f >> 2) * 4096);
    });
    fence();
  };
  auto gat8 = [&](auto RE, u32x4 (&ga)[8], rsrc_t r, unsigned voff, unsigned so0, unsigned so1) {
    fence();
    sfor<0, 4>([&](auto p) { ldg<RE, p * 1024>(ga[p], r, voff, so0); });
    sfor<0, 4>([&](auto p) { ldg<RE, p * 1024>(ga[4 + p], r, voff, so1); });
    fence();
  };
  unsigned ga_ = O_L + c * 32768 + w * 4096;
  unsigned gs_ = O_L + (2 * c + wr) * 2048 + wu * 1024;
  unsigned pub_ = O_L + (2 * c + wr) * 16384 + (2 * j + wu) * 1024;
  unsigned pst_ = O_L + (2 * c + wr) * 2048 + j * 256 + wu * 8;
  unsigned h2_ = O_L + L_H2 + 2 * c * H2T;
  unsigned pj_ = O_L + L_P + j * 32 * PSZ, rj_ = O_L + L_R + j * RSZ;
#define GA opq(ga_)
#define GS opq(gs_)
#define PUB opq(pub_)
#define PST opq(pst_)
#define H2 opq(h2_)
#define PJ opq(pj_)
#define RJ opq(rj_)
#define UW opq(w)
#define UC opq(c)
  constexpr unsigned S1X = 64 * 16384, S1S = 64 * 2048, S1H = 64 * H2T;
  auto p_o = [&]() { return PJ + UC * PSZ; };
  auto r_o = [&]() { const unsigned cc = UC; return RJ + (cc >> 3) * 1024 + ((16 * cc + 4 * UW) & 63) * 16 + ((cc >> 2) & 1) * 8; };
  auto rz_o = [&]() { const unsigned cc = UC; return RJ + 4096 + (cc >> 3) * 64 + (cc & 3) * 16 + ((cc >> 2) & 1) * 8; };
  auto vr = [&]() { const unsigned v = l16; return v < 64 ? v : OOB; };
  auto vrz = [&]() { return opv(tid) == 0 ? 0 : OOB; };
  auto ones4 = [&]() { const unsigned o = opv(~0u); return u32x4{o, o, o, o}; };
  auto ones2 = [&]() { const unsigned o = opv(~0u); return u32x2{o, o}; };

  u32x4 wkv[32], gx[8], gxs, xres[2];
  gat8(F_T, gx, R, l16, GA + L_X, GA + L_X + 16384);
  gxs = ldc(R, l16, GS + L_STX);
  ldW(wkv, IC(I_KV), IC(0), IC(32), R);
  xres[0] = ldw(R, l16, PUB + L_X);
  xres[1] = ldw(R, l16, PUB + L_X + S1X);
  const u32x2 wphi = __builtin_amdgcn_raw_buffer_load_b64(R, lane * 8, O_WP + (4 * j + w) * 512, 0);
  {
    const unsigned t = tid;
    if (t < 112) {
      const unsigned sec = t >> 4, o = sec < 4 ? sec * 512 + 64 * j + (t & 15) * 4 : 2048 + us * 32 + (t - 64) * 4;
      *(LAS u32x4*)(lds + CST + t * 16) = ldw(R, o * 4, O_CS);
    }
  }
  fence();
  {
    const u32x4 o4 = ones4();
    const u32x2 o2 = {o4[0], o4[1]};
    sta(o4, R, l16, PUB + L_H + 16384);
    sta(o4, R, l16, PUB + L_H + 16384 + S1X);
    sta(o4, R, l16, p_o() + LSZ + w * 2048);
    sta(o4, R, l16 + 1024, p_o() + LSZ + w * 2048);
    sta(o4, R, lane < 8 ? l16 : OOB, p_o() + LSZ + 8192 + w * 128);
    stc(o2, R, vr(), r_o() + LSZ);
    stc(o2, R, vrz(), rz_o() + LSZ);
    hold(o4);
  }
  fence();

  for (int lay = 0; lay < 2; ++lay) {
    const rsrc_t Rn = mk(ws, lay == 0 ? WS_BYTES : 0);
    u32x4 wq[16], wo[16], w1[16], w2[16], w3[48], w4[44];
    STAMP(0);

    auto xbase = [&](int q) { return l16 + (((UW + q) & 3) * 4096 + XS); };
    auto stl_w = [&](f32x2 sh, int s) {
      if (lane < 16) *(LAS f32x2*)(lds + (m16 + ((UW >> 1) * 256 + (UW & 1) * 8)) + (STL + s * 512)) = sh;
    };
    auto stage = [&](auto S, const u32x4 (&ga)[8], u32x4 gs) {
      constexpr int s = decltype(S)::value;
      const unsigned x0 = xbase(0);
      sfor<0, 8>([&](auto i) { *(LAS u32x4*)(lds + x0 + s * 32768 + (i >> 2) * 16384 + (i & 3) * 1024) = ga[i]; });
      stl_w(stat_half(asf(gs)), s);
    };
    auto xs_rd = [&](const unsigned (&xb)[4], auto S, auto RTI, auto P) {
      constexpr int s = decltype(S)::value, rt = decltype(RTI)::value, p = decltype(P)::value;
      return *(LAS u32x4*)(lds + xb[p >> 2] + s * 32768 + rt * 16384 + (p & 3) * 1024);
    };
    auto cs_rd = [&](unsigned a, int o) { return *(LAS f32x4*)(lds + a + (CST + o * 4)); };

    float qr[2][2], qn[2][2];
    f32x4 kvp = zero, kvm = zero, zp = zero, zm = zero;
    auto step_kv = [&](auto S, const u32x4 (&ga)[8], u32x4 gs, auto hookA) {
      constexpr int s = decltype(S)::value;
      stage(S, ga, gs);
      f32x4 ak[2] = {zero, zero}, av[2] = {zero, zero};
      sfor<0, 4>([&](auto p) {
        sfor<0, 2>([&](auto rt) {
          ak[rt] = mfma32(wkv[2 * p], ga[rt * 4 + p], ak[rt]);
          av[rt] = mfma32(wkv[2 * p + 1], ga[rt * 4 + p], av[rt]);
        });
      });
      const unsigned xb[4] = {0, xbase(1), xbase(2), xbase(3)};
      wgbar();
      hookA();
      sfor<4, 16>([&](auto p) {
        sfor<0, 2>([&](auto rt) {
          const u32x4 a = xs_rd(xb, S, rt, p);
          ak[rt] = mfma32(wkv[2 * p], a, ak[rt]);
          av[rt] = mfma32(wkv[2 * p + 1], a, av[rt]);
        });
      });
      const unsigned ca = g16 + UW * 64, sa = m16;
      const f32x4 csk = cs_rd(ca, 64), csv = cs_rd(ca, 128);
      u32x2 ident;
      {
        const unsigned d = m16 - 4 * g16;
        ident[0] = (d == 0 ? 0x3f80u : 0u) | (d == 16 ? 0x3f800000u : 0u);
        ident[1] = (d == 32 ? 0x3f80u : 0u) | (d == 48 ? 0x3f800000u : 0u);
      }
      const unsigned b1 = opv(0x3f803f80u);
      const u32x4 bone = {b1, b1, b1, b1};
      f32x4 pk[2];
      u32x2 tv[2];
      sfor<0, 2>([&](auto rt) {
        float mean, var;
        stat_fin(*(LAS f32x4*)(lds + sa + (STL + s * 512 + rt * 256)), mean, var);
        const float r = __builtin_amdgcn_rsqf(var + 1e-5f);
        qr[s][rt] = r; qn[s][rt] = -r * mean;
        pk[rt] = mfma16(wphi, pk4(lnf(ak[rt], csk, r, qn[s][rt])), zero);
        tv[rt] = pk4(mfma16(pk4(lnf(av[rt], csv, r, qn[s][rt])), ident, zero));
      });
      u32x4 ph0, ph1;
      dual_softmax2(pk[0], pk[1], ph0, ph1);
      const u32x4 tp = cat4(pk4(mfma16(u32x2{ph0[0], ph0[1]}, ident, zero)), pk4(mfma16(u32x2{ph1[0], ph1[1]}, ident, zero)));
      const u32x4 tm = cat4(pk4(mfma16(u32x2{ph0[2], ph0[3]}, ident, zero)), pk4(mfma16(u32x2{ph1[2], ph1[3]}, ident, zero)));
      const u32x4 vt = cat4(tv[0], tv[1]);
      kvp = mfma32(tp, vt, kvp); kvm = mfma32(tm, vt, kvm);
      zp = mfma32(tp, bone, zp); zm = mfma32(tm, bone, zm);
    };
    {
      u32x4 gb[8], gbs;
      auto ix = [&](auto RE, u32x4 (&ga)[8], u32x4& gs, unsigned s) {
        gat8(RE, ga, R, l16, GA + L_X + s * S1X, GA + L_X + s * S1X + 16384);
        ldg<RE, 0>(gs, R, l16, GS + L_STX + s * S1S);
        fence();
      };
      AWAIT(32, ix(T_, gx, gxs, 0), (pina(gx), pin(gxs)), mxa(gx, mx4(0, gxs)));
      STAMP(1);
      cla<0, 32>(wkv);
      step_kv(IC(0), gx, gxs, [&]() { ix(F_T, gb, gbs, 1); ldW(wq, IC(I_Q), IC(0), IC(16), R); });
      ldW(wo, IC(I_WO), IC(0), IC(16), R);
      STAMP(2);
      AWAIT(33, ix(T_, gb, gbs, 1), (pina(gb), pin(gbs)), mxa(gb, mx4(0, gbs)));
      STAMP(3);
      step_kv(IC(1), gb, gbs, [&]() { ldW(w1, IC(I_W1), IC(0), IC(16), R); });
    }
    {
      const unsigned vpz = m16 == 0 ? g16 : OOB, po = p_o();
      stc(san(asu(kvp)), R, l16, po + w * 2048);
      stc(san(asu(kvm)), R, l16 + 1024, po + w * 2048);
      stc(san(asu(zp)), R, vpz, po + 8192 + w * 128);
      stc(san(asu(zm)), R, vpz + 64, po + 8192 + w * 128);
    }
    STAMP(4);
    fence();
    auto h2_ab = [&]() { return H2 + (us + UW) * 1024; };
    auto h2_c = [&]() { const unsigned ww = UW; return H2 + (us + 4 + (ww >> 1)) * 1024 + (ww & 1) * 8; };
    {
      const u32x4 o4 = ones4();
      const u32x2 o2 = {o4[0], o4[1]};
      const unsigned va = l16, vc = l16c, ha = h2_ab(), hc = h2_c();
      sfor<0, 2>([&](auto s) {
        sta(o4, R, va, PUB + L_XP + s * S1X);
        sta(o4, R, va, PUB + L_X1 + s * S1X);
        stc(o2, R, g0m, PST + L_STP + s * S1S);
        stc(o2, R, g0m, PST + L_ST1 + s * S1S);
        sfor<0, 2>([&](auto rt) {
          sta(o4, R, va, opq(ha) + s * S1H + rt * H2T);
          stc(o2, R, vc, opq(hc) + s * S1H + rt * H2T);
        });
        sta(o4, Rn, va, PUB + LSZ + L_X + s * S1X);
        stc(o2, Rn, g0m, PST + LSZ + L_STX + s * S1S);
        sta(o4, Rn, va, PUB + LSZ + L_O + s * S1X);
        sta(o4, Rn, va, PUB + LSZ + L_H + 16384 + s * S1X);
      });
      hold(o4);
    }
    ldW(w2, IC(I_W2), IC(0), IC(16), R);

    u32x4 phq[2][2];
    auto step_q = [&](auto S) {
      constexpr int s = decltype(S)::value;
      const unsigned xb[4] = {xbase(0), xbase(1), xbase(2), xbase(3)};
      f32x4 aq[2] = {zero, zero};
      sfor<0, 16>([&](auto p) {
        sfor<0, 2>([&](auto rt) { aq[rt] = mfma32(wq[p], xs_rd(xb, S, rt, p), aq[rt]); });
      });
      const f32x4 csq = cs_rd(g16 + UW * 64, 0);
      const f32x4 p0 = mfma16(wphi, pk4(lnf(aq[0], csq, qr[s][0], qn[s][0])), zero);
      const f32x4 p1 = mfma16(wphi, pk4(lnf(aq[1], csq, qr[s][1], qn[s][1])), zero);
      dual_softmax2(p0, p1, phq[s][0], phq[s][1]);
    };
    clv<0, 16>(wq);
    step_q(IC(0));
    {
      u32x4 pa[2];
      unsigned pz[2];
      auto ip = [&](auto RE) {
        fence();
        const unsigned ln = l16, vq = (ln >> 6) * PSZ, vp = vq + (ln & 48), vz = vq + ((ln & 48) >> 2);
        const unsigned a = PJ + UC * 256 + UW * 64, z = PJ + 8192 + UC * 16;
        ldg<RE, 0>(pa[0], R, vp, a);
        ldg<RE, 0>(pa[1], R, vp, a + 16 * PSZ);
        ldg<RE, 0>(pz[0], R, vz, z);
        ldg<RE, 0>(pz[1], R, vz, z + 16 * PSZ);
        fence();
      };
      ip(F_T);
      ldW(w3, IC(I_W3), IC(0), IC(8), R);
      STAMP(5);
      step_q(IC(1));
      STAMP(6);
      AWAIT(34, ip(T_), (pina(pa), pin(pz[0]), pin(pz[1])), mxa(pa, umax(pz[0], pz[1])));
      STAMP(7);
      f32x4 t = asf(pa[0]) + asf(pa[1]);
      float z = __uint_as_float(pz[0]) + __uint_as_float(pz[1]);
      sfor<0, 4>([&](auto i) {
        float v = t[(int)i];
        v += dppf<0x124>(v); v += dppf<0x128>(v);
        t[(int)i] = rsum_g(v);
      });
      z += dppf<0x124>(z); z += dppf<0x128>(z);
      z = rsum_g(z);
      const f32x4 z4 = {dppf<0x00>(z), dppf<0x55>(z), dppf<0xaa>(z), dppf<0xff>(z)};
      stc(san(pk4(t)), R, vr(), r_o());
      stc(san(pk4(z4)), R, vrz(), rz_o());
    }
    ldW(w3, IC(I_W3), IC(8), IC(8), R);
    u32x4 go0[8], go1[8];
    {
      u32x4 rk[2];
      auto ir = [&](auto RE) {
        fence();
        ldg<RE, 0>(rk[0], R, l16, RJ + UW * 1024);
        ldg<RE, 0>(rk[1], R, g16, RJ + 4096 + UW * 64);
        fence();
      };
      ir(F_T);
      AWAIT(35, ir(T_), pina(rk), mxa(rk, 0));
      STAMP(8);
      const unsigned oo = GA + L_O + j * 2048 - UW * 4096 + (UW >> 1) * 1024 + (UW & 1) * 8, va = l16;
      sfor<0, 2>([&](auto s) {
        sfor<0, 2>([&](auto rt) {
          const f32x4 num = mfma32(rk[0], phq[s][rt], zero), den = mfma32(rk[1], phq[s][rt], zero);
          const float rd = __builtin_amdgcn_rcpf(den[0] + 1e-6f);
          stc(san(pk4(num * rd)), R, va, opq(oo) + rt * 16384 + s * S1X);
        });
      });
    }
    STAMP(9);
    ldW(w3, IC(I_W3), IC(16), IC(8), R);

    auto core = [&](auto S, auto NK, const auto& wf, const auto& ga, f32x4& a0, f32x4& a1, auto hook) {
      constexpr int s = decltype(S)::value, nk = decltype(NK)::value;
      sfor<1, 4>([&](auto X) {
        constexpr int x = 4 - X;
        f32x4 b0 = zero, b1 = zero;
        sfor<0, nk>([&](auto p) {
          b0 = mfma32(wf[4 * p + 2 * (x & 1)], ga[(x >> 1) * nk + p], b0);
          b1 = mfma32(wf[4 * p + 2 * (x & 1) + 1], ga[(x >> 1) * nk + p], b1);
        });
        const unsigned rb = l16 + (RED + (UW ^ x) * 6144);
        *(LAS f32x4*)(lds + rb + (s * 24576 + (x - 1) * 2048)) = b0;
        *(LAS f32x4*)(lds + rb + (s * 24576 + (x - 1) * 2048 + 1024)) = b1;
      });
      sfor<0, nk>([&](auto p) {
        a0 = mfma32(wf[4 * p], ga[p], a0);
        a1 = mfma32(wf[4 * p + 1], ga[p], a1);
      });
      const unsigned rb = l16 + (RED + UW * 6144);
      wgbar();
      hook();
      sfor<0, 3>([&](auto x) {
        a0 += *(LAS f32x4*)(lds + rb + (s * 24576 + x * 2048));
        a1 += *(LAS f32x4*)(lds + rb + (s * 24576 + x * 2048 + 1024));
      });
    };
    auto ic = [&](auto RE, u32x4 (&ga)[8], unsigned voff, unsigned buf, unsigned s) {
      const unsigned o = (UW >> 1) * 16384;
      gat8(RE, ga, R, voff, GA + buf + s * S1X + o, GA + buf + s * S1X + (o ^ 16384));
    };
    auto ist = [&](auto RE, u32x4& gs, unsigned buf, unsigned s) { ldg<RE, 0>(gs, R, l16, GS + buf + s * S1S); fence(); };
    auto un0 = [&](u32x4 v) { return cat4(unpk(v[0]), unpk(v[1])); };
    auto un1 = [&](u32x4 v) { return cat4(unpk(v[2]), unpk(v[3])); };
    auto vh = [&]() {
      const unsigned ww = UW, t = m16 + (256 + (ww & 1 ? 16 : -16) * (int)(1 + (ww >> 1)));
      return (t >> 8) * 16384 + (t & 255) + g16 * 16;
    };
    u32x4 xp[2], x1[2];
    float mu[2], r1[2];
    ic(F_T, go0, l16, L_O, 0);
    ic(F_T, go1, l16, L_O, 1);
    u32x4 gp0[8], gp1[8], gps0, gps1;
    auto step_wo = [&](auto S, const u32x4 (&ga)[8], auto hook) {
      constexpr int s = decltype(S)::value;
      f32x4 a0 = un0(xres[s]), a1 = un1(xres[s]);
      core(S, IC(4), wo, ga, a0, a1, hook);
      xp[s] = san(cat4(pk4(a0), pk4(a1)));
      stc(xp[s], R, l16, PUB + L_XP + s * S1X);
      stc(stat_part(xp[s]), R, g0m, PST + L_STP + s * S1S);
    };
    AWAIT(36, ic(T_, go0, l16, L_O, 0), pina(go0), mxa(go0, 0));
    STAMP(10);
    cla<0, 16>(wo);
    step_wo(IC(0), go0, [&]() {});
    STAMP(11);
    AWAIT(37, ic(T_, go1, l16, L_O, 1), pina(go1), mxa(go1, 0));
    STAMP(12);
    step_wo(IC(1), go1, [&]() { ic(F_T, gp0, l16, L_XP, 0); ist(F_T, gps0, L_STP, 0); });
    ldW(w3, IC(I_W3), IC(24), IC(8), R);
    STAMP(13);
    u32x4 gh0[8], gh1[8];
    auto step_w1 = [&](auto S, const u32x4 (&ga)[8], u32x4 gs, auto hook) {
      constexpr int s = decltype(S)::value;
      stl_w(stat_half(asf(gs)), s);
      f32x4 a0 = zero, a1 = zero;
      core(S, IC(4), w1, ga, a0, a1, hook);
      float var;
      stat_fin(*(LAS f32x4*)(lds + (m16 + (UW >> 1) * 256) + (STL + s * 512)), mu[s], var);
      r1[s] = __builtin_amdgcn_rsqf(var + 1e-5f);
      const float r = r1[s] * __builtin_amdgcn_rsqf(var * r1[s] * r1[s] + 1e-5f), nrm = -r * mu[s];
      const unsigned ca = g16 * 2 + (UW & 1) * 128;
      const f32x4 c0 = cs_rd(ca, 192), c1 = cs_rd(ca, 196);
      stc(san(cat4(gelu4(lnf(a0, c0, r, nrm)), gelu4(lnf(a1, c1, r, nrm)))), R, l16, PUB + L_H + 16384 + s * S1X);
    };
    AWAIT(38, (ic(T_, gp0, l16, L_XP, 0), ist(T_, gps0, L_STP, 0)), (pina(gp0), pin(gps0)), mxa(gp0, mx4(0, gps0)));
    STAMP(14);
    cla<0, 16>(w1);
    step_w1(IC(0), gp0, gps0, [&]() { ic(F_T, gp1, l16, L_XP, 1); ist(F_T, gps1, L_STP, 1); });
    ldW(w3, IC(I_W3), IC(32), IC(8), R);
    STAMP(15);
    AWAIT(39, (ic(T_, gp1, l16, L_XP, 1), ist(T_, gps1, L_STP, 1)), (pina(gp1), pin(gps1)), mxa(gp1, mx4(0, gps1)));
    STAMP(16);
    step_w1(IC(1), gp1, gps1, [&]() { ic(F_T, gh0, vh(), L_H, 0); });
    ldW(w3, IC(I_W3), IC(40), IC(8), R);
    STAMP(17);
    u32x4 g10[8], g11[8], g1s0, g1s1;
    auto i1 = [&](auto RE, u32x4 (&ga)[8], u32x4& gs, unsigned s) {
      gat8(RE, ga, R, l16, GA + L_X1 + s * S1X, GA + L_X1 + s * S1X + 16384);
      ist(RE, gs, L_ST1, s);
    };
    auto step_w2 = [&](auto S, const u32x4 (&ga)[8], auto hook) {
      constexpr int s = decltype(S)::value;
      f32x4 a0 = (un0(xp[s]) - mu[s]) * r1[s], a1 = (un1(xp[s]) - mu[s]) * r1[s];
      core(S, IC(4), w2, ga, a0, a1, hook);
      x1[s] = san(cat4(pk4(a0), pk4(a1)));
      stc(x1[s], R, l16, PUB + L_X1 + s * S1X);
      stc(stat_part(x1[s]), R, g0m, PST + L_ST1 + s * S1S);
    };
    AWAIT(40, ic(T_, gh0, vh(), L_H, 0), pina(gh0), mxa(gh0, 0));
    STAMP(18);
    clv<0, 16>(w2);
    step_w2(IC(0), gh0, [&]() { ic(F_T, gh1, vh(), L_H, 1); });
    ldW(w4, IC(I_W4), IC(0), IC(8), R);
    STAMP(19);
    AWAIT(41, ic(T_, gh1, vh(), L_H, 1), pina(gh1), mxa(gh1, 0));
    STAMP(20);
    step_w2(IC(1), gh1, [&]() { i1(F_T, g10, g1s0, 0); });
    ldW(w4, IC(I_W4), IC(8), IC(8), R);
    STAMP(21);
    u32x4 g20[22], g21[22];
    auto i2 = [&](auto RE, u32x4 (&ga)[22], unsigned s) {
      fence();
      const unsigned va = l16, ve = l16e, ww = UW;
      const unsigned b0 = H2 + (ww >> 1) * H2T + ww * 11264 + s * S1H, b1 = H2 + ((ww >> 1) ^ 1) * H2T + ww * 11264 + s * S1H;
      sfor<0, 11>([&](auto p) { ldg<RE, (p & 3) * 1024>(ga[11 + p], R, p == 10 ? ve : va, b1 + (p >> 2) * 4096); });
      sfor<0, 11>([&](auto p) { ldg<RE, (p & 3) * 1024>(ga[p], R, p == 10 ? ve : va, b0 + (p >> 2) * 4096); });
      fence();
    };
    auto step_w3 = [&](auto S, const u32x4 (&ga)[8], u32x4 gs, auto hookA, auto hookM, auto hookB) {
      constexpr int s = decltype(S)::value;
      stage(S, ga, gs);
      f32x4 a[2][3] = {{zero, zero, zero}, {zero, zero, zero}};
      sfor<0, 4>([&](auto p) {
        sfor<0, 2>([&](auto rt) {
          sfor<0, 3>([&](auto t) { a[rt][t] = mfma32(w3[3 * p + t], ga[rt * 4 + p], a[rt][t]); });
        });
      });
      const unsigned xb[4] = {0, xbase(1), xbase(2), xbase(3)};
      wgbar();
      hookA();
      sfor<4, 16>([&](auto p) {
        if constexpr (p == 10) hookM();
        sfor<0, 2>([&](auto rt) {
          const u32x4 x = xs_rd(xb, S, rt, p);
          sfor<0, 3>([&](auto t) { a[rt][t] = mfma32(w3[3 * p + t], x, a[rt][t]); });
        });
      });
      hookB();
      const unsigned g32 = g16 * 2, ww = UW, ca = g32 + ww * 128, cb = g32 + ((ww >> 1) * 128 + (ww & 1) * 16), sa = m16;
      const f32x4 c0 = cs_rd(ca, 256), c1 = cs_rd(ca, 260), c2 = cs_rd(cb, 256 + 128);
      const unsigned ha = h2_ab(), hc = h2_c();
      sfor<0, 2>([&](auto rt) {
        float mean, var;
        stat_fin(*(LAS f32x4*)(lds + sa + (STL + s * 512 + rt * 256)), mean, var);
        const float r = __builtin_amdgcn_rsqf(var + 1e-5f), nrm = -r * mean;
        stc(san(cat4(gelu4(lnf(a[rt][0], c0, r, nrm)), gelu4(lnf(a[rt][1], c1, r, nrm)))), R, l16, ha + s * S1H + rt * H2T);
        stc(san(gelu4(lnf(a[rt][2], c2, r, nrm))), R, l16c, hc + s * S1H + rt * H2T);
      });
    };
    AWAIT(42, i1(T_, g10, g1s0, 0), (pina(g10), pin(g1s0)), mxa(g10, mx4(0, g1s0)));
    STAMP(22);
    cla<0, 48>(w3);
    step_w3(IC(0), g10, g1s0, [&]() { i1(F_T, g11, g1s1, 1); ldW(w4, IC(I_W4), IC(16), IC(8), R); }, [&]() {}, [&]() {});
    STAMP(23);
    AWAIT(43, i1(T_, g11, g1s1, 1), (pina(g11), pin(g1s1)), mxa(g11, mx4(0, g1s1)));
    STAMP(24);
    step_w3(IC(1), g11, g1s1, [&]() { ldW(w4, IC(I_W4), IC(24), IC(12), R); }, [&]() { ldW(w4, IC(I_W4), IC(36), IC(8), R); },
            [&]() { i2(F_T, g20, 0); });
    ldW(wkv, IC(I_KV), IC(0), IC(16), Rn);
    STAMP(25);
    auto step_w4 = [&](auto S, const u32x4 (&ga)[22], auto hook) {
      constexpr int s = decltype(S)::value;
      f32x4 a0 = un0(xp[s]) + un0(x1[s]), a1 = un1(xp[s]) + un1(x1[s]);
      core(S, IC(11), w4, ga, a0, a1, hook);
      const u32x4 o = cat4(pk4(a0), pk4(a1));
      xres[s] = san(o);
      stc(xres[s], Rn, l16, PUB + LSZ + L_X + s * S1X);
      stc(stat_part(xres[s]), Rn, g0m, PST + LSZ + L_STX + s * S1S);
      const unsigned ww = UW;
      __builtin_amdgcn_raw_buffer_store_b128(o, mk(out, lay == 0 ? 0 : 2048 * 1024), m16 * 64 + g16,
                                             (2 * UC + (ww >> 1) + 64 * s) * 16384 + (2 * j + (ww & 1)) * 64, 0);
      hold(o);
    };
    AWAIT(44, i2(T_, g20, 0), pina(g20), mxa(g20, 0));
    STAMP(26);
    clv<0, 16>(w4);
    cla<16, 44>(w4);
    step_w4(IC(0), g20, [&]() { i2(F_T, g21, 1); });
    ldW(wkv, IC(I_KV), IC(16), IC(8), Rn);
    STAMP(27);
    AWAIT(45, i2(T_, g21, 1), pina(g21), mxa(g21, 0));
    STAMP(28);
    step_w4(IC(1), g21, [&]() {
      gat8(F_T, gx, Rn, l16, GA + LSZ + L_X, GA + LSZ + L_X + 16384);
      gxs = ldc(Rn, l16, GS + LSZ + L_STX);
      fence();
      ldW(wkv, IC(I_KV), IC(24), IC(8), Rn);
    });
    STAMP(29);
    ga_ += LSZ; gs_ += LSZ; pub_ += LSZ; pst_ += LSZ; h2_ += LSZ; pj_ += LSZ; rj_ += LSZ;
  }
}

extern "C" void kernel_launch(void* const* d_in, const int* in_sizes, int n_in, void* d_out, int out_size,
                              void* d_ws, size_t ws_size, hipStream_t stream) {
  if (ws_size < WS_BYTES + LSZ) return;
  k0_prep<<<256, 512, 0, stream>>>((const u16*)d_in[0], (const u16*)d_in[1], (const u16*)d_in[2], (const u16*)d_in[3],
                                   (const u16*)d_in[5], (const u16*)d_in[6], (const u16*)d_in[7], (const u16*)d_in[8],
                                   (char*)d_ws);
  kp_main<<<256, 256, 0, stream>>>((char*)d_ws, (u16*)d_out);
}
```

```cpp
#include <hip/hip_runtime.h>
#include <hip/hip_bf16.h>
#include <stdint.h>
#include <type_traits>

typedef unsigned short u16;
typedef short s16x4 __attribute__((ext_vector_type(4)));
typedef short s16x8 __attribute__((ext_vector_type(8)));
typedef __bf16 bf16x8 __attribute__((ext_vector_type(8)));
typedef __bf16 bf16x2 __attribute__((ext_vector_type(2)));
typedef float f32x4 __attribute__((ext_vector_type(4)));
typedef float f32x2 __attribute__((ext_vector_type(2)));
typedef unsigned u32x4 __attribute__((ext_vector_type(4)));
typedef unsigned u32x2 __attribute__((ext_vector_type(2)));
typedef __amdgpu_buffer_rsrc_t rsrc_t;
#define LAS __attribute__((address_space(3)))

constexpr int F_ = 1376;

#define DEVI __device__ __forceinline__

DEVI unsigned pack2(float a, float b) {
  bf16x2 v = {(__bf16)a, (__bf16)b};
  return __builtin_bit_cast(unsigned, v);
}
DEVI unsigned pack2(f32x2 a) { return pack2(a[0], a[1]); }
DEVI float bflo(unsigned u) { return __uint_as_float(u << 16); }
DEVI float bfhi(unsigned u) { return __uint_as_float(u & 0xffff0000u); }
DEVI f32x2 unpk(unsigned u) { return f32x2{bflo(u), bfhi(u)}; }

DEVI rsrc_t mk(const void* p, unsigned n) { return __builtin_amdgcn_make_buffer_rsrc((void*)p, 0, n, 0x00020000); }
DEVI unsigned uni(unsigned s) { return __builtin_amdgcn_readfirstlane(s); }
DEVI f32x2 fma2(f32x2 a, f32x2 b, f32x2 c) { return __builtin_elementwise_fma(a, b, c); }
DEVI f32x2 bc2(float a) { return f32x2{a, a}; }
DEVI void sw16(float& a, float& b) {
  auto r = __builtin_amdgcn_permlane16_swap(__float_as_uint(a), __float_as_uint(b), false, false);
  a = __uint_as_float(r[0]); b = __uint_as_float(r[1]);
}
DEVI void sw32(float& a, float& b) {
  auto r = __builtin_amdgcn_permlane32_swap(__float_as_uint(a), __float_as_uint(b), false, false);
  a = __uint_as_float(r[0]); b = __uint_as_float(r[1]);
}
DEVI float rsum_g(float v) {
  float a = v, b = v;
  sw16(a, b);
  a = a + b; b = a;
  sw32(a, b);
  return a + b;
}
template <bool MAX> DEVI float red4(float a, float b, float c, float d) {
  sw16(a, b); sw16(c, d);
  float e = MAX ? fmaxf(a, b) : a + b, f = MAX ? fmaxf(c, d) : c + d;
  sw32(e, f);
  return MAX ? fmaxf(e, f) : e + f;
}
DEVI u32x2 stat_part(u32x4 o) {
  f32x2 v0 = unpk(o[0]), v1 = unpk(o[1]), v2 = unpk(o[2]), v3 = unpk(o[3]);
  f32x2 s2 = (v0 + v1) + (v2 + v3);
  float mean = rsum_g(s2[0] + s2[1]) * (1.0f / 32.0f);
  f32x2 mm = bc2(mean);
  v0 -= mm; v1 -= mm; v2 -= mm; v3 -= mm;
  f32x2 q2 = fma2(v1, v1, v0 * v0) + fma2(v3, v3, v2 * v2);
  float q = rsum_g(q2[0] + q2[1]);
  return u32x2{__float_as_uint(mean), __float_as_uint(q)};
}
constexpr unsigned MB = 1u << 20;
constexpr int NF = 188;
constexpr int I_KV = 0, I_Q = 32, I_WO = 48, I_W1 = 64, I_W2 = 80, I_W3 = 96, I_W4 = 144;
constexpr unsigned O_WT = 0, O_CS = 6 * MB, O_WP = 6 * MB + 0x8000, O_PA = 6 * MB + 0x10000, O_CAL = 8 * MB, O_L = 12 * MB, US = 4 * MB, O_H2 = O_L + 2 * MB;
constexpr unsigned D_ST = MB, D_HA = MB + 0x40000, D_SM = MB + 0x80000, L_Z = D_SM, L_R = D_SM + 0x20000;
constexpr unsigned RSZ = 4352;
constexpr unsigned WS_BYTES = 48 * MB;
constexpr unsigned OOB = 0x80000000u;
constexpr int W2P = 1;
constexpr unsigned XPAR = 0;
#define RPAR 1
constexpr int QL = 1;
#define K0AUX 16
#define ARMAUX K0AUX
#define ARMPOS 1
#define CALN 8
#define CALT 2000
#define CALX 1
#define LDAUX 0
constexpr int NCAL = RPAR ? 8 : 0;
typedef unsigned u32x16 __attribute__((ext_vector_type(16)));
#define GAS __attribute__((address_space(1)))
DEVI void wst(rsrc_t r, unsigned o, u32x4 v) { __builtin_amdgcn_raw_buffer_store_b128(v, r, o, 0, K0AUX); }
DEVI void ast(rsrc_t r, unsigned o, u32x4 v) { __builtin_amdgcn_raw_buffer_store_b128(v, r, o, 0, ARMAUX); }
__global__ __launch_bounds__(512) void k0_prep(const u16* wqkv, const u16* w1, const u16* w3, const u16* wo, const u16* w2,
                                               const u16* w4, const u16* x, const u16* wphi, char* ws_) {
  __shared__ float red[512];
  __shared__ u32x4 stg[2048];
#define KST(t, n)
  int b = (int)blockIdx.x, tid = threadIdx.x;
  u32x16 ka;
  char *ws, *src;
#define CT1 " s_waitcnt vmcnt(0)\n s_memtime s[88:89]\n s_waitcnt lgkmcnt(0)\n"
#define CT2 " s_sub_u32 s95, s88, s94\n s_min_u32 s92, s92, s95\n s_mov_b32 s94, s88\n"
#define CLN " .p2align 6\n"
#define COA CST
#define COB CLD
#define CST " global_store_dword v2, v3, %1 sc1\n"
#define CLD " global_load_dword v3, v2, %1 sc1\n"
  asm volatile("s_load_dwordx16 %0, %4, 0\n s_load_dwordx2 %1, %4, 0x40\n s_sub_u32 %2, %2, 8\n s_cbranch_scc0 9f\n"
               "v_readfirstlane_b32 s84, %3\n s_getpc_b64 s[96:97]\n6:\n s_add_u32 s85, s84, 0f-6b\n s_add_u32 s96, s96, s85\n s_addc_u32 s97, s97, 0\n"
               "s_mov_b64 exec, 0\n s_setpc_b64 s[96:97]\n" CLN
               "0:\n s_memtime s[88:89]\n v_lshlrev_b32 v2, 2, %3\n v_and_b32 v3, 0x1c0, %3\n s_and_b32 s85, %2, 7\n v_mad_u32_u24 v2, v3, 60, v2\n"
               "s_mul_i32 s86, s85, 0x8200\n s_add_u32 s86, s86, %5\n v_add_u32 v2, s86, v2\n s_lshr_b32 s86, s85, 2\n s_and_b32 s95, s85, 1\n s_lshl1_add_u32 s86, s95, s86\n" CLN
               "s_lshr_b32 s87, s84, 7\n s_xor_b32 s86, s86, s87\n s_lshr_b32 s87, s84, 4\n v_mov_b32 v4, s87\n s_lshl2_add_u32 s85, s85, %6\n v_mov_b32 v5, s85\n v_mov_b32 v6, 0\n"
               "s_mov_b32 s90, 0\n s_mov_b32 s91, %8\n s_mov_b32 s92, 0xffffff\n s_waitcnt lgkmcnt(0)\n s_mov_b32 s93, s88\n s_mov_b32 s94, s88\n" CLN
               "1:\n" COA CT1 CT2 COB CT1 CLN
               CT2 "s_cbranch_execz 2f\n s_cmp_gt_u32 s90, %7\n s_cbranch_scc1 2f\n s_sub_u32 s90, s88, s93\n s_sub_u32 s91, s91, 1\n s_cmp_lg_u32 s91, 0\n s_cbranch_scc1 1b\n"
               "2:\n s_lshl2_add_u32 s92, s92, s86\n v_mov_b32 v7, s92\n ds_write_b32 v4, v7\n s_waitcnt lgkmcnt(0)\n" CLN
               "s_cbranch_execz 5f\n s_barrier\n5:\n ds_read_b128 v[8:11], v6\n ds_read_b128 v[12:15], v6 offset:16\n s_waitcnt lgkmcnt(0)\n"
               "v_min3_u32 v8, v8, v9, v10\n v_min3_u32 v12, v12, v13, v14\n v_min_u32 v8, v8, v11\n v_min_u32 v12, v12, v15\n v_min_u32 v9, v8, v12\n" CLN
               "global_store_dword v5, v9, %1\n s_cbranch_execnz 3f\n"
               "4:\n s_mov_b64 exec, -1\n s_branch 0b\n"
               "3:\n s_endpgm\n"
               CLN "s_branch 4b\n" CLN "s_branch 4b\n"
               "9:"
               : "={s[64:79]}"(ka), "={s[80:81]}"(ws), "+s"(b), "+v"(tid)
               : "s"(__builtin_amdgcn_kernarg_segment_ptr()), "n"(O_CAL), "n"(O_PA), "n"(CALT), "n"(CALN)
               : "memory", "scc", "s84", "s85", "s86", "s87", "s88", "s89", "s90", "s91", "s92", "s93", "s94", "s95", "s96", "s97", "s98", "s99", "s100", "v2", "v3", "v4", "v5", "v6", "v7",
                 "v8", "v9", "v10", "v11", "v12", "v13", "v14", "v15");
  const int lane = tid & 63, wave = tid >> 6, m = lane & 15, g = lane >> 4;
  const int cls = __builtin_popcountll(0x8000800888011000ull << (63 - (b >> 2)));
  const int rel = b - (int)((0xfcbc8c7c6c403000ull >> (8 * cls)) & 255);
  const int ld = (int)((0x10101010102b1030ull >> (8 * cls)) & 255) * 32;
  const bool isx = cls == 6;
  const int c0 = isx ? 0 : (cls == 5 ? rel & 7 : rel >> 1) * 64, kb = (cls == 5 ? rel >> 3 : rel & 1) * 8, ks = kb + wave;
  const int col = c0 + 4 * m;
  const bool cok = col < ld;
  u32x2 v[8];
  {
    const unsigned s1 = 2 * ld, s2 = 4 * ld, s4 = ld << (cls == 3 ? 5 : 3);
    const unsigned sm = isx ? 1024 : 8, sg = isx ? 16 : ld << (cls == 3 ? 3 : 4), sl = isx ? 256 : 64 * ld, sh = isx ? 16384 : 256 * ld;
    const unsigned sb = isx ? rel * 32768 : kb * 64 * ld + c0 * 2;
    const unsigned nr = cls == 7 ? 0 : isx ? 2048 * 1024 : cls == 5 ? F_ * 1024 : ld * 1024;
    unsigned vo = __umul24(m, sm) + __umul24(g, sg) + __umul24(wave & 3, sl) + __umul24(wave >> 2, sh) + sb;
    vo = cok ? vo : OOB;
    const unsigned s3 = s1 + s2, s5 = s4 + s1, s6 = s4 + s2, s7 = s4 + s3;
    asm volatile("s_mov_b32 m0, %3\n s_waitcnt lgkmcnt(0)\n s_movrels_b64 %0, s[64:65]"
                 : "=s"(src), "+{s[80:81]}"(ws)
                 : "{s[64:79]}"(ka), "s"(2 * cls), "v"(vo), "s"(nr), "s"(s1), "s"(s2), "s"(s3), "s"(s4), "s"(s5), "s"(s6), "s"(s7) : "m0");
    const rsrc_t rs = mk(src, nr);
    const unsigned so[8] = {0, s1, s2, s3, s4, s5, s6, s7};
    if (isx) {
#pragma unroll
      for (int i = 0; i < 4; ++i) {
        const u32x4 t = __builtin_amdgcn_raw_buffer_load_b128(rs, vo + i * 64, 0, LDAUX);
        v[2 * i] = u32x2{t[0], t[1]}; v[2 * i + 1] = u32x2{t[2], t[3]};
      }
    } else {
#pragma unroll
      for (int i = 0; i < 8; ++i) v[i] = __builtin_amdgcn_raw_buffer_load_b64(rs, vo, so[i], LDAUX);
    }
  }
#define KCP 7
  if (wave == KCP) {
    unsigned long long pc;
    asm volatile("s_getpc_b64 s[84:85]\n s_add_u32 s84, s84, kp_code_a@rel32@lo+4\n s_addc_u32 s85, s85, kp_code_a@rel32@hi+12\n"
                 "v_min_u32 v100, kp_code_z-kp_code_a-4, %0\n global_load_dword v100, v100, s[84:85]" :: "v"(b * 256 + lane * 4) : "v100", "s84", "s85", "scc");
  }
  const rsrc_t rw = mk(ws, WS_BYTES);
  auto arms = [&]() {
    const u32x4 e = {~0u, ~0u, ~0u, ~0u}, z = {0u, 0u, 0u, 0u};
    constexpr unsigned N1 = (0x20000 + 32 * RSZ) / 16, N2 = 2 * N1, N3 = N2 + (32768 << QL);
    static_assert(N3 <= 131072, "");
    const unsigned i = b * 512 + tid;
    ast(rw, O_H2 + i * 16, e);
    unsigned o;
    bool zero = false;
    if (i < N2) {
      const unsigned q = i < N1 ? i : i - N1, r = q - 8192, d = r / (RSZ / 16);
      o = O_L + L_Z + (i < N1 ? 0 : 3 * US) + (q < 8192 ? q * 16 : 0x20000 + d * 8192 + (r - d * (RSZ / 16)) * 16);
    } else {
      constexpr unsigned zm = (0x1c000 << QL) + 0x1280;
      const unsigned q = i - N2, x = (q & ((8192 << QL) - 1)) * 16, s = (q >> (13 + QL)) & 1;
      o = O_L + D_HA + ((q >> (14 + QL) ? 0 : 4) + s) * US + x;
      zero = (x & zm) == (s ? zm : 0);
    }
    if (i < N3) ast(rw, o, zero ? z : e);
  };
  if (ARMPOS == 0) arms();
  if (cls < 6) {
    const int mat = (0x531420 >> (4 * cls)) & 15, nks = 43, oc = cls < 3 ? (cls == 0 ? 0 : 1024 + 512 * cls) + c0 : -1;
    int j, w, idx, n0;
    const int U = col >> 5, c4 = (col & 31) >> 2, t = c4 & 1;
    if (mat == 0) {
      const int sec = col >> 9, hd = (col & 511) >> 4;
      j = hd >> 2; w = hd & 3; n0 = col & 15;
      const int p = (ks - 4 * w) & 15;
      idx = sec == 0 ? I_Q + p : I_KV + 2 * p + (sec - 1);
    } else if (mat == 4) {
      n0 = 4 * (c4 >> 1);
      if (U < 32) { j = U >> 2; w = U & 3; idx = I_W3 + 2 * ((ks - 4 * w) & 15) + t; }
      else {
        const int e = U - 32;
        if (e < 6) { j = e >> 1; w = 2 * (e & 1) + t; } else { j = e - 3; w = t; }
        idx = I_W3 + 32 + ((ks - 4 * w) & 15);
      }
    } else {
      n0 = 4 * (c4 >> 1);
      j = U >> 1;
      int p;
      if (mat == 5) { if (ks < 32) { w = ks >> 3; p = ks & 7; } else { w = (ks - 32) / 3; p = 8 + (ks - 32) % 3; } idx = I_W4; }
      else { w = W2P && mat == 3 ? (ks >> 3) | ((ks >> 1) & 2) : ks >> 2; p = ks & 3; idx = I_WO + 16 * (mat - 1); }
      idx += 4 * p + 2 * ((U ^ w) & 1) + t;
    }
    {
      const unsigned d = cok && ks < nks ? O_WT + ((j * 4 + w) * NF + idx) * 1024 : OOB;
      const int T = mat == 0 ? m >> 2 : (m >> 3) * 2 + t;
      u32x4* s = stg + wave * 256;
#pragma unroll
      for (int e = 0; e < 4; ++e) {
        u32x4 o;
#pragma unroll
        for (int q = 0; q < 4; ++q)
          o[q] = __builtin_amdgcn_perm(v[2 * q + 1][e >> 1], v[2 * q][e >> 1], (e & 1) ? 0x07060302u : 0x05040100u);
        s[T * 64 + g * 16 + n0 + e] = o;
      }
      __builtin_amdgcn_fence(__ATOMIC_RELEASE, "wavefront");
      __builtin_amdgcn_wave_barrier();
      __builtin_amdgcn_fence(__ATOMIC_ACQUIRE, "wavefront");
#pragma unroll
      for (int e = 0; e < 4; ++e) {
        const unsigned a = __builtin_amdgcn_readlane(d, 4 * e), c = __builtin_amdgcn_readlane(d, (e >> 1) * 8 + (e & 1));
        wst(rw, (mat == 0 ? a : c) + lane * 16, s[e * 64 + lane]);
      }
    }
    if (ARMPOS == 1) arms();
    if (oc >= 0) {
      f32x2 a0 = {0.f, 0.f}, a1 = {0.f, 0.f};
#pragma unroll
      for (int i = 0; i < 8; ++i) { a0 += unpk(v[i][0]); a1 += unpk(v[i][1]); }
      const float s = red4<false>(a0[0], a0[1], a1[0], a1[1]);
      red[wave * 64 + 4 * m + g] = cok ? s : 0.f;
      __syncthreads();
      if (tid < 64) {
        float t = 0.f;
#pragma unroll
        for (int w = 0; w < 8; ++w) t += red[w * 64 + tid];
        ((GAS float*)(ws + O_CS + (b & 1) * 0x4000))[oc + tid] = t;
      }
    }
  } else if (isx) {
    const int RT = 2 * (b - 188) + (wave >> 2), k0 = 4 * (wave & 3);
    const unsigned xb = O_L + (5 + (RT >> 6)) * US + (((RT & 32) << 8) ^ 8192 ^ XPAR), xm = RPAR ? 8192 : 0;
    float mean[4], q[4];
#pragma unroll
    for (int i = 0; i < 4; ++i) {
      f32x2 v0 = unpk(v[2 * i][0]), v1 = unpk(v[2 * i][1]), v2 = unpk(v[2 * i + 1][0]), v3 = unpk(v[2 * i + 1][1]);
      const f32x2 s2 = (v0 + v1) + (v2 + v3);
      mean[i] = rsum_g(s2[0] + s2[1]) * (1.0f / 32.0f);
      const f32x2 mm = bc2(mean[i]);
      v0 -= mm; v1 -= mm; v2 -= mm; v3 -= mm;
      const f32x2 q2 = fma2(v1, v1, v0 * v0) + fma2(v3, v3, v2 * v2);
      q[i] = q2[0] + q2[1];
    }
#pragma unroll
    for (int i = 0; i < 4; ++i)
      wst(rw, (xb ^ (k0 >> 3) * xm) + (RT & 30) * 32768 + (RT & 1) * 32768 + (k0 >> 3) * 16384 + ((k0 & 7) + i) * 1024 + lane * 16, u32x4{v[2 * i][0], v[2 * i][1], v[2 * i + 1][0], v[2 * i + 1][1]});
    const float qg = red4<false>(q[0], q[1], q[2], q[3]), mg = g & 2 ? (g & 1 ? mean[3] : mean[2]) : (g & 1 ? mean[1] : mean[0]);
    *(GAS f32x2*)(ws + (xb ^ (RT & 1) * xm) + D_ST + (RT & 28) * 4096 + (RT & 3) * 2048 + ((k0 + g) >> 1) * 256 + m * 16 + (g & 1) * 8) = f32x2{mg, qg};
    if (ARMPOS == 1) arms();
  } else {
    if (b == 252) {
      u32x2 o[4];
#pragma unroll
      for (int q = 0; q < 4; ++q) {
        const GAS u16* p = (const GAS u16*)src + (wave * 4 + q) * 256 + g * 64 + m;
        o[q] = u32x2{p[0] | ((unsigned)p[16] << 16), p[32] | ((unsigned)p[48] << 16)};
      }
#pragma unroll
      for (int q = 0; q < 4; ++q) *(GAS u32x2*)(ws + O_WP + (wave * 4 + q) * 512 + lane * 8) = o[q];
    }
    if (ARMPOS == 1) arms();
  }
}

constexpr unsigned NST = 19, LDS_BYTES = 162816;

__global__ __launch_bounds__(256, 1) void kp_main(char* __restrict__ ws, u16* __restrict__ out) {
  __shared__ __attribute__((aligned(1024))) char lds_[LDS_BYTES];
  const unsigned lb = (unsigned)(uintptr_t)(LAS char*)lds_;
  const unsigned long long ka = (unsigned long long)__builtin_amdgcn_kernarg_segment_ptr();
  asm volatile(
"kp_code_a:\n"
"s_load_dwordx2 s[4:5], s[0:1], 0x0\n"
"v_mov_b32 v56, -1\n"
"v_mov_b32 v57, -1\n"
"v_mov_b32 v58, -1\n"
"s_load_dwordx2 s[100:101], s[0:1], 0x8\n"
"s_getpc_b64 s[92:93]\n"
"LPF%=:\n"
"s_lshr_b32 s94, s2, 3\n"
"v_lshlrev_b32 v255, 3, v0\n"
"s_lshl_b32 s94, s94, 11\n"
"v_add_u32 v255, s94, v255\n"
"v_min_u32 v255, LEND%=-LPF%=-4, v255\n"
"global_load_dword a255, v255, s[92:93]\n"
"s_mov_b32 s8, 0x610000\n"
"v_and_b32 v26, 63, v0\n"
"v_lshlrev_b32 v1, 4, v26\n"
"s_and_b32 s10, s2, 7\n"
"s_lshl_b32 s9, s10, 2\n"
"s_lshr_b32 s10, s2, 6\n"
"s_add_u32 s49, s9, s10\n"
"s_and_b32 s32, s49, 15\n"
"s_lshl_b32 s41, s32, 16\n"
"s_and_b32 s54, s49, 16\n"
"s_lshl_b32 s34, s54, 9\n"
"v_lshrrev_b32 v2, 6, v0\n"
"s_nop 0\n"
"v_readfirstlane_b32 s55, v2\n"
"v_mov_b32 v59, -1\n"
"s_lshr_b32 s35, s55, 1\n"
"s_lshl_b32 s9, s35, 13\n"
"s_xor_b32 s10, s9, 0x2000\n"
"s_xor_b32 s9, s34, s10\n"
"s_add_u32 s11, s41, s9\n"
"s_lshl_b32 s12, s35, 14\n"
"s_add_u32 s10, s11, s12\n"
"s_and_b32 s33, s55, 1\n"
"s_lshl_b32 s51, s33, 12\n"
"s_add_u32 s80, s10, s51\n"
"s_mov_b32 s26, 0x1c00000\n"
"s_mov_b32 s27, 0x20000\n"
"s_add_u32 s40, s80, 0x1400000\n"
"s_add_u32 s39, s80, 0x1408000\n"
"s_lshr_b32 s10, s32, 1\n"
"s_lshl_b32 s11, s10, 14\n"
"s_add_u32 s10, s11, s9\n"
"s_and_b32 s50, s49, 1\n"
"s_lshl_b32 s59, s50, 12\n"
"s_lshl_b32 s9, s35, 11\n"
"s_add_u32 s48, s59, s9\n"
"s_add_u32 s9, s10, s48\n"
"s_lshl_b32 s57, s33, 10\n"
"s_add_u32 s81, s9, s57\n"
"s_add_u32 s38, s81, 0x1500000\n"
"s_bfe_u32 s60, s2, 0x30003\n"
"s_lshl_b32 s9, s60, 2\n"
"s_add_u32 s2, s9, s55\n"
"s_mul_i32 s36, s2, 0x2f000\n"
"s_mov_b32 s6, 0x1a00000\n"
"s_mov_b32 s7, 0x20000\n"
"v_add_u32 v2, 0x1000, v1\n"
"v_sub_u32 v4, 0x6f, v0\n"
"v_and_b32 v3, 0x80000000, v4\n"
"s_lshl_b32 s9, s60, 6\n"
"v_lshrrev_b32 v5, 4, v0\n"
"v_and_b32 v39, 15, v0\n"
"v_lshlrev_b32 v4, 2, v39\n"
"v_lshl_add_u32 v6, v5, 9, v4\n"
"v_add_u32 v4, s9, v6\n"
"s_lshl_b32 s10, s60, 7\n"
"s_add_u32 s9, s10, 0x700\n"
"v_mov_b32 v6, s9\n"
"s_lshl_b32 s52, s60, 1\n"
"s_add_u32 s9, s60, 3\n"
"s_cmp_lt_u32 s60, 3\n"
"s_cselect_b32 s56, s52, s9\n"
"s_lshl_b32 s10, s56, 5\n"
"s_add_u32 s9, s10, 0xa80\n"
"v_mov_b32 v7, s9\n"
"v_cmp_gt_u32 vcc, 0x60, v0\n"
"s_nop 1\n"
"v_cndmask_b32 v8, v7, v6, vcc\n"
"v_lshl_add_u32 v6, v0, 2, v8\n"
"v_cmp_gt_u32 vcc, 64, v0\n"
"s_nop 1\n"
"v_cndmask_b32 v7, v6, v4, vcc\n"
"v_lshlrev_b32 v6, 2, v7\n"
"v_add_u32 v4, 0x600000, v6\n"
"v_or_b32 v43, v3, v4\n"
"s_mov_b32 s65, 0x4000\n"
"v_add_u32 v3, 0x2000, v1\n"
"v_add_u32 v4, 0x3000, v1\n"
"s_waitcnt lgkmcnt(0)\n"
"s_nop 0\n"
"s_and_b32 s5, s5, 0xffff\n"
"s_load_dwordx8 s[84:91], s[4:5], s8\n"
"s_add_u32 s24, s4, 0xc00000\n"
"s_addc_u32 s25, s5, 0\n"
"buffer_load_dwordx4 a[132:135], v1, s[24:27], s40 offen\n"
"s_and_b32 s101, s101, 0xffff\n"
"s_mov_b64 s[20:21], s[24:25]\n"
"s_add_u32 s28, s4, 0x1a00000\n"
"s_addc_u32 s29, s5, 0\n"
"s_mov_b64 s[8:9], s[4:5]\n"
"s_mov_b64 s[12:13], s[4:5]\n"
"s_mov_b64 s[16:17], s[4:5]\n"
"s_add_u32 s37, s36, 0x4000\n"
"s_lshl_b32 s1, s2, 9\n"
"s_add_u32 s0, s1, 0x608000\n"
"v_lshl_add_u32 v45, v26, 3, s0\n"
"s_lshr_b32 s2, s60, 2\n"
"s_lshl_b32 s0, s2, 13\n"
"buffer_load_dwordx4 a[136:139], v1, s[24:27], s40 offen offset:1024\n"
"s_xor_b32 s1, s0, 0x2000\n"
"s_xor_b32 s0, s34, s1\n"
"s_add_u32 s1, s41, s0\n"
"s_lshl_b32 s41, s2, 14\n"
"s_add_u32 s0, s1, s41\n"
"s_lshl_b32 s61, s35, 15\n"
"s_and_b32 s41, s60, 3\n"
"s_lshl_b32 s1, s41, 1\n"
"s_add_u32 s41, s1, s33\n"
"s_lshl_b32 s1, s41, 10\n"
"s_add_u32 s53, s61, s1\n"
"s_add_u32 s41, s0, s53\n"
"s_add_u32 s1, s41, 0x1400000\n"
"buffer_load_dwordx4 a[140:143], v1, s[24:27], s40 offen offset:2048\n"
"s_add_u32 s0, s41, 0x1800000\n"
"s_mov_b32 s22, 0x1000000\n"
"s_mov_b32 s23, 0x20000\n"
"s_and_b32 s64, s49, 3\n"
"v_and_b32 v40, 3, v5\n"
"v_xor_b32 v5, s64, v40\n"
"v_and_b32 v8, 0xf0, v1\n"
"v_lshl_add_u32 v36, v5, 8, v8\n"
"s_mov_b32 s30, 0x200000\n"
"s_mov_b32 s31, 0x20000\n"
"v_mov_b32 v64, -1\n"
"v_mov_b32 v65, -1\n"
"buffer_load_dwordx4 a[144:147], v1, s[24:27], s40 offen offset:3072\n"
"v_mov_b32 v66, -1\n"
"v_mov_b32 v67, -1\n"
"v_sub_u32 v5, 0xff, v1\n"
"v_and_b32 v6, 0x80000000, v5\n"
"v_or_b32 v5, v1, v6\n"
"s_and_b32 s58, s55, 2\n"
"s_cmp_lt_u32 s60, 3\n"
"s_cselect_b32 s40, 0, s58\n"
"s_cmp_eq_u32 s40, 0\n"
"s_cselect_b32 s10, 0x1a00000, 0\n"
"s_mov_b32 s11, 0x20000\n"
"s_cmp_lt_u32 s55, 3\n"
"buffer_load_dwordx4 a[148:151], v1, s[24:27], s39 offen\n"
"s_cselect_b32 s14, 0x1a00000, 0\n"
"s_mov_b32 s15, 0x20000\n"
"s_mov_b32 s18, 0x3000000\n"
"s_mov_b32 s19, 0x20000\n"
"s_lshl_b32 s41, s40, 30\n"
"v_or_b32 v6, s41, v1\n"
"s_cmp_lt_u32 s55, 3\n"
"s_cselect_b32 s40, 0, 0x80000000\n"
"v_or_b32 v7, s40, v2\n"
"v_and_b32 v27, 48, v0\n"
"s_sub_u32 s40, s33, s35\n"
"s_mul_i32 s41, s40, 0x3f000\n"
"s_add_u32 s40, s41, 0x40000\n"
"buffer_load_dwordx4 a[152:155], v1, s[24:27], s39 offen offset:1024\n"
"v_lshl_add_u32 v9, v27, 4, s40\n"
"s_cmp_eq_u32 s35, 0\n"
"s_cselect_b32 s40, -16, 16\n"
"s_add_u32 s42, s33, 1\n"
"s_mul_i32 s41, s40, s42\n"
"s_add_u32 s40, s41, 0x100\n"
"v_add_u32 v10, s40, v8\n"
"v_lshrrev_b32 v32, 8, v10\n"
"v_and_b32 v33, 0xff, v10\n"
"v_lshl_add_u32 v10, v32, 15, v33\n"
"v_add_u32 v38, v9, v10\n"
"s_lshl_b32 s40, s55, 12\n"
"buffer_load_dwordx4 a[156:159], v1, s[24:27], s39 offen offset:2048\n"
"v_or_b32 v28, s3, v1\n"
"v_add_u32 v9, s40, v28\n"
"s_add_u32 s41, s55, 1\n"
"s_and_b32 s42, s41, 3\n"
"s_lshl_b32 s41, s42, 12\n"
"v_add_u32 v10, s41, v28\n"
"s_add_u32 s41, s55, 2\n"
"s_and_b32 s42, s41, 3\n"
"s_lshl_b32 s41, s42, 12\n"
"v_add_u32 v11, s41, v28\n"
"s_add_u32 s42, s55, 3\n"
"s_and_b32 s41, s42, 3\n"
"buffer_load_dwordx4 a[160:163], v1, s[24:27], s39 offen offset:3072\n"
"s_lshl_b32 s39, s41, 12\n"
"v_add_u32 v12, s39, v28\n"
"s_xor_b32 s41, s55, 1\n"
"s_mul_i32 s39, s41, 0x1800\n"
"v_add_u32 v13, s39, v28\n"
"s_xor_b32 s39, s55, 2\n"
"s_mul_i32 s41, s39, 0x1800\n"
"s_add_u32 s39, s41, 0x800\n"
"v_add_u32 v14, s39, v28\n"
"s_xor_b32 s39, s55, 3\n"
"s_mul_i32 s41, s39, 0x1800\n"
"s_add_u32 s39, s41, 0x1000\n"
"v_add_u32 v15, s39, v28\n"
"buffer_load_dwordx4 v[60:63], v1, s[24:27], s38 offen\n"
"s_mul_i32 s38, s55, 0x1800\n"
"v_add_u32 v16, s38, v28\n"
"s_lshl_b32 s38, s35, 8\n"
"s_lshl_b32 s66, s33, 3\n"
"s_add_u32 s41, s38, s66\n"
"s_add_u32 s39, s3, 0x14000\n"
"s_add_u32 s42, s41, s39\n"
"v_add_u32 v17, s42, v8\n"
"v_add_u32 v18, s39, v8\n"
"s_add_u32 s41, s38, s39\n"
"v_add_u32 v19, s41, v8\n"
"s_lshl_b32 s62, s55, 6\n"
"s_add_u32 s38, s3, 0x14400\n"
"buffer_load_dwordx4 a[0:3], v1, s[4:7], s36 offen\n"
"s_add_u32 s39, s62, s38\n"
"v_add_u32 v20, s39, v27\n"
"s_lshl_b32 s73, s33, 7\n"
"s_add_u32 s41, s73, s38\n"
"s_add_u32 s39, s41, 0x300\n"
"v_lshl_add_u32 v21, v40, 5, s39\n"
"s_lshl_b32 s39, s55, 7\n"
"s_add_u32 s41, s39, s38\n"
"s_add_u32 s39, s41, 0x400\n"
"v_lshl_add_u32 v22, v40, 5, s39\n"
"s_lshl_b32 s41, s35, 7\n"
"s_lshl_b32 s42, s33, 4\n"
"s_add_u32 s39, s41, s42\n"
"buffer_load_dwordx4 a[4:7], v1, s[4:7], s36 offen offset:1024\n"
"s_add_u32 s41, s39, s38\n"
"s_add_u32 s39, s41, 0x600\n"
"v_lshl_add_u32 v23, v40, 5, s39\n"
"s_add_u32 s39, s40, 0x10000\n"
"v_add_u32 v24, s39, v28\n"
"s_lshl_b32 s39, s33, 13\n"
"s_lshl_b32 s41, s35, 10\n"
"s_add_u32 s40, s39, s41\n"
"s_add_u32 s39, s40, 0x10000\n"
"v_add_u32 v25, s39, v28\n"
"s_mul_i32 s40, s55, 0x4c00\n"
"s_add_u32 s39, s40, s3\n"
"s_add_u32 s70, s39, 0x14c00\n"
"buffer_load_dwordx4 a[8:11], v1, s[4:7], s36 offen offset:2048\n"
"v_add_u32 v28, s70, v1\n"
"v_sub_u32 v29, 0, v39\n"
"v_and_b32 v30, 0x80000000, v29\n"
"v_or_b32 v29, v27, v30\n"
"v_and_b32 v42, 3, v0\n"
"v_lshrrev_b32 v41, 2, v26\n"
"v_xor_b32 v30, s32, v41\n"
"v_lshrrev_b32 v31, 3, v26\n"
"v_and_b32 v46, 4, v0\n"
"v_lshlrev_b32 v34, 10, v46\n"
"v_lshl_add_u32 v26, v31, 14, v34\n"
"buffer_load_dwordx4 a[12:15], v1, s[4:7], s36 offen offset:3072\n"
"v_lshl_add_u32 v31, v30, 8, v26\n"
"v_lshl_add_u32 v30, v42, 4, v31\n"
"s_cmp_eq_u32 s55, 0\n"
"s_cselect_b32 s3, 0, 0x80000000\n"
"v_lshlrev_b32 v31, 2, v42\n"
"v_lshl_add_u32 v26, v41, 8, v31\n"
"v_or_b32 v31, s3, v26\n"
"v_mov_b32 v34, v27\n"
"v_lshl_add_u32 v26, v39, 10, v27\n"
"v_or_b32 v35, 0x80000000, v26\n"
"v_mov_b32 v26, 0x7f\n"
"v_cmp_gt_u32 vcc, 0x7f, v0\n"
"buffer_load_dwordx4 a[16:19], v2, s[4:7], s36 offen\n"
"s_nop 0\n"
"v_cndmask_b32 v37, v26, v0, vcc\n"
"v_lshl_add_u32 v44, v37, 4, s38\n"
"v_lshlrev_b32 v26, 10, v42\n"
"v_lshl_add_u32 v37, v41, 15, v26\n"
"v_add_u32 v26, 0x3fc, v37\n"
"v_sub_u32 v37, 0x7f, v1\n"
"v_and_b32 v47, 0x80000000, v37\n"
"v_or_b32 v37, v26, v47\n"
"v_mov_b32 v47, 0x3f80\n"
"v_mov_b32 v48, 0x3f800000\n"
"buffer_load_dwordx4 a[20:23], v2, s[4:7], s36 offen offset:1024\n"
"v_lshlrev_b32 v26, 2, v27\n"
"v_sub_u32 v49, v8, v26\n"
"v_cmp_eq_u32 vcc, 16, v49\n"
"s_nop 1\n"
"v_cndmask_b32 v50, 0, v48, vcc\n"
"v_cmp_eq_u32 vcc, 0, v49\n"
"s_nop 1\n"
"v_cndmask_b32 v26, v50, v47, vcc\n"
"v_cmp_eq_u32 vcc, 48, v49\n"
"buffer_load_dwordx4 a[24:27], v2, s[4:7], s36 offen offset:2048\n"
"s_nop 0\n"
"v_cndmask_b32 v50, 0, v48, vcc\n"
"v_cmp_eq_u32 vcc, 32, v49\n"
"s_nop 1\n"
"v_cndmask_b32 v27, v50, v47, vcc\n"
"s_cmp_lt_u32 s60, 3\n"
"s_cselect_b32 s74, 0, -1\n"
"s_mov_b32 s75, s74\n"
"s_add_u32 s38, s36, 0x8000\n"
"s_add_u32 s39, s36, 0xc000\n"
"s_add_u32 s40, s36, 0x10000\n"
"s_add_u32 s41, s36, 0x14000\n"
"buffer_load_dwordx4 a[28:31], v2, s[4:7], s36 offen offset:3072\n"
"s_add_u32 s42, s36, 0x18000\n"
"s_add_u32 s43, s36, 0x1c000\n"
"s_add_u32 s44, s36, 0x20000\n"
"s_add_u32 s45, s36, 0x24000\n"
"s_add_u32 s46, s36, 0x28000\n"
"s_add_u32 s47, s36, 0x2c000\n"
"s_lshl_b32 s67, s60, 15\n"
"s_add_u32 s3, s67, 0x1a0000\n"
"s_lshr_b32 s68, s49, 3\n"
"s_lshl_b32 s69, s68, 10\n"
"s_add_u32 s63, s3, s69\n"
"s_lshl_b32 s69, s49, 4\n"
"s_lshl_b32 s77, s55, 2\n"
"buffer_load_dwordx4 v[80:83], v43, s[4:7], 0 offen\n"
"s_add_u32 s72, s69, s77\n"
"s_and_b32 s71, s72, 63\n"
"s_lshl_b32 s69, s71, 4\n"
"s_add_u32 s71, s63, s69\n"
"s_bfe_u32 s63, s49, 0x10002\n"
"s_lshl_b32 s69, s63, 3\n"
"s_add_u32 s63, s71, s69\n"
"s_add_u32 s71, s67, 0x1a1000\n"
"s_lshl_b32 s72, s68, 6\n"
"s_add_u32 s67, s71, s72\n"
"s_lshl_b32 s71, s64, 4\n"
"s_add_u32 s68, s67, s71\n"
"s_add_u32 s64, s68, s69\n"
"buffer_load_dwordx4 v[88:91], v43, s[4:7], s65 offen\n"
"s_lshl_b32 s65, s49, 1\n"
"s_add_u32 s67, s65, s35\n"
"s_lshl_b32 s65, s67, 14\n"
"s_add_u32 s67, s52, s33\n"
"s_lshl_b32 s52, s67, 6\n"
"s_add_u32 s67, s65, s52\n"
"s_mov_b32 s68, 0x1000\n"
"s_mov_b32 s69, s70\n"
"s_mov_b32 s70, 0x4000\n"
"s_mov_b32 s71, 0\n"
"s_mov_b32 s72, 0xffff0000\n"
"buffer_load_dwordx4 a[32:35], v3, s[4:7], s36 offen\n"
"buffer_load_dwordx4 a[40:43], v3, s[4:7], s36 offen offset:2048\n"
"buffer_load_dwordx4 a[48:51], v4, s[4:7], s36 offen\n"
"buffer_load_dwordx4 a[56:59], v4, s[4:7], s36 offen offset:2048\n"
"buffer_load_dwordx4 a[64:67], v1, s[4:7], s37 offen\n"
"buffer_load_dwordx4 a[72:75], v1, s[4:7], s37 offen offset:2048\n"
"s_waitcnt lgkmcnt(0)\n"
"s_waitcnt lgkmcnt(0)\n"
"s_min_u32 s84, s84, s88\n"
"s_min_u32 s85, s85, s89\n"
"buffer_load_dwordx4 a[80:83], v2, s[4:7], s37 offen\n"
"s_min_u32 s86, s86, s90\n"
"s_min_u32 s87, s87, s91\n"
"s_min_u32 s84, s84, s86\n"
"s_min_u32 s85, s85, s87\n"
"s_min_u32 s84, s84, s85\n"
"s_lshl_b32 s84, s84, 13\n"
"s_and_b32 s82, s84, 0x6000\n"
"s_and_b32 s76, s49, 11\n"
"s_lshl_b32 s78, s76, 16\n"
"s_lshr_b32 s86, s49, 1\n"
"s_xor_b32 s65, s49, s86\n"
"s_and_b32 s52, s65, 4\n"
"s_lshl_b32 s84, s52, 12\n"
"buffer_load_dwordx4 a[88:91], v2, s[4:7], s37 offen offset:2048\n"
"s_add_u32 s52, s34, s84\n"
"s_add_u32 s79, s78, s52\n"
"s_lshl_b32 s52, s2, 18\n"
"s_add_u32 s65, s79, s52\n"
"s_add_u32 s52, s65, s53\n"
"s_xor_b32 s52, s52, s82\n"
"s_and_b32 s65, s49, 8\n"
"s_lshl_b32 s83, s65, 13\n"
"s_and_b32 s53, s49, 2\n"
"s_lshl_b32 s85, s53, 14\n"
"s_add_u32 s53, s83, s85\n"
"s_and_b32 s85, s49, 4\n"
"s_lshl_b32 s83, s85, 12\n"
"buffer_load_dwordx4 a[96:99], v3, s[4:7], s37 offen\n"
"s_add_u32 s89, s34, s83\n"
"s_add_u32 s85, s53, s89\n"
"s_add_u32 s83, s85, s48\n"
"s_lshl_b32 s53, s60, 8\n"
"s_add_u32 s48, s53, s66\n"
"s_add_u32 s53, s83, s48\n"
"s_xor_b32 s53, s53, s82\n"
"s_lshl_b32 s48, s35, 18\n"
"s_add_u32 s66, s79, s48\n"
"s_add_u32 s48, s66, s51\n"
"s_xor_b32 s48, s48, s82\n"
"s_add_u32 s51, s83, s57\n"
"s_xor_b32 s51, s51, s82\n"
"buffer_load_dwordx4 a[104:107], v3, s[4:7], s37 offen offset:2048\n"
"s_lshl_b32 s57, s76, 17\n"
"s_bfe_u32 s76, s60, 0x10001\n"
"s_lshl_b32 s79, s76, 19\n"
"s_add_u32 s66, s57, s79\n"
"s_lshr_b32 s79, s60, 1\n"
"s_and_b32 s83, s79, 2\n"
"s_lshl_b32 s85, s83, 14\n"
"s_add_u32 s83, s66, s85\n"
"s_xor_b32 s85, s54, 16\n"
"s_lshl_b32 s66, s85, 9\n"
"s_lshl_b32 s54, s79, 2\n"
"s_xor_b32 s85, s49, s54\n"
"s_and_b32 s54, s85, 4\n"
"buffer_load_dwordx4 a[112:115], v4, s[4:7], s37 offen\n"
"s_lshl_b32 s87, s54, 12\n"
"s_add_u32 s85, s66, s87\n"
"s_add_u32 s54, s83, s85\n"
"s_add_u32 s85, s54, 0xe00000\n"
"s_and_b32 s83, s60, 1\n"
"s_lshl_b32 s87, s83, 2\n"
"s_add_u32 s54, s87, s55\n"
"s_lshl_b32 s87, s54, 10\n"
"s_add_u32 s54, s85, s87\n"
"s_xor_b32 s54, s54, s82\n"
"s_lshl_b32 s85, s56, 1\n"
"s_add_u32 s56, s85, s55\n"
"s_mul_i32 s85, s56, 43\n"
"buffer_load_dwordx4 a[120:123], v4, s[4:7], s37 offen offset:2048\n"
"s_bfe_u32 s87, s85, 0x10008\n"
"s_lshl_b32 s55, s87, 18\n"
"s_add_u32 s87, s78, s55\n"
"s_lshr_b32 s55, s85, 8\n"
"s_and_b32 s88, s55, 2\n"
"s_lshl_b32 s85, s88, 14\n"
"s_add_u32 s88, s87, s85\n"
"s_add_u32 s85, s66, s84\n"
"s_add_u32 s87, s88, s85\n"
"s_add_u32 s84, s87, 0x1600000\n"
"s_lshl_b32 s87, s56, 10\n"
"s_mul_i32 s88, s55, 0x1800\n"
"s_sub_u32 s56, s87, s88\n"
"buffer_load_dwordx4 a[36:39], v3, s[4:7], s36 offen offset:1024\n"
"s_add_u32 s55, s84, s56\n"
"s_xor_b32 s55, s55, s82\n"
"s_lshl_b32 s56, s33, 19\n"
"s_add_u32 s87, s57, s56\n"
"s_lshl_b32 s84, s58, 14\n"
"s_add_u32 s56, s87, s84\n"
"s_xor_b32 s58, s49, s77\n"
"s_and_b32 s57, s58, 4\n"
"s_lshl_b32 s58, s57, 12\n"
"s_add_u32 s57, s66, s58\n"
"s_add_u32 s58, s56, s57\n"
"s_add_u32 s57, s58, 0xe00000\n"
"s_lshl_b32 s58, s35, 16\n"
"buffer_load_dwordx4 a[44:47], v3, s[4:7], s36 offen offset:3072\n"
"s_add_u32 s56, s57, s58\n"
"s_xor_b32 s56, s56, s82\n"
"s_xor_b32 s57, s56, 0x10000\n"
"s_lshl_b32 s58, s33, 18\n"
"s_add_u32 s77, s78, s58\n"
"s_add_u32 s58, s77, s84\n"
"s_add_u32 s77, s58, s85\n"
"s_add_u32 s58, s77, 0x1600000\n"
"s_xor_b32 s58, s58, s82\n"
"s_add_u32 s77, s52, 0x400000\n"
"s_lshl_b32 s84, s60, 18\n"
"s_lshl_b32 s78, s86, 14\n"
"s_add_u32 s85, s84, s78\n"
"buffer_load_dwordx4 a[52:55], v4, s[4:7], s36 offen offset:1024\n"
"s_add_u32 s78, s85, s59\n"
"s_lshl_b32 s87, s35, 4\n"
"s_xor_b32 s85, s87, 16\n"
"s_lshl_b32 s59, s85, 9\n"
"s_and_b32 s85, s82, 0x2000\n"
"s_xor_b32 s90, s59, s85\n"
"s_add_u32 s88, s78, s90\n"
"s_lshl_b32 s90, s33, 1\n"
"s_bfe_u32 s78, s49, 0x20002\n"
"s_xor_b32 s59, s90, s78\n"
"s_lshl_b32 s78, s59, 10\n"
"s_add_u32 s59, s88, s78\n"
"s_xor_b32 s78, s59, 0x400\n"
"buffer_load_dwordx4 a[60:63], v4, s[4:7], s36 offen offset:3072\n"
"s_lshl_b32 s88, s49, 8\n"
"s_add_u32 s91, s88, 0x180000\n"
"v_and_b32 v47, 1, v0\n"
"v_and_b32 v43, 2, v0\n"
"v_lshlrev_b32 v48, 12, v43\n"
"v_xor_b32 v49, s85, v48\n"
"v_lshl_add_u32 v48, v41, 14, v49\n"
"v_lshl_add_u32 v41, v47, 7, v48\n"
"v_add_u32 v47, s91, v41\n"
"v_sub_u32 v41, 0x1ff, v1\n"
"v_and_b32 v48, 0x80000000, v41\n"
"buffer_load_dwordx4 a[68:71], v1, s[4:7], s37 offen offset:1024\n"
"v_or_b32 v41, v47, v48\n"
"s_lshr_b32 s91, s49, 2\n"
"s_lshl_b32 s92, s91, 15\n"
"s_add_u32 s91, s92, s89\n"
"s_and_b32 s92, s86, 1\n"
"s_lshl_b32 s89, s92, 12\n"
"s_add_u32 s86, s91, s89\n"
"s_lshl_b32 s89, s50, 2\n"
"s_add_u32 s91, s90, s35\n"
"s_add_u32 s50, s89, s91\n"
"s_lshl_b32 s89, s50, 7\n"
"s_add_u32 s50, s86, s89\n"
"s_xor_b32 s86, s50, s82\n"
"buffer_load_dwordx4 a[76:79], v1, s[4:7], s37 offen offset:3072\n"
"s_add_u32 s50, s48, s61\n"
"s_sub_u32 s89, s86, s50\n"
"s_add_u32 s61, s89, 0x188000\n"
"s_cmp_eq_u32 s35, 0\n"
"s_cselect_b64 vcc, -1, 0\n"
"v_cndmask_b32 v47, v33, v8, vcc\n"
"v_lshl_add_u32 v8, v40, 5, v47\n"
"v_add_u32 v33, s61, v8\n"
"v_cmp_eq_u32 vcc, 1, v32\n"
"s_waitcnt vmcnt(36)\n"
"ds_write_b128 v9, a[132:135]\n"
"buffer_load_dwordx4 a[84:87], v2, s[4:7], s37 offen offset:1024\n"
"v_cndmask_b32 v8, v33, v38, vcc\n"
"v_and_b32 v32, 16, v0\n"
"v_lshlrev_b32 v33, 27, v32\n"
"v_bfe_u32 v32, v0, 5, 1\n"
"v_and_b32 v48, 8, v0\n"
"v_lshlrev_b32 v47, 10, v48\n"
"v_xor_b32 v48, s85, v47\n"
"v_lshl_add_u32 v47, v46, 12, v48\n"
"v_lshl_add_u32 v46, v42, 4, v47\n"
"v_lshl_add_u32 v42, v32, 2, v46\n"
"v_or_b32 v32, v33, v42\n"
"buffer_load_dwordx4 a[92:95], v2, s[4:7], s37 offen offset:3072\n"
"v_and_b32 v42, 0xf1, v0\n"
"v_sub_u32 v33, 0, v42\n"
"v_and_b32 v0, 0x80000000, v33\n"
"v_lshl_add_u32 v42, v43, 1, v47\n"
"v_or_b32 v33, v0, v42\n"
"s_cmp_eq_u32 s76, 0\n"
"s_cselect_b32 s61, 33, 31\n"
"s_add_u32 s86, s49, s61\n"
"s_lshr_b32 s61, s86, 5\n"
"s_lshl_b32 s49, s61, 22\n"
"s_and_b32 s61, s86, 31\n"
"s_lshr_b32 s89, s61, 2\n"
"buffer_load_dwordx4 a[100:103], v3, s[4:7], s37 offen offset:1024\n"
"s_lshl_b32 s86, s89, 15\n"
"s_and_b32 s90, s61, 16\n"
"s_lshl_b32 s89, s90, 9\n"
"s_and_b32 s90, s61, 4\n"
"s_lshl_b32 s91, s90, 12\n"
"s_add_u32 s90, s89, s91\n"
"s_add_u32 s89, s86, s90\n"
"s_bfe_u32 s86, s61, 0x10001\n"
"s_lshl_b32 s90, s86, 12\n"
"s_add_u32 s86, s89, s90\n"
"s_and_b32 s89, s61, 1\n"
"s_lshl_b32 s61, s89, 2\n"
"s_add_u32 s89, s61, s79\n"
"buffer_load_dwordx4 a[108:111], v3, s[4:7], s37 offen offset:3072\n"
"s_lshl_b32 s61, s89, 7\n"
"s_waitcnt vmcnt(31)\n"
"v_mfma_f32_16x16x32_bf16 v[72:75], a[0:3], a[132:135], 0\n"
"s_add_u32 s89, s86, s61\n"
"s_xor_b32 s79, s89, s82\n"
"v_mfma_f32_16x16x32_bf16 v[84:87], a[0:3], a[148:151], 0\n"
"s_add_u32 s61, s49, s79\n"
"s_lshl_b32 s49, s83, 1\n"
"s_add_u32 s79, s49, s33\n"
"s_lshl_b32 s49, s79, 10\n"
"s_add_u32 s79, s61, s49\n"
"s_add_u32 s49, s79, 0x140000\n"
"buffer_load_dwordx4 a[116:119], v4, s[4:7], s37 offen offset:1024\n"
"s_add_u32 s61, s2, -15\n"
"s_waitcnt vmcnt(31)\n"
"v_mfma_f32_16x16x32_bf16 v[68:71], a[4:7], a[132:135], 0\n"
"s_cmp_eq_u32 s76, 0\n"
"s_cselect_b32 s79, s61, 0\n"
"v_mfma_f32_16x16x32_bf16 v[76:79], a[4:7], a[148:151], 0\n"
"v_add_u32 v0, s79, v39\n"
"v_lshlrev_b32 v42, 4, v0\n"
"v_lshl_add_u32 v39, v40, 5, v42\n"
"v_add_u32 v42, s49, v39\n"
"v_mov_b32 v43, 0x80000000\n"
"buffer_load_dwordx4 a[124:127], v4, s[4:7], s37 offen offset:3072\n"
"s_add_u32 s49, s2, 1\n"
"s_waitcnt vmcnt(31)\n"
"v_mfma_f32_16x16x32_bf16 v[72:75], a[8:11], a[136:139], v[72:75]\n"
"s_cmp_eq_u32 s35, s76\n"
"s_cselect_b32 s2, 0, s49\n"
"v_mfma_f32_16x16x32_bf16 v[84:87], a[8:11], a[152:155], v[84:87]\n"
"v_cmp_gt_u32 vcc, s2, v0\n"
"v_add_f32 v0, v60, v62\n"
"v_mov_b32 v40, v0\n"
"v_cndmask_b32 v39, v43, v42, vcc\n"
"s_mov_b32 s49, s50\n"
"buffer_load_dwordx2 a[128:129], v45, s[4:7], 0 offen\n"
"s_xor_b32 s2, s35, 1\n"
"s_waitcnt vmcnt(31)\n"
"v_mfma_f32_16x16x32_bf16 v[68:71], a[12:15], a[136:139], v[68:71]\n"
"s_lshl_b32 s35, s2, 15\n"
"s_add_u32 s50, s48, s35\n"
"v_mfma_f32_16x16x32_bf16 v[76:79], a[12:15], a[152:155], v[76:79]\n"
"s_lshl_b32 s35, s60, 14\n"
"s_add_u32 s2, s35, 0x180000\n"
"s_lshl_b32 s60, s87, 9\n"
"s_xor_b32 s35, s60, s85\n"
"s_add_u32 s60, s2, s35\n"
"s_add_u32 s35, s60, s88\n"
"buffer_load_dwordx4 v[48:51], v1, s[24:27], s1 offen\n"
"s_add_u32 s60, s35, s73\n"
"s_waitcnt vmcnt(31)\n"
"v_mfma_f32_16x16x32_bf16 v[72:75], a[16:19], a[140:143], v[72:75]\n"
"s_xor_b32 s1, s66, s85\n"
"v_permlane16_swap_b32 v0, v40\n"
"v_mfma_f32_16x16x32_bf16 v[84:87], a[16:19], a[156:159], v[84:87]\n"
"s_add_u32 s35, s84, s1\n"
"v_add_f32 v42, v0, v40\n"
"s_add_u32 s61, s35, s62\n"
"v_mov_b32 v0, v42\n"
"s_xor_b32 s1, s34, s85\n"
"s_add_u32 s34, s2, s1\n"
"buffer_load_dwordx4 v[52:55], v1, s[24:27], s0 offen\n"
"v_permlane32_swap_b32 v42, v0\n"
"s_waitcnt vmcnt(31)\n"
"v_mfma_f32_16x16x32_bf16 v[68:71], a[20:23], a[140:143], v[68:71]\n"
"s_lshl_b32 s0, s32, 4\n"
"s_add_u32 s62, s34, s0\n"
"v_mfma_f32_16x16x32_bf16 v[76:79], a[20:23], a[156:159], v[76:79]\n"
"v_add_f32 v40, v42, v0\n"
"s_lshl_b32 s0, s65, 11\n"
"v_mul_f32 v42, 0x3e000000, v40\n"
"s_add_u32 s2, s3, s0\n"
"v_sub_f32 v0, v60, v42\n"
"s_add_u32 s0, s2, s1\n"
"buffer_store_dwordx4 v[64:67], v1, s[20:23], s52 offen sc1\n"
"s_waitcnt vmcnt(31)\n"
"v_mfma_f32_16x16x32_bf16 v[72:75], a[24:27], a[144:147], v[72:75]\n"
"v_sub_f32 v45, v62, v42\n"
"v_mul_f32 v40, v0, v0\n"
"v_mfma_f32_16x16x32_bf16 v[84:87], a[24:27], a[160:163], v[84:87]\n"
"s_lshl_b32 s1, s33, 11\n"
"v_fmac_f32 v40, v45, v45\n"
"v_add_f32 v45, v61, v63\n"
"s_add_u32 s65, s0, s1\n"
"v_fmamk_f32 v0, v40, 0x42000000, v45\n"
"s_add_u32 s1, s0, 0x1000\n"
"buffer_store_dwordx4 v[64:67], v1, s[20:23], s77 offen sc1\n"
"s_waitcnt vmcnt(31)\n"
"v_mfma_f32_16x16x32_bf16 v[68:71], a[28:31], a[144:147], v[68:71]\n"
"v_mov_b32 v45, v0\n"
"s_add_u32 s66, s1, s73\n"
"v_mfma_f32_16x16x32_bf16 v[76:79], a[28:31], a[160:163], v[76:79]\n"
"s_add_u32 s2, s52, 0x800000\n"
"v_permlane16_swap_b32 v0, v45\n"
"v_add_f32 v40, v0, v45\n"
"s_add_u32 s0, s53, 0x900000\n"
"v_mov_b32 v0, v40\n"
"buffer_store_dwordx4 v[64:67], v36, s[28:31], s59 offen sc1\n"
"s_add_u32 s1, s52, 0xc00000\n"
"v_permlane32_swap_b32 v40, v0\n"
"s_add_u32 s3, s53, 0xd00000\n"
"v_add_f32 v43, v40, v0\n"
"ds_write_b128 v9, a[136:139] offset:1024\n"
"ds_write_b128 v9, a[140:143] offset:2048\n"
"buffer_store_dwordx4 v[64:67], v36, s[28:31], s78 offen sc1\n"
"s_waitcnt vmcnt(31)\n"
"v_pk_add_f32 v[80:81], v[80:81], v[88:89]\n"
"v_pk_add_f32 v[82:83], v[82:83], v[90:91]\n"
"ds_write_b128 v44, v[80:83]\n"
"s_add_u32 s28, s4, 0xe00000\n"
"s_addc_u32 s29, s5, 0\n"
"ds_write_b128 v9, a[144:147] offset:3072\n"
"buffer_store_dwordx4 v[56:59], v1, s[20:23], s2 offen sc1\n"
"ds_write_b128 v9, a[148:151] offset:16384\n"
"ds_write_b128 v9, a[152:155] offset:17408\n"
"ds_write_b128 v9, a[156:159] offset:18432\n"
"buffer_store_dwordx2 v[56:57], v5, s[20:23], s0 offen sc1\n"
"ds_write_b128 v9, a[160:163] offset:19456\n"
"ds_write_b64 v17, v[42:43]\n"
"buffer_store_dwordx4 v[56:59], v1, s[20:23], s1 offen sc1\n"
"buffer_store_dwordx2 v[56:57], v5, s[20:23], s3 offen sc1\n"
"s_branch CkvA%=\n"
"LOOP%=:\n"
"s_waitcnt vmcnt(17)\n"
"v_mfma_f32_16x16x32_bf16 v[72:75], a[0:3], a[132:135], 0\n"
"s_waitcnt vmcnt(9)\n"
"v_add_f32 v40, v60, v62\n"
"v_mov_b32 v43, v40\n"
"v_mfma_f32_16x16x32_bf16 v[84:87], a[0:3], a[148:151], 0\n"
"v_add_f32 v0, v61, v63\n"
"v_permlane16_swap_b32 v40, v43\n"
"v_mfma_f32_16x16x32_bf16 v[72:75], a[8:11], a[136:139], v[72:75]\n"
"v_add_f32 v42, v40, v43\n"
"v_mov_b32 v40, v42\n"
"v_mfma_f32_16x16x32_bf16 v[84:87], a[8:11], a[152:155], v[84:87]\n"
"v_mov_b32 v44, -1\n"
"v_permlane32_swap_b32 v42, v40\n"
"v_mfma_f32_16x16x32_bf16 v[72:75], a[16:19], a[140:143], v[72:75]\n"
"v_add_f32 v56, v42, v40\n"
"v_mul_f32 v42, 0x3e000000, v56\n"
"v_mfma_f32_16x16x32_bf16 v[84:87], a[16:19], a[156:159], v[84:87]\n"
"v_sub_f32 v40, v60, v42\n"
"v_sub_f32 v56, v62, v42\n"
"v_mfma_f32_16x16x32_bf16 v[68:71], a[4:7], a[132:135], 0\n"
"v_mul_f32 v57, v40, v40\n"
"v_fmac_f32 v57, v56, v56\n"
"v_mfma_f32_16x16x32_bf16 v[76:79], a[4:7], a[148:151], 0\n"
"v_fmamk_f32 v40, v57, 0x42000000, v0\n"
"v_mov_b32 v0, v40\n"
"v_mfma_f32_16x16x32_bf16 v[72:75], a[24:27], a[144:147], v[72:75]\n"
"v_mov_b32 v45, -1\n"
"v_mov_b32 v46, -1\n"
"v_mfma_f32_16x16x32_bf16 v[84:87], a[24:27], a[160:163], v[84:87]\n"
"v_mov_b32 v47, -1\n"
"s_add_u32 s0, s52, 0x800000\n"
"v_mfma_f32_16x16x32_bf16 v[68:71], a[12:15], a[136:139], v[68:71]\n"
"v_permlane16_swap_b32 v40, v0\n"
"buffer_store_dwordx4 v[44:47], v1, s[20:23], s0 offen sc1\n"
"v_mfma_f32_16x16x32_bf16 v[76:79], a[12:15], a[152:155], v[76:79]\n"
"v_add_f32 v56, v40, v0\n"
"s_add_u32 s1, s53, 0x900000\n"
"v_mfma_f32_16x16x32_bf16 v[68:71], a[20:23], a[140:143], v[68:71]\n"
"v_mov_b32 v0, v56\n"
"s_add_u32 s0, s52, 0xc00000\n"
"v_mfma_f32_16x16x32_bf16 v[76:79], a[20:23], a[156:159], v[76:79]\n"
"v_permlane32_swap_b32 v56, v0\n"
"v_add_f32 v43, v56, v0\n"
"v_mfma_f32_16x16x32_bf16 v[68:71], a[28:31], a[144:147], v[68:71]\n"
"buffer_store_dwordx2 v[44:45], v5, s[20:23], s1 offen sc1\n"
"v_mfma_f32_16x16x32_bf16 v[76:79], a[28:31], a[160:163], v[76:79]\n"
"s_add_u32 s1, s53, 0xd00000\n"
"v_cmp_u_f32 vcc, v72, v84\n"
"v_cmp_u_f32 s[2:3], v43, v43\n"
"ds_write_b128 v9, a[132:135]\n"
"s_or_b64 vcc, vcc, s[2:3]\n"
"ds_write_b128 v9, a[136:139] offset:1024\n"
"buffer_store_dwordx4 v[44:47], v1, s[20:23], s0 offen sc1\n"
"ds_write_b128 v9, a[140:143] offset:2048\n"
"ds_write_b128 v9, a[144:147] offset:3072\n"
"ds_write_b128 v9, a[148:151] offset:16384\n"
"buffer_store_dwordx2 v[44:45], v5, s[20:23], s1 offen sc1\n"
"ds_write_b128 v9, a[152:155] offset:17408\n"
"ds_write_b128 v9, a[156:159] offset:18432\n"
"ds_write_b128 v9, a[160:163] offset:19456\n"
"ds_write_b64 v17, v[42:43]\n"
"s_cbranch_vccnz RkvA%=\n"
"CkvA%=:\n"
"s_waitcnt lgkmcnt(0)\n"
"s_barrier\n"
"buffer_load_dwordx4 v[224:227], v1, s[4:7], s38 offen\n"
"s_add_u32 s3, s80, 0x1800000\n"
"s_add_u32 s2, s80, 0x1808000\n"
"s_add_u32 s1, s81, 0x1900000\n"
"ds_read_b128 a[148:151], v10\n"
"v_mov_b32 v56, -1\n"
"v_mov_b32 v57, -1\n"
"v_mov_b32 v58, -1\n"
"ds_read_b128 a[152:155], v10 offset:16384\n"
"v_mov_b32 v59, -1\n"
"s_add_u32 s0, s52, 0x1000000\n"
"v_mov_b32 v44, 0x3f803f80\n"
"ds_read_b128 a[156:159], v10 offset:1024\n"
"v_mov_b32 v45, 0x3f803f80\n"
"v_mov_b32 v46, 0x3f803f80\n"
"buffer_load_dwordx4 v[228:231], v1, s[4:7], s38 offen offset:1024\n"
"ds_read_b128 a[160:163], v10 offset:17408\n"
"v_mov_b32 v47, 0x3f803f80\n"
"ds_read_b128 a[176:179], v10 offset:2048\n"
"ds_read_b128 a[172:175], v10 offset:18432\n"
"ds_read_b128 v[88:91], v10 offset:3072\n"
"s_waitcnt vmcnt(36) lgkmcnt(6)\n"
"v_mfma_f32_16x16x32_bf16 v[72:75], a[32:35], a[148:151], v[72:75]\n"
"buffer_load_dwordx4 v[232:235], v1, s[4:7], s38 offen offset:2048\n"
"s_waitcnt lgkmcnt(5)\n"
"v_mfma_f32_16x16x32_bf16 v[84:87], a[32:35], a[152:155], v[84:87]\n"
"ds_read_b128 a[220:223], v10 offset:19456\n"
"s_waitcnt vmcnt(36) lgkmcnt(5)\n"
"v_mfma_f32_16x16x32_bf16 v[72:75], a[40:43], a[156:159], v[72:75]\n"
"ds_read_b128 a[168:171], v11\n"
"s_waitcnt vmcnt(21)\n"
"v_mfma_f32_16x16x32_bf16 v[68:71], a[36:39], a[148:151], v[68:71]\n"
"ds_read_b128 a[216:219], v11 offset:16384\n"
"s_waitcnt lgkmcnt(6)\n"
"v_mfma_f32_16x16x32_bf16 v[84:87], a[40:43], a[160:163], v[84:87]\n"
"buffer_load_dwordx4 v[236:239], v1, s[4:7], s38 offen offset:3072\n"
"s_waitcnt lgkmcnt(5)\n"
"v_mfma_f32_16x16x32_bf16 v[72:75], a[48:51], a[176:179], v[72:75]\n"
"ds_read_b128 a[212:215], v11 offset:1024\n"
"s_waitcnt lgkmcnt(5)\n"
"v_mfma_f32_16x16x32_bf16 v[84:87], a[48:51], a[172:175], v[84:87]\n"
"ds_read_b128 a[208:211], v11 offset:17408\n"
"s_waitcnt lgkmcnt(5)\n"
"v_mfma_f32_16x16x32_bf16 v[72:75], a[56:59], v[88:91], v[72:75]\n"
"ds_read_b128 a[164:167], v11 offset:2048\n"
"v_mfma_f32_16x16x32_bf16 v[76:79], a[36:39], a[152:155], v[76:79]\n"
"ds_read_b128 a[204:207], v11 offset:18432\n"
"s_waitcnt lgkmcnt(6)\n"
"v_mfma_f32_16x16x32_bf16 v[84:87], a[56:59], a[220:223], v[84:87]\n"
"ds_read_b128 a[200:203], v11 offset:3072\n"
"buffer_load_dwordx4 a[196:199], v1, s[24:27], s3 offen sc1\n"
"s_waitcnt lgkmcnt(6)\n"
"v_mfma_f32_16x16x32_bf16 v[72:75], a[64:67], a[168:171], v[72:75]\n"
"ds_read_b128 a[192:195], v11 offset:19456\n"
"s_waitcnt lgkmcnt(6)\n"
"v_mfma_f32_16x16x32_bf16 v[84:87], a[64:67], a[216:219], v[84:87]\n"
"ds_read_b128 a[132:135], v12\n"
"s_waitcnt lgkmcnt(6)\n"
"v_mfma_f32_16x16x32_bf16 v[72:75], a[72:75], a[212:215], v[72:75]\n"
"ds_read_b128 a[136:139], v12 offset:16384\n"
"s_waitcnt lgkmcnt(6)\n"
"v_mfma_f32_16x16x32_bf16 v[84:87], a[72:75], a[208:211], v[84:87]\n"
"buffer_load_dwordx4 a[188:191], v1, s[24:27], s3 offen offset:1024 sc1\n"
"s_waitcnt lgkmcnt(5)\n"
"v_mfma_f32_16x16x32_bf16 v[72:75], a[80:83], a[164:167], v[72:75]\n"
"ds_read_b128 a[140:143], v12 offset:1024\n"
"s_waitcnt lgkmcnt(5)\n"
"v_mfma_f32_16x16x32_bf16 v[84:87], a[80:83], a[204:207], v[84:87]\n"
"ds_read_b128 a[144:147], v12 offset:17408\n"
"s_waitcnt lgkmcnt(5)\n"
"v_mfma_f32_16x16x32_bf16 v[72:75], a[88:91], a[200:203], v[72:75]\n"
"ds_read_b128 a[148:151], v12 offset:2048\n"
"s_waitcnt vmcnt(23)\n"
"v_mfma_f32_16x16x32_bf16 v[68:71], a[44:47], a[156:159], v[68:71]\n"
"buffer_load_dwordx4 a[184:187], v1, s[24:27], s3 offen offset:2048 sc1\n"
"s_waitcnt lgkmcnt(5)\n"
"v_mfma_f32_16x16x32_bf16 v[84:87], a[88:91], a[192:195], v[84:87]\n"
"ds_read_b128 a[152:155], v12 offset:18432\n"
"s_waitcnt lgkmcnt(5)\n"
"v_mfma_f32_16x16x32_bf16 v[72:75], a[96:99], a[132:135], v[72:75]\n"
"ds_read_b128 v[60:63], v18\n"
"s_waitcnt lgkmcnt(5)\n"
"v_mfma_f32_16x16x32_bf16 v[84:87], a[96:99], a[136:139], v[84:87]\n"
"ds_read_b128 v[80:83], v18 offset:256\n"
"v_mfma_f32_16x16x32_bf16 v[76:79], a[44:47], a[160:163], v[76:79]\n"
"buffer_load_dwordx4 a[180:183], v1, s[24:27], s3 offen offset:3072 sc1\n"
"s_waitcnt lgkmcnt(5)\n"
"v_mfma_f32_16x16x32_bf16 v[72:75], a[104:107], a[140:143], v[72:75]\n"
"ds_read_b128 a[156:159], v12 offset:3072\n"
"s_waitcnt lgkmcnt(5)\n"
"v_mfma_f32_16x16x32_bf16 v[84:87], a[104:107], a[144:147], v[84:87]\n"
"ds_read_b128 a[160:163], v12 offset:19456\n"
"s_waitcnt lgkmcnt(5)\n"
"v_mfma_f32_16x16x32_bf16 v[72:75], a[112:115], a[148:151], v[72:75]\n"
"ds_read_b128 v[64:67], v20 offset:256\n"
"s_waitcnt vmcnt(24)\n"
"v_mfma_f32_16x16x32_bf16 v[68:71], a[52:55], a[176:179], v[68:71]\n"
"buffer_load_dwordx4 a[176:179], v1, s[24:27], s2 offen sc1\n"
"s_waitcnt lgkmcnt(5)\n"
"v_mfma_f32_16x16x32_bf16 v[84:87], a[112:115], a[152:155], v[84:87]\n"
"s_waitcnt lgkmcnt(4)\n"
"v_sub_f32 v40, v60, v62\n"
"s_waitcnt lgkmcnt(3)\n"
"v_sub_f32 v42, v80, v82\n"
"v_mfma_f32_16x16x32_bf16 v[76:79], a[52:55], a[172:175], v[76:79]\n"
"v_mul_f32 v0, v40, v40\n"
"v_add_f32 v40, v61, v63\n"
"s_waitcnt vmcnt(24)\n"
"v_mfma_f32_16x16x32_bf16 v[68:71], a[60:63], v[88:91], v[68:71]\n"
"v_mul_f32 v43, v42, v42\n"
"v_add_f32 v42, v81, v83\n"
"buffer_load_dwordx4 a[172:175], v1, s[24:27], s2 offen offset:1024 sc1\n"
"s_waitcnt lgkmcnt(2)\n"
"v_mfma_f32_16x16x32_bf16 v[72:75], a[120:123], a[156:159], v[72:75]\n"
"v_fmamk_f32 v88, v0, 0x43000000, v40\n"
"v_fmamk_f32 v40, v43, 0x43000000, v42\n"
"s_waitcnt lgkmcnt(1)\n"
"v_mfma_f32_16x16x32_bf16 v[84:87], a[120:123], a[160:163], v[84:87]\n"
"v_mul_f32 v0, 0x3b000000, v88\n"
"v_mul_f32 v42, 0x3b000000, v40\n"
"v_mfma_f32_16x16x32_bf16 v[76:79], a[60:63], a[220:223], v[76:79]\n"
"v_add_f32 v0, 0x3727c5ac, v0\n"
"v_add_f32 v42, 0x3727c5ac, v42\n"
"s_waitcnt vmcnt(24)\n"
"v_mfma_f32_16x16x32_bf16 v[68:71], a[68:71], a[168:171], v[68:71]\n"
"buffer_load_dwordx4 a[168:171], v1, s[24:27], s2 offen offset:2048 sc1\n"
"v_mfma_f32_16x16x32_bf16 v[76:79], a[68:71], a[216:219], v[76:79]\n"
"v_rsq_f32 v40, v0\n"
"s_waitcnt vmcnt(24)\n"
"v_mfma_f32_16x16x32_bf16 v[68:71], a[76:79], a[212:215], v[68:71]\n"
"v_rsq_f32 v0, v42\n"
"v_mfma_f32_16x16x32_bf16 v[76:79], a[76:79], a[208:211], v[76:79]\n"
"v_add_f32 v42, v60, v62\n"
"v_add_f32 v43, v80, v82\n"
"s_waitcnt vmcnt(21)\n"
"v_mfma_f32_16x16x32_bf16 v[68:71], a[84:87], a[164:167], v[68:71]\n"
"buffer_load_dwordx4 a[164:167], v1, s[24:27], s2 offen offset:3072 sc1\n"
"v_mfma_f32_16x16x32_bf16 v[76:79], a[84:87], a[204:207], v[76:79]\n"
"v_mul_f32 v60, 0.5, v42\n"
"v_mul_f32 v88, 0.5, v43\n"
"s_waitcnt vmcnt(21)\n"
"v_mfma_f32_16x16x32_bf16 v[68:71], a[92:95], a[200:203], v[68:71]\n"
"v_mul_f32 v42, -v40, v60\n"
"v_mul_f32 v80, v72, v40\n"
"v_mfma_f32_16x16x32_bf16 v[76:79], a[92:95], a[192:195], v[76:79]\n"
"v_mul_f32 v81, v73, v40\n"
"v_mul_f32 v82, v74, v40\n"
"s_waitcnt vmcnt(19)\n"
"v_mfma_f32_16x16x32_bf16 v[68:71], a[100:103], a[132:135], v[68:71]\n"
"buffer_load_dwordx4 v[60:63], v1, s[24:27], s1 offen sc1\n"
"v_mfma_f32_16x16x32_bf16 v[76:79], a[100:103], a[136:139], v[76:79]\n"
"v_mul_f32 v83, v75, v40\n"
"v_mul_f32 v43, -v0, v88\n"
"s_waitcnt vmcnt(19)\n"
"v_mfma_f32_16x16x32_bf16 v[68:71], a[108:111], a[140:143], v[68:71]\n"
"v_mul_f32 v72, v84, v0\n"
"v_mul_f32 v73, v85, v0\n"
"v_mfma_f32_16x16x32_bf16 v[76:79], a[108:111], a[144:147], v[76:79]\n"
"v_mul_f32 v74, v86, v0\n"
"v_mul_f32 v75, v87, v0\n"
"s_waitcnt vmcnt(18)\n"
"v_mfma_f32_16x16x32_bf16 v[68:71], a[116:119], a[148:151], v[68:71]\n"
"buffer_store_dwordx4 v[56:59], v1, s[24:27], s0 offen sc1\n"
"v_mfma_f32_16x16x32_bf16 v[76:79], a[116:119], a[152:155], v[76:79]\n"
"s_waitcnt lgkmcnt(0)\n"
"v_fmac_f32 v80, v64, v42\n"
"v_fmac_f32 v81, v65, v42\n"
"s_waitcnt vmcnt(18)\n"
"v_mfma_f32_16x16x32_bf16 v[68:71], a[124:127], a[156:159], v[68:71]\n"
"v_fmac_f32 v82, v66, v42\n"
"v_fmac_f32 v83, v67, v42\n"
"v_mfma_f32_16x16x32_bf16 v[76:79], a[124:127], a[160:163], v[76:79]\n"
"v_fmac_f32 v72, v64, v43\n"
"v_fmac_f32 v73, v65, v43\n"
"v_fmac_f32 v74, v66, v43\n"
"buffer_store_dwordx4 v[56:59], v6, s[4:7], s55 offen sc1\n"
"v_fmac_f32 v75, v67, v43\n"
"s_nop 0\n"
"v_cvt_pk_bf16_f32 v56, v80, v81\n"
"v_cvt_pk_bf16_f32 v57, v82, v83\n"
"v_cvt_pk_bf16_f32 v80, v72, v73\n"
"v_cvt_pk_bf16_f32 v81, v74, v75\n"
"s_waitcnt vmcnt(25)\n"
"v_mfma_f32_16x16x16_bf16 v[88:91], a[128:129], v[56:57], 0\n"
"ds_read_b128 v[56:59], v20 offset:512\n"
"v_mul_f32 v72, v68, v40\n"
"v_mfma_f32_16x16x16_bf16 v[64:67], a[128:129], v[80:81], 0\n"
"v_mul_f32 v73, v69, v40\n"
"v_mul_f32 v74, v70, v40\n"
"buffer_load_dwordx4 v[240:243], v2, s[4:7], s38 offen\n"
"v_mul_f32 v75, v71, v40\n"
"v_mul_f32 v68, v76, v0\n"
"v_max_f32 v69, v88, v89\n"
"v_min_f32 v71, v88, v89\n"
"v_max3_f32 v69, v69, v90, v91\n"
"v_max_f32 v70, v64, v65\n"
"v_min_f32 v82, v64, v65\n"
"v_max3_f32 v81, -v71, -v90, -v91\n"
"v_max3_f32 v70, v70, v66, v67\n"
"v_max3_f32 v80, -v82, -v66, -v67\n"
"v_permlane16_swap_b32 v69, v81\n"
"v_max_f32 v71, v69, v81\n"
"v_permlane16_swap_b32 v70, v80\n"
"buffer_load_dwordx4 v[244:247], v2, s[4:7], s38 offen offset:1024\n"
"v_max_f32 v82, v70, v80\n"
"v_mul_f32 v70, v77, v0\n"
"v_mul_f32 v69, v78, v0\n"
"v_permlane32_swap_b32 v71, v82\n"
"v_max_f32 v81, v71, v82\n"
"v_mul_f32 v81, 0xbfb8aa3b, v81\n"
"v_mov_b32 v83, v81\n"
"v_mul_f32 v71, v79, v0\n"
"s_waitcnt lgkmcnt(0)\n"
"v_fmac_f32 v72, v56, v42\n"
"v_permlane16_swap_b32 v81, v83\n"
"v_mov_b32 v84, v81\n"
"v_mov_b32 v92, v83\n"
"v_fmac_f32 v73, v57, v42\n"
"buffer_load_dwordx4 v[248:251], v2, s[4:7], s38 offen offset:2048\n"
"v_permlane32_swap_b32 v81, v84\n"
"v_permlane32_swap_b32 v83, v92\n"
"v_fmamk_f32 v76, v88, 0x3fb8aa3b, v81\n"
"v_fmamk_f32 v77, v89, 0x3fb8aa3b, v81\n"
"v_fmamk_f32 v78, v90, 0x3fb8aa3b, v81\n"
"v_fmamk_f32 v80, v91, 0x3fb8aa3b, v81\n"
"v_fmamk_f32 v79, v88, 0xbfb8aa3b, v83\n"
"v_fmamk_f32 v81, v89, 0xbfb8aa3b, v83\n"
"v_fmamk_f32 v82, v90, 0xbfb8aa3b, v83\n"
"v_fmamk_f32 v85, v91, 0xbfb8aa3b, v83\n"
"v_fmamk_f32 v83, v64, 0x3fb8aa3b, v84\n"
"v_fmamk_f32 v86, v65, 0x3fb8aa3b, v84\n"
"v_fmamk_f32 v87, v66, 0x3fb8aa3b, v84\n"
"v_fmamk_f32 v90, v67, 0x3fb8aa3b, v84\n"
"buffer_load_dwordx4 v[252:255], v2, s[4:7], s38 offen offset:3072\n"
"v_fmamk_f32 v84, v64, 0xbfb8aa3b, v92\n"
"v_fmamk_f32 v88, v65, 0xbfb8aa3b, v92\n"
"v_fmamk_f32 v89, v66, 0xbfb8aa3b, v92\n"
"v_fmamk_f32 v91, v67, 0xbfb8aa3b, v92\n"
"v_exp_f32 v76, v76\n"
"v_exp_f32 v77, v77\n"
"v_exp_f32 v78, v78\n"
"v_exp_f32 v80, v80\n"
"v_exp_f32 v79, v79\n"
"buffer_load_dwordx4 a[252:255], v3, s[4:7], s38 offen\n"
"v_exp_f32 v81, v81\n"
"v_exp_f32 v82, v82\n"
"v_exp_f32 v85, v85\n"
"v_exp_f32 v83, v83\n"
"v_exp_f32 v86, v86\n"
"v_exp_f32 v87, v87\n"
"v_exp_f32 v90, v90\n"
"buffer_load_dwordx4 a[248:251], v3, s[4:7], s38 offen offset:1024\n"
"v_exp_f32 v84, v84\n"
"v_exp_f32 v88, v88\n"
"v_exp_f32 v89, v89\n"
"v_exp_f32 v91, v91\n"
"v_add_f32 v64, v76, v77\n"
"v_add_f32 v92, v78, v80\n"
"v_add_f32 v65, v79, v81\n"
"v_add_f32 v93, v82, v85\n"
"v_add_f32 v66, v83, v86\n"
"v_add_f32 v94, v87, v90\n"
"buffer_load_dwordx4 a[244:247], v3, s[4:7], s38 offen offset:2048\n"
"v_add_f32 v67, v84, v88\n"
"v_add_f32 v95, v89, v91\n"
"v_add_f32 v64, v64, v92\n"
"v_add_f32 v65, v65, v93\n"
"v_add_f32 v66, v66, v94\n"
"v_add_f32 v67, v67, v95\n"
"v_permlane16_swap_b32 v64, v65\n"
"v_add_f32 v92, v64, v65\n"
"v_permlane16_swap_b32 v66, v67\n"
"v_add_f32 v64, v66, v67\n"
"v_fmac_f32 v74, v58, v42\n"
"v_fmac_f32 v75, v59, v42\n"
"v_permlane32_swap_b32 v92, v64\n"
"buffer_load_dwordx4 a[240:243], v3, s[4:7], s38 offen offset:3072\n"
"v_add_f32 v95, v92, v64\n"
"v_rcp_f32 v95, v95\n"
"v_fmac_f32 v68, v56, v43\n"
"v_mov_b32 v94, v95\n"
"v_fmac_f32 v70, v57, v43\n"
"v_fmac_f32 v69, v58, v43\n"
"v_permlane16_swap_b32 v95, v94\n"
"v_mov_b32 v93, v95\n"
"v_mov_b32 v92, v94\n"
"v_fmac_f32 v71, v59, v43\n"
"v_permlane32_swap_b32 v95, v93\n"
"v_permlane32_swap_b32 v94, v92\n"
"buffer_load_dwordx4 a[152:155], v4, s[4:7], s38 offen\n"
"s_waitcnt vmcnt(19)\n"
"v_mfma_f32_16x16x32_bf16 v[64:67], a[0:3], a[196:199], 0\n"
"v_mul_f32 v76, v76, v95\n"
"v_mul_f32 v77, v77, v95\n"
"v_mfma_f32_16x16x32_bf16 v[56:59], a[4:7], a[196:199], 0\n"
"v_mul_f32 v78, v78, v95\n"
"v_mul_f32 v80, v80, v95\n"
"v_mul_f32 v79, v79, v94\n"
"v_mul_f32 v81, v81, v94\n"
"v_mul_f32 v82, v82, v94\n"
"v_mul_f32 v85, v85, v94\n"
"v_mul_f32 v83, v83, v93\n"
"v_mul_f32 v86, v86, v93\n"
"buffer_load_dwordx4 a[148:151], v4, s[4:7], s38 offen offset:1024\n"
"v_mul_f32 v87, v87, v93\n"
"v_mul_f32 v90, v90, v93\n"
"s_waitcnt vmcnt(19)\n"
"v_mfma_f32_16x16x32_bf16 v[64:67], a[8:11], a[188:191], v[64:67]\n"
"v_mul_f32 v84, v84, v92\n"
"v_mul_f32 v88, v88, v92\n"
"v_mfma_f32_16x16x32_bf16 v[56:59], a[12:15], a[188:191], v[56:59]\n"
"v_mul_f32 v89, v89, v92\n"
"v_mul_f32 v91, v91, v92\n"
"v_cvt_pk_bf16_f32 v92, v72, v73\n"
"v_cvt_pk_bf16_f32 v93, v74, v75\n"
"v_cvt_pk_bf16_f32 v100, v68, v70\n"
"buffer_load_dwordx4 a[156:159], v4, s[4:7], s38 offen offset:2048\n"
"v_mfma_f32_16x16x16_bf16 v[72:75], v[92:93], v[26:27], 0\n"
"v_cvt_pk_bf16_f32 v101, v69, v71\n"
"v_cvt_pk_bf16_f32 v96, v76, v77\n"
"v_cvt_pk_bf16_f32 v97, v78, v80\n"
"s_waitcnt vmcnt(19)\n"
"v_mfma_f32_16x16x32_bf16 v[64:67], a[16:19], a[184:187], v[64:67]\n"
"v_cvt_pk_bf16_f32 v98, v79, v81\n"
"v_cvt_pk_bf16_f32 v99, v82, v85\n"
"v_mfma_f32_16x16x16_bf16 v[92:95], v[100:101], v[26:27], 0\n"
"v_cvt_pk_bf16_f32 v100, v83, v86\n"
"v_cvt_pk_bf16_f32 v101, v87, v90\n"
"v_mfma_f32_16x16x16_bf16 v[80:83], v[96:97], v[26:27], 0\n"
"buffer_load_dwordx4 a[160:163], v4, s[4:7], s38 offen offset:3072\n"
"v_mfma_f32_16x16x16_bf16 v[68:71], v[100:101], v[26:27], 0\n"
"v_cvt_pk_bf16_f32 v102, v84, v88\n"
"v_cvt_pk_bf16_f32 v103, v89, v91\n"
"v_mfma_f32_16x16x16_bf16 v[88:91], v[98:99], v[26:27], 0\n"
"v_cvt_pk_bf16_f32 v76, v72, v73\n"
"v_cvt_pk_bf16_f32 v77, v74, v75\n"
"v_mfma_f32_16x16x16_bf16 v[84:87], v[102:103], v[26:27], 0\n"
"v_cvt_pk_bf16_f32 v78, v92, v93\n"
"v_cvt_pk_bf16_f32 v79, v94, v95\n"
"s_waitcnt vmcnt(19)\n"
"v_mfma_f32_16x16x32_bf16 v[64:67], a[24:27], a[180:183], v[64:67]\n"
"buffer_load_dwordx4 a[136:139], v1, s[4:7], s39 offen\n"
"v_mfma_f32_16x16x32_bf16 v[56:59], a[20:23], a[184:187], v[56:59]\n"
"v_cvt_pk_bf16_f32 v92, v80, v81\n"
"v_cvt_pk_bf16_f32 v93, v82, v83\n"
"v_mfma_f32_16x16x32_bf16 v[56:59], a[28:31], a[180:183], v[56:59]\n"
"v_cvt_pk_bf16_f32 v94, v68, v69\n"
"v_cvt_pk_bf16_f32 v95, v70, v71\n"
"v_cvt_pk_bf16_f32 v80, v88, v89\n"
"s_waitcnt vmcnt(19)\n"
"v_mfma_f32_16x16x32_bf16 v[72:75], a[0:3], a[176:179], 0\n"
"v_cvt_pk_bf16_f32 v81, v90, v91\n"
"v_cvt_pk_bf16_f32 v82, v84, v85\n"
"v_mfma_f32_16x16x32_bf16 v[68:71], a[4:7], a[176:179], 0\n"
"buffer_load_dwordx4 a[132:135], v1, s[4:7], s39 offen offset:1024\n"
"v_mfma_f32_16x16x32_bf16 a[192:195], v[92:95], v[76:79], 0\n"
"v_cvt_pk_bf16_f32 v83, v86, v87\n"
"ds_write_b128 v9, a[196:199] offset:32768\n"
"v_mfma_f32_16x16x32_bf16 a[140:143], v[92:95], v[44:47], 0\n"
"ds_write_b128 v9, a[188:191] offset:33792\n"
"s_waitcnt vmcnt(19)\n"
"v_mfma_f32_16x16x32_bf16 v[72:75], a[8:11], a[172:175], v[72:75]\n"
"buffer_load_dwordx4 a[200:203], v1, s[4:7], s39 offen offset:2048\n"
"v_mfma_f32_16x16x32_bf16 v[68:71], a[12:15], a[172:175], v[68:71]\n"
"ds_write_b128 v9, a[184:187] offset:34816\n"
"v_mfma_f32_16x16x32_bf16 a[212:215], v[80:83], v[76:79], 0\n"
"ds_write_b128 v9, a[180:183] offset:35840\n"
"s_waitcnt vmcnt(19)\n"
"v_mfma_f32_16x16x32_bf16 v[72:75], a[16:19], a[168:171], v[72:75]\n"
"buffer_load_dwordx4 a[204:207], v1, s[4:7], s39 offen offset:3072\n"
"v_mfma_f32_16x16x32_bf16 v[68:71], a[20:23], a[168:171], v[68:71]\n"
"ds_write_b128 v9, a[176:179] offset:49152\n"
"v_mfma_f32_16x16x32_bf16 a[144:147], v[80:83], v[44:47], 0\n"
"ds_write_b128 v9, a[172:175] offset:50176\n"
"s_waitcnt vmcnt(19)\n"
"v_mfma_f32_16x16x32_bf16 v[72:75], a[24:27], a[164:167], v[72:75]\n"
"ds_write_b128 v9, a[168:171] offset:51200\n"
"v_mfma_f32_16x16x32_bf16 v[68:71], a[28:31], a[164:167], v[68:71]\n"
"ds_write_b128 v9, a[164:167] offset:52224\n"
"s_nop 4\n"
"v_cmp_u_f32 vcc, v64, v72\n"
"s_waitcnt vmcnt(18)\n"
"v_add_f32 v44, v60, v62\n"
"v_mov_b32 v45, v44\n"
"v_add_f32 v46, v61, v63\n"
"s_nop 0\n"
"v_permlane16_swap_b32 v44, v45\n"
"v_add_f32 v47, v44, v45\n"
"v_mov_b32 v44, v47\n"
"s_nop 1\n"
"v_permlane32_swap_b32 v47, v44\n"
"v_add_f32 v76, v47, v44\n"
"v_mul_f32 v44, 0x3e000000, v76\n"
"v_sub_f32 v47, v60, v44\n"
"v_sub_f32 v76, v62, v44\n"
"v_mul_f32 v60, v47, v47\n"
"v_fmac_f32 v60, v76, v76\n"
"v_fmamk_f32 v47, v60, 0x42000000, v46\n"
"v_mov_b32 v60, v47\n"
"s_nop 1\n"
"v_permlane16_swap_b32 v47, v60\n"
"v_add_f32 v46, v47, v60\n"
"v_mov_b32 v47, v46\n"
"s_nop 1\n"
"v_permlane32_swap_b32 v46, v47\n"
"v_add_f32 v45, v46, v47\n"
"v_cmp_u_f32 s[0:1], v45, v45\n"
"ds_write_b64 v17, v[44:45] offset:512\n"
"s_or_b64 vcc, vcc, s[0:1]\n"
"s_cbranch_vccnz RkvB%=\n"
"CkvB%=:\n"
"s_waitcnt lgkmcnt(0)\n"
"s_barrier\n"
"buffer_load_dwordx4 a[208:211], v2, s[4:7], s39 offen\n"
"ds_read_b128 a[0:3], v10 offset:32768\n"
"s_xor_b32 s0, s59, 0x400\n"
"v_mov_b32 v60, 0x3f803f80\n"
"v_mov_b32 v61, 0x3f803f80\n"
"ds_read_b128 a[176:179], v10 offset:49152\n"
"v_mov_b32 v62, 0x3f803f80\n"
"v_mov_b32 v63, 0x3f803f80\n"
"ds_read_b128 v[80:83], v10 offset:33792\n"
"ds_read_b128 v[76:79], v10 offset:50176\n"
"buffer_load_dwordx4 v[176:179], v2, s[4:7], s39 offen offset:1024\n"
"ds_read_b128 v[44:47], v10 offset:34816\n"
"ds_read_b128 a[172:175], v10 offset:51200\n"
"ds_read_b128 a[168:171], v10 offset:35840\n"
"s_waitcnt lgkmcnt(6)\n"
"v_mfma_f32_16x16x32_bf16 v[64:67], a[32:35], a[0:3], v[64:67]\n"
"ds_read_b128 a[164:167], v10 offset:52224\n"
"buffer_load_dwordx4 v[172:175], v2, s[4:7], s39 offen offset:2048\n"
"s_waitcnt lgkmcnt(6)\n"
"v_mfma_f32_16x16x32_bf16 v[72:75], a[32:35], a[176:179], v[72:75]\n"
"ds_read_b128 a[28:31], v11 offset:32768\n"
"s_waitcnt lgkmcnt(6)\n"
"v_mfma_f32_16x16x32_bf16 v[64:67], a[40:43], v[80:83], v[64:67]\n"
"ds_read_b128 a[24:27], v11 offset:49152\n"
"s_waitcnt lgkmcnt(6)\n"
"v_mfma_f32_16x16x32_bf16 v[72:75], a[40:43], v[76:79], v[72:75]\n"
"ds_read_b128 a[20:23], v11 offset:33792\n"
"s_waitcnt lgkmcnt(6)\n"
"v_mfma_f32_16x16x32_bf16 v[64:67], a[48:51], v[44:47], v[64:67]\n"
"buffer_load_dwordx4 v[168:171], v2, s[4:7], s39 offen offset:3072\n"
"s_waitcnt lgkmcnt(5)\n"
"v_mfma_f32_16x16x32_bf16 v[72:75], a[48:51], a[172:175], v[72:75]\n"
"ds_read_b128 a[16:19], v11 offset:50176\n"
"s_waitcnt lgkmcnt(5)\n"
"v_mfma_f32_16x16x32_bf16 v[64:67], a[56:59], a[168:171], v[64:67]\n"
"ds_read_b128 a[12:15], v11 offset:34816\n"
"s_waitcnt lgkmcnt(5)\n"
"v_mfma_f32_16x16x32_bf16 v[72:75], a[56:59], a[164:167], v[72:75]\n"
"ds_read_b128 a[8:11], v11 offset:51200\n"
"v_mfma_f32_16x16x32_bf16 v[56:59], a[36:39], a[0:3], v[56:59]\n"
"buffer_load_dwordx4 v[164:167], v3, s[4:7], s39 offen\n"
"s_waitcnt lgkmcnt(5)\n"
"v_mfma_f32_16x16x32_bf16 v[64:67], a[64:67], a[28:31], v[64:67]\n"
"ds_read_b128 a[4:7], v11 offset:35840\n"
"s_waitcnt lgkmcnt(5)\n"
"v_mfma_f32_16x16x32_bf16 v[72:75], a[64:67], a[24:27], v[72:75]\n"
"ds_read_b128 a[0:3], v11 offset:52224\n"
"s_waitcnt lgkmcnt(5)\n"
"v_mfma_f32_16x16x32_bf16 v[64:67], a[72:75], a[20:23], v[64:67]\n"
"ds_read_b128 a[32:35], v12 offset:32768\n"
"v_mfma_f32_16x16x32_bf16 v[68:71], a[36:39], a[176:179], v[68:71]\n"
"buffer_load_dwordx4 a[64:67], v3, s[4:7], s39 offen offset:1024\n"
"s_waitcnt lgkmcnt(5)\n"
"v_mfma_f32_16x16x32_bf16 v[72:75], a[72:75], a[16:19], v[72:75]\n"
"ds_read_b128 a[40:43], v12 offset:49152\n"
"s_waitcnt lgkmcnt(5)\n"
"v_mfma_f32_16x16x32_bf16 v[64:67], a[80:83], a[12:15], v[64:67]\n"
"ds_read_b128 a[48:51], v12 offset:33792\n"
"s_waitcnt lgkmcnt(5)\n"
"v_mfma_f32_16x16x32_bf16 v[72:75], a[80:83], a[8:11], v[72:75]\n"
"ds_read_b128 a[56:59], v12 offset:50176\n"
"v_mfma_f32_16x16x32_bf16 v[56:59], a[44:47], v[80:83], v[56:59]\n"
"buffer_load_dwordx4 a[80:83], v3, s[4:7], s39 offen offset:2048\n"
"s_waitcnt lgkmcnt(5)\n"
"v_mfma_f32_16x16x32_bf16 v[64:67], a[88:91], a[4:7], v[64:67]\n"
"ds_read_b128 a[36:39], v12 offset:34816\n"
"s_waitcnt lgkmcnt(5)\n"
"v_mfma_f32_16x16x32_bf16 v[72:75], a[88:91], a[0:3], v[72:75]\n"
"ds_read_b128 a[72:75], v12 offset:51200\n"
"s_waitcnt lgkmcnt(5)\n"
"v_mfma_f32_16x16x32_bf16 v[64:67], a[96:99], a[32:35], v[64:67]\n"
"ds_read_b128 v[80:83], v18 offset:512\n"
"v_mfma_f32_16x16x32_bf16 v[68:71], a[44:47], v[76:79], v[68:71]\n"
"buffer_load_dwordx4 a[44:47], v3, s[4:7], s39 offen offset:3072\n"
"s_waitcnt lgkmcnt(5)\n"
"v_mfma_f32_16x16x32_bf16 v[72:75], a[96:99], a[40:43], v[72:75]\n"
"ds_read_b128 v[84:87], v18 offset:768\n"
"s_waitcnt lgkmcnt(5)\n"
"v_mfma_f32_16x16x32_bf16 v[64:67], a[104:107], a[48:51], v[64:67]\n"
"ds_read_b128 a[88:91], v12 offset:35840\n"
"s_waitcnt lgkmcnt(5)\n"
"v_mfma_f32_16x16x32_bf16 v[72:75], a[104:107], a[56:59], v[72:75]\n"
"ds_read_b128 a[96:99], v12 offset:52224\n"
"v_mfma_f32_16x16x32_bf16 v[56:59], a[52:55], v[44:47], v[56:59]\n"
"buffer_load_dwordx4 a[104:107], v4, s[4:7], s39 offen\n"
"s_waitcnt lgkmcnt(5)\n"
"v_mfma_f32_16x16x32_bf16 v[64:67], a[112:115], a[36:39], v[64:67]\n"
"ds_read_b128 v[76:79], v20 offset:256\n"
"s_waitcnt lgkmcnt(4)\n"
"v_sub_f32 v45, v80, v82\n"
"v_mfma_f32_16x16x32_bf16 v[72:75], a[112:115], a[72:75], v[72:75]\n"
"v_mul_f32 v44, v45, v45\n"
"v_add_f32 v45, v81, v83\n"
"v_mfma_f32_16x16x32_bf16 v[68:71], a[52:55], a[172:175], v[68:71]\n"
"v_fmamk_f32 v46, v44, 0x43000000, v45\n"
"v_mul_f32 v44, 0x3b000000, v46\n"
"v_mfma_f32_16x16x32_bf16 v[56:59], a[60:63], a[168:171], v[56:59]\n"
"buffer_load_dwordx4 a[52:55], v4, s[4:7], s39 offen offset:1024\n"
"s_waitcnt lgkmcnt(2)\n"
"v_mfma_f32_16x16x32_bf16 v[64:67], a[120:123], a[88:91], v[64:67]\n"
"v_sub_f32 v46, v84, v86\n"
"v_mul_f32 v45, v46, v46\n"
"s_waitcnt lgkmcnt(1)\n"
"v_mfma_f32_16x16x32_bf16 v[72:75], a[120:123], a[96:99], v[72:75]\n"
"v_add_f32 v46, v85, v87\n"
"v_fmamk_f32 v47, v45, 0x43000000, v46\n"
"v_mfma_f32_16x16x32_bf16 v[68:71], a[60:63], a[164:167], v[68:71]\n"
"v_mul_f32 v46, 0x3b000000, v47\n"
"v_add_f32 v44, 0x3727c5ac, v44\n"
"v_mfma_f32_16x16x32_bf16 v[56:59], a[68:71], a[28:31], v[56:59]\n"
"buffer_load_dwordx4 a[60:63], v4, s[4:7], s39 offen offset:2048\n"
"v_mfma_f32_16x16x32_bf16 v[68:71], a[68:71], a[24:27], v[68:71]\n"
"v_add_f32 v46, 0x3727c5ac, v46\n"
"v_rsq_f32 v45, v44\n"
"v_mfma_f32_16x16x32_bf16 v[56:59], a[76:79], a[20:23], v[56:59]\n"
"v_rsq_f32 v44, v46\n"
"v_mfma_f32_16x16x32_bf16 v[68:71], a[76:79], a[16:19], v[68:71]\n"
"v_add_f32 v46, v80, v82\n"
"v_add_f32 v47, v84, v86\n"
"v_mfma_f32_16x16x32_bf16 v[56:59], a[84:87], a[12:15], v[56:59]\n"
"buffer_load_dwordx4 a[76:79], v4, s[4:7], s39 offen offset:3072\n"
"v_mfma_f32_16x16x32_bf16 v[68:71], a[84:87], a[8:11], v[68:71]\n"
"v_mul_f32 v80, 0.5, v46\n"
"v_mul_f32 v84, 0.5, v47\n"
"v_mfma_f32_16x16x32_bf16 v[56:59], a[92:95], a[4:7], v[56:59]\n"
"v_mul_f32 v46, -v45, v80\n"
"v_mul_f32 v80, v64, v45\n"
"v_mfma_f32_16x16x32_bf16 v[68:71], a[92:95], a[0:3], v[68:71]\n"
"v_mul_f32 v81, v65, v45\n"
"v_mul_f32 v82, v66, v45\n"
"v_mfma_f32_16x16x32_bf16 v[56:59], a[100:103], a[32:35], v[56:59]\n"
"buffer_load_dwordx4 a[92:95], v1, s[4:7], s40 offen\n"
"v_mfma_f32_16x16x32_bf16 v[68:71], a[100:103], a[40:43], v[68:71]\n"
"v_mul_f32 v83, v67, v45\n"
"v_mul_f32 v47, -v44, v84\n"
"v_mfma_f32_16x16x32_bf16 v[56:59], a[108:111], a[48:51], v[56:59]\n"
"v_mul_f32 v64, v72, v44\n"
"v_mul_f32 v65, v73, v44\n"
"v_mfma_f32_16x16x32_bf16 v[68:71], a[108:111], a[56:59], v[68:71]\n"
"v_mul_f32 v66, v74, v44\n"
"v_mul_f32 v67, v75, v44\n"
"v_mfma_f32_16x16x32_bf16 v[56:59], a[116:119], a[36:39], v[56:59]\n"
"buffer_load_dwordx4 a[108:111], v1, s[4:7], s40 offen offset:1024\n"
"v_mfma_f32_16x16x32_bf16 v[68:71], a[116:119], a[72:75], v[68:71]\n"
"s_waitcnt lgkmcnt(0)\n"
"v_fmac_f32 v80, v76, v46\n"
"v_fmac_f32 v81, v77, v46\n"
"v_mfma_f32_16x16x32_bf16 v[56:59], a[124:127], a[88:91], v[56:59]\n"
"v_fmac_f32 v82, v78, v46\n"
"v_fmac_f32 v83, v79, v46\n"
"v_mfma_f32_16x16x32_bf16 v[68:71], a[124:127], a[96:99], v[68:71]\n"
"v_fmac_f32 v64, v76, v47\n"
"v_fmac_f32 v65, v77, v47\n"
"v_fmac_f32 v66, v78, v47\n"
"v_fmac_f32 v67, v79, v47\n"
"buffer_load_dwordx4 a[124:127], v1, s[4:7], s40 offen offset:2048\n"
"v_cvt_pk_bf16_f32 v72, v80, v81\n"
"v_cvt_pk_bf16_f32 v73, v82, v83\n"
"v_cvt_pk_bf16_f32 v76, v64, v65\n"
"v_cvt_pk_bf16_f32 v77, v66, v67\n"
"v_mfma_f32_16x16x16_bf16 v[84:87], a[128:129], v[72:73], 0\n"
"ds_read_b128 v[72:75], v20 offset:512\n"
"v_mul_f32 v64, v56, v45\n"
"v_mfma_f32_16x16x16_bf16 v[88:91], a[128:129], v[76:77], 0\n"
"v_mul_f32 v65, v57, v45\n"
"v_mul_f32 v66, v58, v45\n"
"v_mul_f32 v67, v59, v45\n"
"v_mul_f32 v56, v68, v44\n"
"buffer_load_dwordx4 a[116:119], v1, s[4:7], s40 offen offset:3072\n"
"v_max_f32 v57, v84, v85\n"
"v_min_f32 v59, v84, v85\n"
"v_max3_f32 v57, v57, v86, v87\n"
"v_max_f32 v58, v88, v89\n"
"v_min_f32 v78, v88, v89\n"
"v_max3_f32 v76, -v59, -v86, -v87\n"
"v_max3_f32 v58, v58, v90, v91\n"
"v_max3_f32 v77, -v78, -v90, -v91\n"
"v_permlane16_swap_b32 v57, v76\n"
"v_max_f32 v59, v57, v76\n"
"v_permlane16_swap_b32 v58, v77\n"
"v_max_f32 v76, v58, v77\n"
"v_mul_f32 v77, v69, v44\n"
"buffer_load_dwordx4 a[100:103], v2, s[4:7], s40 offen\n"
"v_permlane32_swap_b32 v59, v76\n"
"v_max_f32 v57, v59, v76\n"
"v_mul_f32 v57, 0xbfb8aa3b, v57\n"
"v_mov_b32 v76, v57\n"
"v_mul_f32 v78, v70, v44\n"
"v_mul_f32 v79, v71, v44\n"
"v_permlane16_swap_b32 v57, v76\n"
"v_mov_b32 v69, v57\n"
"v_mov_b32 v92, v76\n"
"s_waitcnt lgkmcnt(0)\n"
"v_fmac_f32 v64, v72, v46\n"
"v_permlane32_swap_b32 v57, v69\n"
"v_permlane32_swap_b32 v76, v92\n"
"v_fmamk_f32 v80, v84, 0x3fb8aa3b, v57\n"
"buffer_load_dwordx4 a[84:87], v2, s[4:7], s40 offen offset:1024\n"
"v_fmamk_f32 v81, v85, 0x3fb8aa3b, v57\n"
"v_fmamk_f32 v82, v86, 0x3fb8aa3b, v57\n"
"v_fmamk_f32 v83, v87, 0x3fb8aa3b, v57\n"
"v_fmamk_f32 v57, v84, 0xbfb8aa3b, v76\n"
"v_fmamk_f32 v58, v85, 0xbfb8aa3b, v76\n"
"v_fmamk_f32 v59, v86, 0xbfb8aa3b, v76\n"
"v_fmamk_f32 v68, v87, 0xbfb8aa3b, v76\n"
"v_fmamk_f32 v85, v88, 0x3fb8aa3b, v69\n"
"v_fmamk_f32 v87, v89, 0x3fb8aa3b, v69\n"
"v_fmamk_f32 v84, v90, 0x3fb8aa3b, v69\n"
"v_fmamk_f32 v86, v91, 0x3fb8aa3b, v69\n"
"v_fmamk_f32 v70, v88, 0xbfb8aa3b, v92\n"
"v_fmamk_f32 v71, v89, 0xbfb8aa3b, v92\n"
"v_fmamk_f32 v69, v90, 0xbfb8aa3b, v92\n"
"buffer_load_dwordx4 a[164:167], v2, s[4:7], s40 offen offset:2048\n"
"v_fmamk_f32 v76, v91, 0xbfb8aa3b, v92\n"
"v_exp_f32 v80, v80\n"
"v_exp_f32 v81, v81\n"
"v_exp_f32 v82, v82\n"
"v_exp_f32 v83, v83\n"
"v_exp_f32 v57, v57\n"
"v_exp_f32 v58, v58\n"
"v_exp_f32 v59, v59\n"
"buffer_load_dwordx4 a[168:171], v2, s[4:7], s40 offen offset:3072\n"
"v_exp_f32 v68, v68\n"
"v_exp_f32 v85, v85\n"
"v_exp_f32 v87, v87\n"
"v_exp_f32 v84, v84\n"
"v_exp_f32 v86, v86\n"
"v_exp_f32 v70, v70\n"
"v_exp_f32 v71, v71\n"
"buffer_load_dwordx4 a[172:175], v3, s[4:7], s40 offen\n"
"v_exp_f32 v69, v69\n"
"v_exp_f32 v76, v76\n"
"v_add_f32 v88, v80, v81\n"
"v_add_f32 v92, v82, v83\n"
"v_add_f32 v89, v57, v58\n"
"v_add_f32 v93, v59, v68\n"
"v_add_f32 v90, v85, v87\n"
"v_add_f32 v94, v84, v86\n"
"v_add_f32 v91, v70, v71\n"
"v_add_f32 v95, v69, v76\n"
"v_add_f32 v88, v88, v92\n"
"v_add_f32 v89, v89, v93\n"
"buffer_load_dwordx4 a[176:179], v3, s[4:7], s40 offen offset:1024\n"
"v_add_f32 v90, v90, v94\n"
"v_add_f32 v91, v91, v95\n"
"v_permlane16_swap_b32 v88, v89\n"
"v_add_f32 v92, v88, v89\n"
"v_permlane16_swap_b32 v90, v91\n"
"v_add_f32 v88, v90, v91\n"
"v_fmac_f32 v65, v73, v46\n"
"v_fmac_f32 v66, v74, v46\n"
"v_permlane32_swap_b32 v92, v88\n"
"v_add_f32 v89, v92, v88\n"
"v_rcp_f32 v89, v89\n"
"v_fmac_f32 v67, v75, v46\n"
"buffer_load_dwordx4 a[180:183], v3, s[4:7], s40 offen offset:2048\n"
"v_mov_b32 v88, v89\n"
"v_fmac_f32 v56, v72, v47\n"
"v_fmac_f32 v77, v73, v47\n"
"v_permlane16_swap_b32 v89, v88\n"
"v_mov_b32 v90, v89\n"
"v_fmac_f32 v78, v74, v47\n"
"v_fmac_f32 v79, v75, v47\n"
"v_permlane32_swap_b32 v89, v90\n"
"v_mul_f32 v80, v80, v89\n"
"v_mul_f32 v81, v81, v89\n"
"v_mul_f32 v82, v82, v89\n"
"v_mul_f32 v83, v83, v89\n"
"v_mul_f32 v85, v85, v90\n"
"buffer_load_dwordx4 a[184:187], v3, s[4:7], s40 offen offset:3072\n"
"v_mul_f32 v87, v87, v90\n"
"v_mul_f32 v84, v84, v90\n"
"v_mul_f32 v86, v86, v90\n"
"v_cvt_pk_bf16_f32 v90, v64, v65\n"
"v_cvt_pk_bf16_f32 v91, v66, v67\n"
"v_cvt_pk_bf16_f32 v64, v56, v77\n"
"v_cvt_pk_bf16_f32 v65, v78, v79\n"
"v_mfma_f32_16x16x16_bf16 v[72:75], v[90:91], v[26:27], 0\n"
"v_cvt_pk_bf16_f32 v96, v80, v81\n"
"v_cvt_pk_bf16_f32 v97, v82, v83\n"
"v_mfma_f32_16x16x16_bf16 v[92:95], v[64:65], v[26:27], 0\n"
"v_cvt_pk_bf16_f32 v80, v85, v87\n"
"buffer_load_dwordx4 a[188:191], v4, s[4:7], s40 offen\n"
"v_mfma_f32_16x16x16_bf16 v[100:103], v[96:97], v[26:27], 0\n"
"v_cvt_pk_bf16_f32 v81, v84, v86\n"
"v_mov_b32 v56, v88\n"
"v_cvt_pk_bf16_f32 v64, v72, v73\n"
"v_mfma_f32_16x16x16_bf16 v[84:87], v[80:81], v[26:27], 0\n"
"v_cvt_pk_bf16_f32 v65, v74, v75\n"
"v_cvt_pk_bf16_f32 v66, v92, v93\n"
"v_cvt_pk_bf16_f32 v67, v94, v95\n"
"v_permlane32_swap_b32 v88, v56\n"
"v_cvt_pk_bf16_f32 v72, v100, v101\n"
"v_cvt_pk_bf16_f32 v73, v102, v103\n"
"v_mul_f32 v57, v57, v88\n"
"buffer_load_dwordx4 v[100:103], v4, s[4:7], s40 offen offset:1024\n"
"v_cvt_pk_bf16_f32 v74, v84, v85\n"
"v_cvt_pk_bf16_f32 v75, v86, v87\n"
"v_mul_f32 v58, v58, v88\n"
"v_mul_f32 v59, v59, v88\n"
"v_mfma_f32_16x16x32_bf16 a[192:195], v[72:75], v[64:67], a[192:195]\n"
"v_mul_f32 v68, v68, v88\n"
"v_mul_f32 v70, v70, v56\n"
"v_mul_f32 v71, v71, v56\n"
"v_mul_f32 v69, v69, v56\n"
"v_mul_f32 v76, v76, v56\n"
"v_cvt_pk_bf16_f32 v98, v57, v58\n"
"v_cvt_pk_bf16_f32 v99, v59, v68\n"
"v_cvt_pk_bf16_f32 v82, v70, v71\n"
"buffer_store_dwordx4 a[192:195], v36, s[28:31], s59 offen sc1\n"
"v_mfma_f32_16x16x16_bf16 v[56:59], v[98:99], v[26:27], 0\n"
"v_cvt_pk_bf16_f32 v83, v69, v76\n"
"buffer_load_dwordx4 a[192:195], v4, s[4:7], s40 offen offset:2048\n"
"v_mfma_f32_16x16x32_bf16 a[140:143], v[72:75], v[60:63], a[140:143]\n"
"v_mfma_f32_16x16x16_bf16 v[72:75], v[82:83], v[26:27], 0\n"
"s_nop 3\n"
"v_cvt_pk_bf16_f32 v68, v56, v57\n"
"buffer_load_dwordx4 a[196:199], v4, s[4:7], s40 offen offset:3072\n"
"v_cvt_pk_bf16_f32 v69, v58, v59\n"
"s_nop 0\n"
"v_cvt_pk_bf16_f32 v70, v72, v73\n"
"v_cvt_pk_bf16_f32 v71, v74, v75\n"
"s_nop 1\n"
"v_mfma_f32_16x16x32_bf16 a[212:215], v[68:71], v[64:67], a[212:215]\n"
"v_mfma_f32_16x16x32_bf16 a[144:147], v[68:71], v[60:63], a[144:147]\n"
"s_nop 6\n"
"buffer_store_dwordx4 a[212:215], v36, s[28:31], s0 offen sc1\n"
"buffer_store_dwordx4 a[140:143], v29, s[20:23], s60 offen sc1\n"
"buffer_store_dwordx4 a[144:147], v29, s[20:23], s60 offen offset:64 sc1\n"
"s_add_u32 s32, s54, 0x400000\n"
"s_add_u32 s3, s54, 0x410000\n"
"s_add_u32 s2, s55, 0x100000\n"
"s_add_u32 s1, s61, 0x20000\n"
"s_add_u32 s0, s62, 0x1000\n"
"ds_read_b128 a[144:147], v9\n"
"v_mov_b32 v64, -1\n"
"v_mov_b32 v65, -1\n"
"v_mov_b32 v66, -1\n"
"ds_read_b128 a[212:215], v9 offset:16384\n"
"v_mov_b32 v67, -1\n"
"buffer_store_dwordx4 v[64:67], v1, s[4:7], s32 offen sc1\n"
"ds_read_b128 v[76:79], v9 offset:1024\n"
"ds_read_b128 a[140:143], v9 offset:17408\n"
"ds_read_b128 v[80:83], v9 offset:2048\n"
"ds_read_b128 v[72:75], v9 offset:18432\n"
"buffer_store_dwordx4 v[64:67], v1, s[4:7], s3 offen sc1\n"
"s_waitcnt lgkmcnt(5)\n"
"v_mfma_f32_16x16x32_bf16 v[60:63], v[224:227], a[144:147], 0\n"
"ds_read_b128 a[144:147], v9 offset:3072\n"
"s_waitcnt lgkmcnt(5)\n"
"v_mfma_f32_16x16x32_bf16 v[56:59], v[224:227], a[212:215], 0\n"
"ds_read_b128 a[212:215], v9 offset:19456\n"
"ds_read_b128 v[68:71], v10\n"
"s_waitcnt lgkmcnt(6)\n"
"v_mfma_f32_16x16x32_bf16 v[60:63], v[228:231], v[76:79], v[60:63]\n"
"buffer_store_dwordx4 v[64:67], v6, s[4:7], s2 offen sc1\n"
"s_waitcnt lgkmcnt(5)\n"
"v_mfma_f32_16x16x32_bf16 v[56:59], v[228:231], a[140:143], v[56:59]\n"
"ds_read_b128 a[140:143], v10 offset:16384\n"
"s_waitcnt lgkmcnt(5)\n"
"v_mfma_f32_16x16x32_bf16 v[60:63], v[232:235], v[80:83], v[60:63]\n"
"ds_read_b128 v[80:83], v10 offset:1024\n"
"s_waitcnt lgkmcnt(5)\n"
"v_mfma_f32_16x16x32_bf16 v[56:59], v[232:235], v[72:75], v[56:59]\n"
"ds_read_b128 v[72:75], v10 offset:17408\n"
"s_waitcnt lgkmcnt(5)\n"
"v_mfma_f32_16x16x32_bf16 v[60:63], v[236:239], a[144:147], v[60:63]\n"
"buffer_load_dwordx4 a[144:147], v1, s[4:7], s41 offen\n"
"s_waitcnt lgkmcnt(4)\n"
"v_mfma_f32_16x16x32_bf16 v[56:59], v[236:239], a[212:215], v[56:59]\n"
"ds_read_b128 a[212:215], v10 offset:2048\n"
"s_waitcnt vmcnt(51) lgkmcnt(4)\n"
"v_mfma_f32_16x16x32_bf16 v[60:63], v[240:243], v[68:71], v[60:63]\n"
"ds_read_b128 v[68:71], v10 offset:18432\n"
"ds_read_b128 v[64:67], v10 offset:3072\n"
"s_waitcnt lgkmcnt(5)\n"
"v_mfma_f32_16x16x32_bf16 v[56:59], v[240:243], a[140:143], v[56:59]\n"
"buffer_load_dwordx4 a[140:143], v1, s[4:7], s41 offen offset:1024\n"
"s_waitcnt vmcnt(51) lgkmcnt(4)\n"
"v_mfma_f32_16x16x32_bf16 v[60:63], v[244:247], v[80:83], v[60:63]\n"
"ds_read_b128 v[96:99], v10 offset:19456\n"
"s_waitcnt lgkmcnt(4)\n"
"v_mfma_f32_16x16x32_bf16 v[56:59], v[244:247], v[72:75], v[56:59]\n"
"ds_read_b128 v[76:79], v11\n"
"ds_read_b128 v[80:83], v11 offset:16384\n"
"s_waitcnt vmcnt(50) lgkmcnt(5)\n"
"v_mfma_f32_16x16x32_bf16 v[60:63], v[248:251], a[212:215], v[60:63]\n"
"buffer_load_dwordx4 a[212:215], v1, s[4:7], s41 offen offset:2048\n"
"s_waitcnt lgkmcnt(4)\n"
"v_mfma_f32_16x16x32_bf16 v[56:59], v[248:251], v[68:71], v[56:59]\n"
"ds_read_b128 v[72:75], v11 offset:1024\n"
"s_waitcnt vmcnt(50) lgkmcnt(4)\n"
"v_mfma_f32_16x16x32_bf16 v[60:63], v[252:255], v[64:67], v[60:63]\n"
"ds_read_b128 v[68:71], v11 offset:17408\n"
"ds_read_b128 v[64:67], v11 offset:2048\n"
"s_waitcnt lgkmcnt(5)\n"
"v_mfma_f32_16x16x32_bf16 v[56:59], v[252:255], v[96:99], v[56:59]\n"
"buffer_load_dwordx4 a[216:219], v1, s[4:7], s41 offen offset:3072\n"
"s_waitcnt vmcnt(50) lgkmcnt(4)\n"
"v_mfma_f32_16x16x32_bf16 v[60:63], a[252:255], v[76:79], v[60:63]\n"
"ds_read_b128 v[96:99], v11 offset:18432\n"
"s_waitcnt lgkmcnt(4)\n"
"v_mfma_f32_16x16x32_bf16 v[56:59], a[252:255], v[80:83], v[56:59]\n"
"ds_read_b128 v[76:79], v11 offset:3072\n"
"ds_read_b128 v[80:83], v11 offset:19456\n"
"s_waitcnt vmcnt(49) lgkmcnt(5)\n"
"v_mfma_f32_16x16x32_bf16 v[60:63], a[248:251], v[72:75], v[60:63]\n"
"buffer_load_dwordx4 a[220:223], v2, s[4:7], s41 offen\n"
"s_waitcnt lgkmcnt(4)\n"
"v_mfma_f32_16x16x32_bf16 v[56:59], a[248:251], v[68:71], v[56:59]\n"
"ds_read_b128 v[72:75], v12\n"
"s_waitcnt vmcnt(49) lgkmcnt(4)\n"
"v_mfma_f32_16x16x32_bf16 v[60:63], a[244:247], v[64:67], v[60:63]\n"
"ds_read_b128 v[68:71], v12 offset:16384\n"
"ds_read_b128 v[64:67], v12 offset:1024\n"
"s_waitcnt lgkmcnt(5)\n"
"v_mfma_f32_16x16x32_bf16 v[56:59], a[244:247], v[96:99], v[56:59]\n"
"buffer_load_dwordx4 a[224:227], v2, s[4:7], s41 offen offset:1024\n"
"s_waitcnt vmcnt(49) lgkmcnt(4)\n"
"v_mfma_f32_16x16x32_bf16 v[60:63], a[240:243], v[76:79], v[60:63]\n"
"ds_read_b128 v[96:99], v12 offset:17408\n"
"s_waitcnt lgkmcnt(4)\n"
"v_mfma_f32_16x16x32_bf16 v[56:59], a[240:243], v[80:83], v[56:59]\n"
"ds_read_b128 v[76:79], v12 offset:2048\n"
"ds_read_b128 v[80:83], v12 offset:18432\n"
"s_waitcnt vmcnt(48) lgkmcnt(5)\n"
"v_mfma_f32_16x16x32_bf16 v[60:63], a[152:155], v[72:75], v[60:63]\n"
"buffer_load_dwordx4 a[228:231], v2, s[4:7], s41 offen offset:2048\n"
"s_waitcnt lgkmcnt(4)\n"
"v_mfma_f32_16x16x32_bf16 v[56:59], a[152:155], v[68:71], v[56:59]\n"
"ds_read_b128 v[72:75], v12 offset:3072\n"
"s_waitcnt vmcnt(48) lgkmcnt(4)\n"
"v_mfma_f32_16x16x32_bf16 v[60:63], a[148:151], v[64:67], v[60:63]\n"
"ds_read_b128 v[68:71], v12 offset:19456\n"
"ds_read_b128 v[64:67], v20\n"
"s_waitcnt lgkmcnt(5)\n"
"v_mfma_f32_16x16x32_bf16 v[56:59], a[148:151], v[96:99], v[56:59]\n"
"buffer_load_dwordx4 a[232:235], v2, s[4:7], s41 offen offset:3072\n"
"s_waitcnt vmcnt(48) lgkmcnt(4)\n"
"v_mfma_f32_16x16x32_bf16 v[60:63], a[156:159], v[76:79], v[60:63]\n"
"s_waitcnt lgkmcnt(3)\n"
"v_mfma_f32_16x16x32_bf16 v[56:59], a[156:159], v[80:83], v[56:59]\n"
"s_waitcnt vmcnt(47) lgkmcnt(2)\n"
"v_mfma_f32_16x16x32_bf16 v[60:63], a[160:163], v[72:75], v[60:63]\n"
"buffer_load_dwordx4 a[236:239], v3, s[4:7], s41 offen\n"
"s_waitcnt lgkmcnt(1)\n"
"v_mfma_f32_16x16x32_bf16 v[56:59], a[160:163], v[68:71], v[56:59]\n"
"s_nop 4\n"
"v_mul_f32 v70, v60, v40\n"
"s_waitcnt lgkmcnt(0)\n"
"v_fmac_f32 v70, v64, v42\n"
"buffer_load_dwordx4 v[204:207], v3, s[4:7], s41 offen offset:1024\n"
"v_mul_f32 v71, v61, v40\n"
"v_fmac_f32 v71, v65, v42\n"
"v_cvt_pk_bf16_f32 v68, v70, v71\n"
"v_mul_f32 v70, v62, v40\n"
"v_mul_f32 v71, v63, v40\n"
"v_mul_f32 v40, v56, v0\n"
"v_mul_f32 v60, v57, v0\n"
"v_mul_f32 v61, v58, v0\n"
"v_mul_f32 v62, v59, v0\n"
"v_fmac_f32 v70, v66, v42\n"
"buffer_load_dwordx4 v[200:203], v3, s[4:7], s41 offen offset:2048\n"
"v_fmac_f32 v71, v67, v42\n"
"v_fmac_f32 v40, v64, v43\n"
"v_fmac_f32 v60, v65, v43\n"
"v_fmac_f32 v61, v66, v43\n"
"v_fmac_f32 v62, v67, v43\n"
"v_cvt_pk_bf16_f32 v69, v70, v71\n"
"v_cvt_pk_bf16_f32 v42, v40, v60\n"
"v_cvt_pk_bf16_f32 v43, v61, v62\n"
"v_mfma_f32_16x16x16_bf16 v[72:75], a[128:129], v[68:69], 0\n"
"s_nop 0\n"
"v_mfma_f32_16x16x16_bf16 v[80:83], a[128:129], v[42:43], 0\n"
"buffer_load_dwordx4 v[196:199], v3, s[4:7], s41 offen offset:3072\n"
"s_nop 4\n"
"v_max_f32 v0, v72, v73\n"
"v_min_f32 v42, v72, v73\n"
"buffer_load_dwordx4 v[192:195], v4, s[4:7], s41 offen\n"
"v_max3_f32 v0, v0, v74, v75\n"
"v_max3_f32 v40, -v42, -v74, -v75\n"
"v_max_f32 v42, v80, v81\n"
"v_min_f32 v56, v80, v81\n"
"v_max3_f32 v42, v42, v82, v83\n"
"v_max3_f32 v43, -v56, -v82, -v83\n"
"buffer_load_dwordx4 v[188:191], v4, s[4:7], s41 offen offset:1024\n"
"v_permlane16_swap_b32 v0, v40\n"
"v_permlane16_swap_b32 v42, v43\n"
"v_max_f32 v56, v0, v40\n"
"v_max_f32 v0, v42, v43\n"
"s_nop 1\n"
"v_permlane32_swap_b32 v56, v0\n"
"v_max_f32 v60, v56, v0\n"
"v_mul_f32 v60, 0xbfb8aa3b, v60\n"
"v_mov_b32 v70, v60\n"
"s_nop 1\n"
"v_permlane16_swap_b32 v60, v70\n"
"buffer_load_dwordx4 v[56:59], v30, s[28:31], s61 offen sc1\n"
"v_mov_b32 v40, v60\n"
"v_mov_b32 v0, v70\n"
"s_nop 0\n"
"v_permlane32_swap_b32 v60, v40\n"
"v_fmamk_f32 v42, v72, 0x3fb8aa3b, v60\n"
"v_permlane32_swap_b32 v70, v0\n"
"v_fmamk_f32 v43, v73, 0x3fb8aa3b, v60\n"
"v_fmamk_f32 v64, v74, 0x3fb8aa3b, v60\n"
"v_fmamk_f32 v65, v75, 0x3fb8aa3b, v60\n"
"v_fmamk_f32 v66, v72, 0xbfb8aa3b, v70\n"
"v_fmamk_f32 v67, v73, 0xbfb8aa3b, v70\n"
"v_fmamk_f32 v68, v74, 0xbfb8aa3b, v70\n"
"buffer_load_dwordx4 v[60:63], v30, s[28:31], s1 offen sc1\n"
"v_fmamk_f32 v69, v75, 0xbfb8aa3b, v70\n"
"v_fmamk_f32 v70, v80, 0x3fb8aa3b, v40\n"
"v_fmamk_f32 v71, v81, 0x3fb8aa3b, v40\n"
"v_fmamk_f32 v72, v82, 0x3fb8aa3b, v40\n"
"v_fmamk_f32 v73, v83, 0x3fb8aa3b, v40\n"
"v_fmamk_f32 v74, v80, 0xbfb8aa3b, v0\n"
"v_fmamk_f32 v76, v81, 0xbfb8aa3b, v0\n"
"v_fmamk_f32 v75, v82, 0xbfb8aa3b, v0\n"
"v_fmamk_f32 v77, v83, 0xbfb8aa3b, v0\n"
"v_exp_f32 v42, v42\n"
"v_exp_f32 v43, v43\n"
"buffer_load_dword v0, v31, s[20:23], s62 offen sc1\n"
"v_exp_f32 v64, v64\n"
"buffer_load_dword v40, v31, s[20:23], s0 offen sc1\n"
"v_exp_f32 v65, v65\n"
"buffer_load_dwordx4 v[184:187], v4, s[4:7], s41 offen offset:2048\n"
"v_exp_f32 v66, v66\n"
"v_exp_f32 v67, v67\n"
"v_exp_f32 v68, v68\n"
"v_exp_f32 v69, v69\n"
"v_exp_f32 v70, v70\n"
"v_exp_f32 v71, v71\n"
"v_exp_f32 v72, v72\n"
"buffer_load_dwordx4 v[180:183], v4, s[4:7], s41 offen offset:3072\n"
"v_exp_f32 v73, v73\n"
"v_exp_f32 v74, v74\n"
"v_exp_f32 v76, v76\n"
"v_exp_f32 v75, v75\n"
"v_exp_f32 v77, v77\n"
"v_add_f32 v78, v42, v43\n"
"v_add_f32 v82, v64, v65\n"
"v_add_f32 v79, v66, v67\n"
"v_add_f32 v83, v68, v69\n"
"buffer_load_dwordx4 a[72:75], v1, s[4:7], s42 offen\n"
"v_add_f32 v80, v70, v71\n"
"v_add_f32 v84, v72, v73\n"
"v_add_f32 v81, v74, v76\n"
"v_add_f32 v85, v75, v77\n"
"v_add_f32 v78, v78, v82\n"
"v_add_f32 v79, v79, v83\n"
"v_add_f32 v80, v80, v84\n"
"v_add_f32 v81, v81, v85\n"
"v_permlane16_swap_b32 v78, v79\n"
"v_add_f32 v82, v78, v79\n"
"v_permlane16_swap_b32 v80, v81\n"
"v_add_f32 v78, v80, v81\n"
"buffer_load_dwordx4 a[48:51], v1, s[4:7], s42 offen offset:1024\n"
"s_nop 0\n"
"v_permlane32_swap_b32 v82, v78\n"
"v_add_f32 v81, v82, v78\n"
"v_rcp_f32 v81, v81\n"
"s_nop 0\n"
"v_mov_b32 v79, v81\n"
"s_nop 1\n"
"v_permlane16_swap_b32 v81, v79\n"
"v_mov_b32 v80, v81\n"
"v_mov_b32 v78, v79\n"
"s_nop 0\n"
"v_permlane32_swap_b32 v81, v80\n"
"buffer_load_dwordx4 a[40:43], v1, s[4:7], s42 offen offset:2048\n"
"v_permlane32_swap_b32 v79, v78\n"
"v_mul_f32 v42, v42, v81\n"
"v_mul_f32 v43, v43, v81\n"
"v_mul_f32 v64, v64, v81\n"
"v_mul_f32 v65, v65, v81\n"
"v_mul_f32 v66, v66, v79\n"
"v_mul_f32 v67, v67, v79\n"
"v_mul_f32 v68, v68, v79\n"
"v_mul_f32 v69, v69, v79\n"
"v_mul_f32 v70, v70, v80\n"
"v_mul_f32 v71, v71, v80\n"
"s_waitcnt vmcnt(8)\n"
"v_max3_u32 v79, v56, v57, v58\n"
"v_mul_f32 v72, v72, v80\n"
"buffer_load_dwordx4 a[32:35], v1, s[4:7], s42 offen offset:3072\n"
"v_mul_f32 v73, v73, v80\n"
"v_mul_f32 v74, v74, v78\n"
"v_mul_f32 v76, v76, v78\n"
"v_mul_f32 v75, v75, v78\n"
"v_mul_f32 v77, v77, v78\n"
"v_cvt_pk_bf16_f32 v80, v42, v43\n"
"v_cvt_pk_bf16_f32 v81, v64, v65\n"
"v_cvt_pk_bf16_f32 v82, v66, v67\n"
"v_cvt_pk_bf16_f32 v83, v68, v69\n"
"v_cvt_pk_bf16_f32 v64, v70, v71\n"
"v_cvt_pk_bf16_f32 v65, v72, v73\n"
"v_cvt_pk_bf16_f32 v66, v74, v76\n"
"s_waitcnt vmcnt(8)\n"
"v_max3_u32 v42, v59, v60, v61\n"
"v_cvt_pk_bf16_f32 v67, v75, v77\n"
"ds_write_b128 v24, v[80:83]\n"
"ds_write_b128 v24, v[64:67] offset:1024\n"
"s_waitcnt vmcnt(7)\n"
"v_max3_u32 v43, v62, v63, v0\n"
"s_waitcnt vmcnt(6)\n"
"v_max3_u32 v64, v40, v79, v42\n"
"v_max_u32 v42, v43, v64\n"
"v_cmp_eq_u32 vcc, -1, v42\n"
"s_cbranch_vccnz Rred%=\n"
"Bred%=:\n"
"v_add_f32 v43, v0, v40\n"
"v_add_f32 v0, v56, v60\n"
"v_add_f32 v40, v57, v61\n"
"v_add_f32 v42, v58, v62\n"
"v_add_f32 v64, v59, v63\n"
"v_add_f32_dpp v56, v43, v43 row_ror:4 row_mask:0xf bank_mask:0xf\n"
"v_permlane16_swap_b32 v0, v40\n"
"v_permlane16_swap_b32 v42, v64\n"
"v_add_f32_dpp v43, v56, v56 row_ror:8 row_mask:0xf bank_mask:0xf\n"
"v_add_f32 v56, v0, v40\n"
"v_add_f32 v0, v42, v64\n"
"v_mov_b32 v40, v43\n"
"s_nop 0\n"
"v_permlane32_swap_b32 v56, v0\n"
"v_add_f32 v42, v56, v0\n"
"v_permlane16_swap_b32 v43, v40\n"
"v_add_f32 v0, v43, v40\n"
"v_add_f32_dpp v56, v42, v42 row_ror:4 row_mask:0xf bank_mask:0xf\n"
"v_mov_b32 v43, v0\n"
"s_nop 0\n"
"v_add_f32_dpp v40, v56, v56 row_ror:8 row_mask:0xf bank_mask:0xf\n"
"v_mov_b32 v42, v40\n"
"v_permlane32_swap_b32 v0, v43\n"
"v_add_f32 v56, v0, v43\n"
"v_permlane16_swap_b32 v40, v42\n"
"v_cvt_pk_bf16_f32 v43, v40, v42\n"
"v_mov_b32_dpp v40, v56 quad_perm:[1,1,3,3] row_mask:0xf bank_mask:0xf\n"
"v_cvt_pk_bf16_f32 v0, v56, v40\n"
"Cred%=:\n"
"buffer_store_dword v43, v32, s[20:23], s63 offen sc1\n"
"ds_read_b128 v[56:59], v9 offset:32768\n"
"s_mov_b64 s[28:29], s[100:101]\n"
"buffer_store_dword v0, v33, s[20:23], s64 offen sc1\n"
"ds_read_b128 v[64:67], v9 offset:49152\n"
"buffer_load_dwordx4 a[0:3], v2, s[4:7], s42 offen\n"
"ds_read_b128 v[68:71], v9 offset:33792\n"
"ds_read_b128 v[72:75], v9 offset:50176\n"
"ds_read_b128 v[80:83], v9 offset:34816\n"
"ds_read_b128 v[76:79], v9 offset:51200\n"
"s_waitcnt lgkmcnt(5)\n"
"v_mfma_f32_16x16x32_bf16 v[60:63], v[224:227], v[56:59], 0\n"
"buffer_load_dwordx4 a[4:7], v2, s[4:7], s42 offen offset:1024\n"
"ds_read_b128 v[84:87], v9 offset:35840\n"
"s_waitcnt lgkmcnt(5)\n"
"v_mfma_f32_16x16x32_bf16 v[56:59], v[224:227], v[64:67], 0\n"
"ds_read_b128 v[64:67], v9 offset:52224\n"
"ds_read_b128 v[224:227], v10 offset:32768\n"
"s_waitcnt lgkmcnt(6)\n"
"v_mfma_f32_16x16x32_bf16 v[60:63], v[228:231], v[68:71], v[60:63]\n"
"ds_read_b128 v[68:71], v10 offset:49152\n"
"buffer_load_dwordx4 a[8:11], v2, s[4:7], s42 offen offset:2048\n"
"s_waitcnt lgkmcnt(6)\n"
"v_mfma_f32_16x16x32_bf16 v[56:59], v[228:231], v[72:75], v[56:59]\n"
"ds_read_b128 v[72:75], v10 offset:33792\n"
"s_waitcnt lgkmcnt(6)\n"
"v_mfma_f32_16x16x32_bf16 v[60:63], v[232:235], v[80:83], v[60:63]\n"
"ds_read_b128 v[80:83], v10 offset:50176\n"
"s_waitcnt lgkmcnt(6)\n"
"v_mfma_f32_16x16x32_bf16 v[56:59], v[232:235], v[76:79], v[56:59]\n"
"ds_read_b128 v[76:79], v10 offset:34816\n"
"s_waitcnt lgkmcnt(6)\n"
"v_mfma_f32_16x16x32_bf16 v[60:63], v[236:239], v[84:87], v[60:63]\n"
"buffer_load_dwordx4 a[12:15], v2, s[4:7], s42 offen offset:3072\n"
"s_waitcnt lgkmcnt(5)\n"
"v_mfma_f32_16x16x32_bf16 v[56:59], v[236:239], v[64:67], v[56:59]\n"
"ds_read_b128 v[64:67], v10 offset:51200\n"
"s_waitcnt lgkmcnt(5)\n"
"v_mfma_f32_16x16x32_bf16 v[60:63], v[240:243], v[224:227], v[60:63]\n"
"ds_read_b128 v[224:227], v10 offset:35840\n"
"s_waitcnt lgkmcnt(5)\n"
"v_mfma_f32_16x16x32_bf16 v[56:59], v[240:243], v[68:71], v[56:59]\n"
"ds_read_b128 v[228:231], v10 offset:52224\n"
"s_waitcnt lgkmcnt(5)\n"
"v_mfma_f32_16x16x32_bf16 v[60:63], v[244:247], v[72:75], v[60:63]\n"
"buffer_load_dwordx4 a[16:19], v1, s[8:11], s44 offen\n"
"s_waitcnt lgkmcnt(4)\n"
"v_mfma_f32_16x16x32_bf16 v[56:59], v[244:247], v[80:83], v[56:59]\n"
"ds_read_b128 v[232:235], v11 offset:32768\n"
"s_waitcnt lgkmcnt(4)\n"
"v_mfma_f32_16x16x32_bf16 v[60:63], v[248:251], v[76:79], v[60:63]\n"
"ds_read_b128 v[236:239], v11 offset:49152\n"
"ds_read_b128 v[240:243], v11 offset:33792\n"
"s_waitcnt lgkmcnt(5)\n"
"v_mfma_f32_16x16x32_bf16 v[56:59], v[248:251], v[64:67], v[56:59]\n"
"buffer_load_dwordx4 a[20:23], v1, s[8:11], s44 offen offset:1024\n"
"s_waitcnt lgkmcnt(4)\n"
"v_mfma_f32_16x16x32_bf16 v[60:63], v[252:255], v[224:227], v[60:63]\n"
"ds_read_b128 v[244:247], v11 offset:50176\n"
"s_waitcnt lgkmcnt(4)\n"
"v_mfma_f32_16x16x32_bf16 v[56:59], v[252:255], v[228:231], v[56:59]\n"
"ds_read_b128 v[248:251], v11 offset:34816\n"
"ds_read_b128 v[252:255], v11 offset:51200\n"
"s_waitcnt lgkmcnt(5)\n"
"v_mfma_f32_16x16x32_bf16 v[60:63], a[252:255], v[232:235], v[60:63]\n"
"buffer_load_dwordx4 a[24:27], v1, s[8:11], s44 offen offset:2048\n"
"s_waitcnt lgkmcnt(4)\n"
"v_mfma_f32_16x16x32_bf16 v[56:59], a[252:255], v[236:239], v[56:59]\n"
"ds_read_b128 v[236:239], v11 offset:35840\n"
"s_waitcnt lgkmcnt(4)\n"
"v_mfma_f32_16x16x32_bf16 v[60:63], a[248:251], v[240:243], v[60:63]\n"
"ds_read_b128 a[252:255], v11 offset:52224\n"
"ds_read_b128 v[240:243], v12 offset:32768\n"
"s_waitcnt lgkmcnt(5)\n"
"v_mfma_f32_16x16x32_bf16 v[56:59], a[248:251], v[244:247], v[56:59]\n"
"buffer_load_dwordx4 a[28:31], v1, s[8:11], s44 offen offset:3072\n"
"s_waitcnt lgkmcnt(4)\n"
"v_mfma_f32_16x16x32_bf16 v[60:63], a[244:247], v[248:251], v[60:63]\n"
"ds_read_b128 v[244:247], v12 offset:49152\n"
"s_waitcnt lgkmcnt(4)\n"
"v_mfma_f32_16x16x32_bf16 v[56:59], a[244:247], v[252:255], v[56:59]\n"
"ds_read_b128 v[248:251], v12 offset:33792\n"
"ds_read_b128 v[252:255], v12 offset:50176\n"
"s_waitcnt lgkmcnt(5)\n"
"v_mfma_f32_16x16x32_bf16 v[60:63], a[240:243], v[236:239], v[60:63]\n"
"buffer_load_dwordx4 a[56:59], v3, s[4:7], s42 offen\n"
"s_waitcnt lgkmcnt(4)\n"
"v_mfma_f32_16x16x32_bf16 v[56:59], a[240:243], a[252:255], v[56:59]\n"
"ds_read_b128 a[252:255], v12 offset:34816\n"
"s_waitcnt lgkmcnt(4)\n"
"v_mfma_f32_16x16x32_bf16 v[60:63], a[152:155], v[240:243], v[60:63]\n"
"ds_read_b128 a[248:251], v12 offset:51200\n"
"ds_read_b128 a[244:247], v12 offset:35840\n"
"s_waitcnt lgkmcnt(5)\n"
"v_mfma_f32_16x16x32_bf16 v[56:59], a[152:155], v[244:247], v[56:59]\n"
"buffer_load_dwordx4 a[152:155], v3, s[4:7], s42 offen offset:1024\n"
"s_waitcnt lgkmcnt(4)\n"
"v_mfma_f32_16x16x32_bf16 v[60:63], a[148:151], v[248:251], v[60:63]\n"
"ds_read_b128 a[240:243], v12 offset:52224\n"
"s_waitcnt lgkmcnt(4)\n"
"v_mfma_f32_16x16x32_bf16 v[56:59], a[148:151], v[252:255], v[56:59]\n"
"ds_read_b128 v[64:67], v20\n"
"s_waitcnt lgkmcnt(4)\n"
"v_mfma_f32_16x16x32_bf16 v[60:63], a[156:159], a[252:255], v[60:63]\n"
"buffer_load_dwordx4 v[216:219], v2, s[8:11], s44 offen\n"
"s_waitcnt lgkmcnt(3)\n"
"v_mfma_f32_16x16x32_bf16 v[56:59], a[156:159], a[248:251], v[56:59]\n"
"s_waitcnt lgkmcnt(2)\n"
"v_mfma_f32_16x16x32_bf16 v[60:63], a[160:163], a[244:247], v[60:63]\n"
"s_waitcnt lgkmcnt(1)\n"
"v_mfma_f32_16x16x32_bf16 v[56:59], a[160:163], a[240:243], v[56:59]\n"
"buffer_load_dwordx4 v[208:211], v3, s[4:7], s42 offen offset:2048\n"
"s_nop 4\n"
"v_mul_f32 v0, v60, v45\n"
"s_waitcnt lgkmcnt(0)\n"
"v_fmac_f32 v0, v64, v46\n"
"v_mul_f32 v40, v61, v45\n"
"buffer_load_dwordx4 v[228:231], v3, s[4:7], s42 offen offset:3072\n"
"v_fmac_f32 v40, v65, v46\n"
"v_cvt_pk_bf16_f32 v42, v0, v40\n"
"v_mul_f32 v0, v62, v45\n"
"v_mul_f32 v40, v63, v45\n"
"v_mul_f32 v45, v56, v44\n"
"v_mul_f32 v60, v57, v44\n"
"v_mul_f32 v61, v58, v44\n"
"v_mul_f32 v62, v59, v44\n"
"v_fmac_f32 v0, v66, v46\n"
"v_fmac_f32 v40, v67, v46\n"
"v_fmac_f32 v45, v64, v47\n"
"v_fmac_f32 v60, v65, v47\n"
"v_fmac_f32 v61, v66, v47\n"
"buffer_load_dwordx4 v[224:227], v2, s[8:11], s44 offen offset:1024\n"
"v_fmac_f32 v62, v67, v47\n"
"v_cvt_pk_bf16_f32 v43, v0, v40\n"
"v_cvt_pk_bf16_f32 v64, v45, v60\n"
"v_cvt_pk_bf16_f32 v65, v61, v62\n"
"v_mfma_f32_16x16x16_bf16 v[56:59], a[128:129], v[42:43], 0\n"
"s_nop 0\n"
"v_mfma_f32_16x16x16_bf16 v[44:47], a[128:129], v[64:65], 0\n"
"buffer_load_dwordx4 v[212:215], v4, s[4:7], s42 offen\n"
"s_nop 4\n"
"v_max_f32 v0, v56, v57\n"
"v_min_f32 v42, v56, v57\n"
"buffer_load_dwordx4 v[232:235], v4, s[4:7], s42 offen offset:1024\n"
"v_max3_f32 v0, v0, v58, v59\n"
"v_max3_f32 v40, -v42, -v58, -v59\n"
"v_max_f32 v42, v44, v45\n"
"v_min_f32 v60, v44, v45\n"
"v_max3_f32 v42, v42, v46, v47\n"
"v_max3_f32 v43, -v60, -v46, -v47\n"
"buffer_load_dwordx4 a[148:151], v2, s[8:11], s44 offen offset:2048\n"
"v_permlane16_swap_b32 v0, v40\n"
"v_permlane16_swap_b32 v42, v43\n"
"v_max_f32 v60, v0, v40\n"
"v_max_f32 v0, v42, v43\n"
"s_nop 1\n"
"v_permlane32_swap_b32 v60, v0\n"
"v_max_f32 v63, v60, v0\n"
"v_mul_f32 v63, 0xbfb8aa3b, v63\n"
"v_mov_b32 v62, v63\n"
"s_nop 1\n"
"v_permlane16_swap_b32 v63, v62\n"
"buffer_load_dwordx4 v[220:223], v4, s[4:7], s42 offen offset:2048\n"
"v_mov_b32 v61, v63\n"
"v_mov_b32 v60, v62\n"
"s_nop 0\n"
"v_permlane32_swap_b32 v63, v61\n"
"v_fmamk_f32 v0, v56, 0x3fb8aa3b, v63\n"
"v_permlane32_swap_b32 v62, v60\n"
"v_fmamk_f32 v40, v57, 0x3fb8aa3b, v63\n"
"v_fmamk_f32 v42, v58, 0x3fb8aa3b, v63\n"
"v_fmamk_f32 v43, v59, 0x3fb8aa3b, v63\n"
"v_fmamk_f32 v64, v56, 0xbfb8aa3b, v62\n"
"v_fmamk_f32 v65, v57, 0xbfb8aa3b, v62\n"
"v_fmamk_f32 v66, v58, 0xbfb8aa3b, v62\n"
"v_fmamk_f32 v67, v59, 0xbfb8aa3b, v62\n"
"buffer_load_dwordx4 a[160:163], v4, s[4:7], s42 offen offset:3072\n"
"v_fmamk_f32 v68, v44, 0x3fb8aa3b, v61\n"
"v_fmamk_f32 v69, v45, 0x3fb8aa3b, v61\n"
"v_fmamk_f32 v70, v46, 0x3fb8aa3b, v61\n"
"v_fmamk_f32 v71, v47, 0x3fb8aa3b, v61\n"
"v_fmamk_f32 v72, v44, 0xbfb8aa3b, v60\n"
"v_fmamk_f32 v73, v45, 0xbfb8aa3b, v60\n"
"v_fmamk_f32 v74, v46, 0xbfb8aa3b, v60\n"
"v_fmamk_f32 v75, v47, 0xbfb8aa3b, v60\n"
"v_exp_f32 v0, v0\n"
"v_exp_f32 v40, v40\n"
"v_exp_f32 v42, v42\n"
"buffer_load_dwordx4 v[240:243], v2, s[8:11], s44 offen offset:3072\n"
"v_exp_f32 v43, v43\n"
"v_exp_f32 v64, v64\n"
"v_exp_f32 v65, v65\n"
"v_exp_f32 v66, v66\n"
"v_exp_f32 v67, v67\n"
"v_exp_f32 v68, v68\n"
"v_exp_f32 v69, v69\n"
"buffer_load_dwordx4 v[44:47], v1, s[20:23], s65 offen sc1\n"
"v_exp_f32 v70, v70\n"
"v_exp_f32 v71, v71\n"
"v_exp_f32 v72, v72\n"
"v_exp_f32 v73, v73\n"
"v_exp_f32 v74, v74\n"
"v_exp_f32 v75, v75\n"
"v_add_f32 v60, v0, v40\n"
"buffer_load_dwordx4 v[56:59], v34, s[20:23], s66 offen sc1\n"
"v_add_f32 v62, v42, v43\n"
"v_add_f32 v61, v64, v65\n"
"v_add_f32 v63, v66, v67\n"
"v_add_f32 v76, v68, v69\n"
"v_add_f32 v78, v70, v71\n"
"v_add_f32 v77, v72, v73\n"
"v_add_f32 v79, v74, v75\n"
"v_add_f32 v60, v60, v62\n"
"v_add_f32 v61, v61, v63\n"
"v_add_f32 v76, v76, v78\n"
"v_add_f32 v77, v77, v79\n"
"v_permlane16_swap_b32 v60, v61\n"
"v_add_f32 v78, v60, v61\n"
"buffer_load_dwordx4 v[60:63], v1, s[20:23], s65 offen offset:1024 sc1\n"
"v_permlane16_swap_b32 v76, v77\n"
"v_add_f32 v79, v76, v77\n"
"s_nop 1\n"
"v_permlane32_swap_b32 v78, v79\n"
"v_add_f32 v80, v78, v79\n"
"v_rcp_f32 v80, v80\n"
"s_nop 0\n"
"v_mov_b32 v83, v80\n"
"s_nop 1\n"
"v_permlane16_swap_b32 v80, v83\n"
"buffer_load_dwordx4 v[76:79], v34, s[20:23], s66 offen offset:64 sc1\n"
"v_mov_b32 v82, v80\n"
"v_mov_b32 v81, v83\n"
"s_nop 0\n"
"v_permlane32_swap_b32 v80, v82\n"
"v_mul_f32 v0, v0, v80\n"
"v_permlane32_swap_b32 v83, v81\n"
"v_mul_f32 v40, v40, v80\n"
"v_mul_f32 v42, v42, v80\n"
"v_mul_f32 v43, v43, v80\n"
"v_mul_f32 v64, v64, v83\n"
"v_mul_f32 v65, v65, v83\n"
"v_mul_f32 v66, v66, v83\n"
"buffer_load_dword v80, v41, s[20:23], 0 offen sc1\n"
"v_mul_f32 v67, v67, v83\n"
"v_mul_f32 v68, v68, v82\n"
"v_mul_f32 v69, v69, v82\n"
"v_mul_f32 v70, v70, v82\n"
"v_mul_f32 v71, v71, v82\n"
"v_mul_f32 v72, v72, v81\n"
"v_mul_f32 v73, v73, v81\n"
"v_mul_f32 v74, v74, v81\n"
"v_mul_f32 v75, v75, v81\n"
"v_cvt_pk_bf16_f32 v84, v0, v40\n"
"v_cvt_pk_bf16_f32 v85, v42, v43\n"
"v_cvt_pk_bf16_f32 v86, v64, v65\n"
"v_cvt_pk_bf16_f32 v87, v66, v67\n"
"v_cvt_pk_bf16_f32 v64, v68, v69\n"
"v_cvt_pk_bf16_f32 v65, v70, v71\n"
"v_cvt_pk_bf16_f32 v66, v72, v73\n"
"v_cvt_pk_bf16_f32 v67, v74, v75\n"
"ds_write_b128 v24, v[84:87] offset:2048\n"
"ds_write_b128 v24, v[64:67] offset:3072\n"
"s_waitcnt lgkmcnt(0)\n"
"s_barrier\n"
"ds_read_b128 a[248:251], v25\n"
"ds_read_b128 a[244:247], v25 offset:4096\n"
"ds_read_b128 a[240:243], v25 offset:2048\n"
"ds_read_b128 a[156:159], v25 offset:6144\n"
"s_waitcnt vmcnt(4)\n"
"v_max3_u32 v0, v44, v45, v46\n"
"s_waitcnt vmcnt(3)\n"
"v_max3_u32 v40, v47, v56, v57\n"
"s_waitcnt vmcnt(2)\n"
"v_max3_u32 v43, v58, v59, v60\n"
"v_max3_u32 v64, v61, v62, v63\n"
"v_max3_u32 v42, v40, v43, v64\n"
"s_waitcnt vmcnt(1)\n"
"v_max3_u32 v40, v76, v77, v78\n"
"s_waitcnt vmcnt(0)\n"
"v_max3_u32 v43, v79, v80, v0\n"
"v_max3_u32 v0, v40, v43, v42\n"
"v_cmp_eq_u32 vcc, -1, v0\n"
"s_cbranch_vccnz RO%=\n"
"BO%=:\n"
"s_waitcnt lgkmcnt(3)\n"
"v_mfma_f32_16x16x32_bf16 v[88:91], v[56:59], a[248:251], 0\n"
"s_waitcnt lgkmcnt(2)\n"
"v_mfma_f32_16x16x32_bf16 v[72:75], v[76:79], a[244:247], 0\n"
"s_waitcnt lgkmcnt(1)\n"
"v_mfma_f32_16x16x32_bf16 v[80:83], v[56:59], a[240:243], 0\n"
"s_waitcnt lgkmcnt(0)\n"
"v_mfma_f32_16x16x32_bf16 v[84:87], v[76:79], a[156:159], 0\n"
"v_mfma_f32_16x16x32_bf16 v[68:71], v[44:47], a[248:251], 0\n"
"v_mfma_f32_16x16x32_bf16 v[64:67], v[60:63], a[244:247], 0\n"
"v_mfma_f32_16x16x32_bf16 v[56:59], v[44:47], a[240:243], 0\n"
"v_mfma_f32_16x16x32_bf16 v[44:47], v[60:63], a[156:159], 0\n"
"v_add_f32 v43, 0x358637bd, v88\n"
"v_rcp_f32 v43, v43\n"
"v_add_f32 v42, 0x358637bd, v72\n"
"v_add_f32 v40, 0x358637bd, v80\n"
"v_add_f32 v0, 0x358637bd, v84\n"
"v_rcp_f32 v42, v42\n"
"v_rcp_f32 v40, v40\n"
"v_rcp_f32 v0, v0\n"
"v_mul_f32 v68, v68, v43\n"
"v_mul_f32 v69, v69, v43\n"
"v_mul_f32 v70, v70, v43\n"
"v_mul_f32 v71, v71, v43\n"
"v_mul_f32 v64, v64, v42\n"
"v_mul_f32 v65, v65, v42\n"
"v_mul_f32 v66, v66, v42\n"
"v_mul_f32 v67, v67, v42\n"
"v_mul_f32 v56, v56, v40\n"
"v_mul_f32 v57, v57, v40\n"
"v_mul_f32 v58, v58, v40\n"
"v_mul_f32 v59, v59, v40\n"
"v_mul_f32 v44, v44, v0\n"
"v_mul_f32 v45, v45, v0\n"
"v_mul_f32 v46, v46, v0\n"
"v_mul_f32 v47, v47, v0\n"
"v_cvt_pk_bf16_f32 v60, v68, v69\n"
"v_cvt_pk_bf16_f32 v61, v70, v71\n"
"v_cvt_pk_bf16_f32 v62, v64, v65\n"
"v_cvt_pk_bf16_f32 v63, v66, v67\n"
"v_cvt_pk_bf16_f32 v64, v56, v57\n"
"v_cvt_pk_bf16_f32 v65, v58, v59\n"
"v_cvt_pk_bf16_f32 v66, v44, v45\n"
"v_cvt_pk_bf16_f32 v67, v46, v47\n"
"CO%=:\n"
"buffer_store_dwordx4 v[60:63], v1, s[20:23], s52 offen sc1\n"
"s_add_u32 s1, s52, 0x400000\n"
"s_add_u32 s0, s48, 0x400000\n"
"buffer_store_dwordx4 v[64:67], v1, s[20:23], s1 offen sc1\n"
"buffer_load_dwordx4 v[236:239], v1, s[4:7], s43 offen\n"
"buffer_load_dwordx4 v[252:255], v1, s[4:7], s43 offen offset:1024\n"
"buffer_load_dwordx4 v[248:251], v3, s[8:11], s44 offen\n"
"buffer_load_dwordx4 v[244:247], v1, s[4:7], s43 offen offset:2048\n"
"buffer_load_dwordx4 a[248:251], v1, s[4:7], s43 offen offset:3072\n"
"buffer_load_dwordx4 a[252:255], v3, s[8:11], s44 offen offset:1024\n"
"buffer_load_dwordx4 a[156:159], v2, s[4:7], s43 offen\n"
"buffer_load_dwordx4 a[240:243], v2, s[4:7], s43 offen offset:1024\n"
"buffer_load_dwordx4 a[36:39], v3, s[8:11], s44 offen offset:2048\n"
"buffer_load_dwordx4 a[244:247], v2, s[4:7], s43 offen offset:2048\n"
"s_sleep 13\n"
"buffer_load_dword v0, v37, s[20:23], s0 offen sc1\n"
"s_waitcnt vmcnt(0)\n"
"v_cmp_eq_u32 vcc, -1, v0\n"
"s_cbranch_vccnz RwoA%=\n"
"BwoA%=:\n"
"buffer_load_dwordx4 a[68:71], v1, s[20:23], s50 offen sc1\n"
"s_add_u32 s1, s50, 0x400000\n"
"s_add_u32 s0, s49, 0x400000\n"
"v_lshlrev_b32 v60, 16, v48\n"
"v_and_b32 v61, s72, v48\n"
"v_lshlrev_b32 v62, 16, v49\n"
"v_and_b32 v63, s72, v49\n"
"v_lshlrev_b32 v64, 16, v50\n"
"v_and_b32 v65, s72, v50\n"
"v_lshlrev_b32 v66, 16, v51\n"
"v_and_b32 v67, s72, v51\n"
"buffer_load_dwordx4 v[92:95], v1, s[20:23], s50 offen offset:1024 sc1\n"
"buffer_load_dwordx4 v[84:87], v1, s[20:23], s50 offen offset:2048 sc1\n"
"buffer_load_dwordx4 v[56:59], v1, s[20:23], s50 offen offset:3072 sc1\n"
"buffer_load_dwordx4 v[160:163], v1, s[20:23], s49 offen sc1\n"
"buffer_load_dwordx4 v[76:79], v1, s[20:23], s49 offen offset:1024 sc1\n"
"buffer_load_dwordx4 v[72:75], v1, s[20:23], s49 offen offset:2048 sc1\n"
"buffer_load_dwordx4 v[80:83], v1, s[20:23], s49 offen offset:3072 sc1\n"
"s_waitcnt vmcnt(7)\n"
"v_mfma_f32_16x16x32_bf16 v[44:47], a[200:203], a[68:71], 0\n"
"buffer_load_dwordx4 v[68:71], v1, s[20:23], s1 offen sc1\n"
"v_mfma_f32_16x16x32_bf16 a[88:91], a[204:207], a[68:71], 0\n"
"v_mfma_f32_16x16x32_bf16 v[96:99], a[136:139], a[68:71], 0\n"
"v_mfma_f32_16x16x32_bf16 v[88:91], a[132:135], a[68:71], 0\n"
"s_waitcnt vmcnt(7)\n"
"v_mfma_f32_16x16x32_bf16 v[44:47], v[172:175], v[92:95], v[44:47]\n"
"buffer_load_dwordx4 a[96:99], v1, s[20:23], s1 offen offset:1024 sc1\n"
"v_mfma_f32_16x16x32_bf16 a[88:91], v[168:171], v[92:95], a[88:91]\n"
"v_mfma_f32_16x16x32_bf16 v[96:99], a[208:211], v[92:95], v[96:99]\n"
"v_mfma_f32_16x16x32_bf16 v[88:91], v[176:179], v[92:95], v[88:91]\n"
"buffer_load_dwordx4 a[112:115], v1, s[20:23], s1 offen offset:2048 sc1\n"
"s_waitcnt vmcnt(8)\n"
"v_mfma_f32_16x16x32_bf16 v[44:47], a[80:83], v[84:87], v[44:47]\n"
"v_mfma_f32_16x16x32_bf16 a[88:91], a[44:47], v[84:87], a[88:91]\n"
"v_mfma_f32_16x16x32_bf16 v[96:99], v[164:167], v[84:87], v[96:99]\n"
"v_mfma_f32_16x16x32_bf16 v[88:91], a[64:67], v[84:87], v[88:91]\n"
"buffer_load_dwordx4 a[68:71], v1, s[20:23], s1 offen offset:3072 sc1\n"
"s_waitcnt vmcnt(8)\n"
"v_mfma_f32_16x16x32_bf16 v[44:47], a[60:63], v[56:59], v[44:47]\n"
"v_mfma_f32_16x16x32_bf16 a[88:91], a[76:79], v[56:59], a[88:91]\n"
"v_mfma_f32_16x16x32_bf16 v[96:99], a[104:107], v[56:59], v[96:99]\n"
"v_mfma_f32_16x16x32_bf16 v[88:91], a[52:55], v[56:59], v[88:91]\n"
"buffer_load_dwordx4 a[120:123], v1, s[20:23], s0 offen sc1\n"
"s_waitcnt vmcnt(8)\n"
"v_mfma_f32_16x16x32_bf16 v[56:59], a[200:203], v[160:163], 0\n"
"v_mfma_f32_16x16x32_bf16 v[84:87], a[204:207], v[160:163], 0\n"
"v_mfma_f32_16x16x32_bf16 v[60:63], a[136:139], v[160:163], v[60:63]\n"
"ds_write_b128 v15, v[44:47]\n"
"buffer_load_dwordx4 v[92:95], v1, s[20:23], s0 offen offset:1024 sc1\n"
"v_mfma_f32_16x16x32_bf16 v[64:67], a[132:135], v[160:163], v[64:67]\n"
"ds_write_b128 v15, a[88:91] offset:1024\n"
"s_waitcnt vmcnt(8)\n"
"v_mfma_f32_16x16x32_bf16 v[56:59], v[172:175], v[76:79], v[56:59]\n"
"ds_write_b128 v14, v[96:99]\n"
"v_mfma_f32_16x16x32_bf16 v[84:87], v[168:171], v[76:79], v[84:87]\n"
"buffer_load_dwordx4 v[96:99], v1, s[20:23], s0 offen offset:2048 sc1\n"
"v_mfma_f32_16x16x32_bf16 v[60:63], a[208:211], v[76:79], v[60:63]\n"
"ds_write_b128 v14, v[88:91] offset:1024\n"
"s_waitcnt vmcnt(8)\n"
"v_mfma_f32_16x16x32_bf16 v[56:59], a[80:83], v[72:75], v[56:59]\n"
"v_mfma_f32_16x16x32_bf16 v[84:87], a[44:47], v[72:75], v[84:87]\n"
"buffer_load_dwordx4 v[88:91], v1, s[20:23], s0 offen offset:3072 sc1\n"
"v_mfma_f32_16x16x32_bf16 v[64:67], v[176:179], v[76:79], v[64:67]\n"
"v_mfma_f32_16x16x32_bf16 v[60:63], v[164:167], v[72:75], v[60:63]\n"
"s_waitcnt vmcnt(8)\n"
"v_mfma_f32_16x16x32_bf16 v[56:59], a[60:63], v[80:83], v[56:59]\n"
"v_mfma_f32_16x16x32_bf16 v[84:87], a[76:79], v[80:83], v[84:87]\n"
"v_mfma_f32_16x16x32_bf16 v[64:67], a[64:67], v[72:75], v[64:67]\n"
"v_mfma_f32_16x16x32_bf16 v[60:63], a[104:107], v[80:83], v[60:63]\n"
"v_mfma_f32_16x16x32_bf16 v[64:67], a[52:55], v[80:83], v[64:67]\n"
"s_nop 3\n"
"ds_write_b128 v13, v[56:59]\n"
"v_cmp_u_f32 vcc, v44, v56\n"
"ds_write_b128 v13, v[84:87] offset:1024\n"
"s_cbranch_vccnz RwoA%=\n"
"CwoA%=:\n"
"s_waitcnt lgkmcnt(0)\n"
"s_barrier\n"
"ds_read_b128 v[76:79], v16\n"
"s_add_u32 s1, s52, 0x800000\n"
"s_add_u32 s0, s53, 0x900000\n"
"s_waitcnt vmcnt(7)\n"
"v_mfma_f32_16x16x32_bf16 v[44:47], a[200:203], v[68:71], 0\n"
"ds_read_b128 v[48:51], v16 offset:2048\n"
"v_mov_b32 v56, -1\n"
"v_mfma_f32_16x16x32_bf16 v[156:159], a[204:207], v[68:71], 0\n"
"ds_read_b128 v[80:83], v16 offset:4096\n"
"v_mov_b32 v57, -1\n"
"v_mfma_f32_16x16x32_bf16 v[160:163], a[136:139], v[68:71], 0\n"
"ds_read_b128 v[72:75], v16 offset:1024\n"
"v_mov_b32 v58, -1\n"
"v_mfma_f32_16x16x32_bf16 a[88:91], a[132:135], v[68:71], 0\n"
"ds_read_b128 v[68:71], v16 offset:3072\n"
"v_mov_b32 v59, -1\n"
"s_waitcnt vmcnt(6)\n"
"v_mfma_f32_16x16x32_bf16 v[44:47], v[172:175], a[96:99], v[44:47]\n"
"ds_read_b128 v[84:87], v16 offset:5120\n"
"s_add_u32 s2, s52, 0x1400000\n"
"buffer_store_dwordx4 v[56:59], v1, s[24:27], s2 offen sc1\n"
"s_waitcnt lgkmcnt(5)\n"
"v_pk_add_f32 v[60:61], v[60:61], v[76:77]\n"
"v_pk_add_f32 v[62:63], v[62:63], v[78:79]\n"
"v_mfma_f32_16x16x32_bf16 v[156:159], v[168:171], a[96:99], v[156:159]\n"
"s_add_u32 s3, s52, 0x1800000\n"
"s_add_u32 s2, s53, 0x1900000\n"
"v_mfma_f32_16x16x32_bf16 v[160:163], a[208:211], a[96:99], v[160:163]\n"
"s_mul_i32 m0, s68, 0\n"
"s_waitcnt lgkmcnt(3)\n"
"v_pk_add_f32 v[48:49], v[48:49], v[80:81]\n"
"v_pk_add_f32 v[50:51], v[50:51], v[82:83]\n"
"v_pk_add_f32 v[60:61], v[60:61], v[48:49]\n"
"buffer_store_dwordx4 v[56:59], v1, s[24:27], s3 offen sc1\n"
"s_waitcnt lgkmcnt(2)\n"
"v_pk_add_f32 v[64:65], v[64:65], v[72:73]\n"
"v_pk_add_f32 v[66:67], v[66:67], v[74:75]\n"
"v_pk_add_f32 v[62:63], v[62:63], v[50:51]\n"
"v_cvt_pk_bf16_f32 v48, v60, v61\n"
"s_waitcnt lgkmcnt(0)\n"
"v_pk_add_f32 v[68:69], v[68:69], v[84:85]\n"
"v_pk_add_f32 v[70:71], v[70:71], v[86:87]\n"
"v_pk_add_f32 v[64:65], v[64:65], v[68:69]\n"
"v_pk_add_f32 v[66:67], v[66:67], v[70:71]\n"
"v_cvt_pk_bf16_f32 v49, v62, v63\n"
"v_cvt_pk_bf16_f32 v50, v64, v65\n"
"v_cvt_pk_bf16_f32 v51, v66, v67\n"
"v_pk_add_f32 v[42:43], v[60:61], v[62:63]\n"
"buffer_store_dwordx4 v[48:51], v1, s[20:23], s1 offen sc1\n"
"v_pk_add_f32 v[68:69], v[64:65], v[66:67]\n"
"v_pk_add_f32 v[42:43], v[42:43], v[68:69]\n"
"v_add_f32 v0, v42, v43\n"
"buffer_store_dwordx2 v[56:57], v5, s[24:27], s2 offen sc1\n"
"v_mov_b32 v42, v0\n"
"s_waitcnt vmcnt(7)\n"
"v_mfma_f32_16x16x32_bf16 v[68:71], a[200:203], a[120:123], 0\n"
"s_add_u32 m0, m0, s69\n"
"v_permlane16_swap_b32 v0, v42\n"
"v_add_f32 v40, v0, v42\n"
"v_mov_b32 v0, v40\n"
"v_mfma_f32_16x16x32_bf16 v[84:87], a[204:207], a[120:123], 0\n"
"v_lshlrev_b32 v56, 16, v52\n"
"v_permlane32_swap_b32 v40, v0\n"
"v_add_f32 v72, v40, v0\n"
"buffer_load_dwordx4 v1, s[16:19], s45 offen offset:0 lds\n"
"v_mul_f32 v42, 0x3d000000, v72\n"
"v_mul_f32 v72, 0xbd000000, v72\n"
"v_pk_add_f32 v[74:75], v[60:61], v[72:73] op_sel_hi:[1,0]\n"
"v_pk_add_f32 v[78:79], v[64:65], v[72:73] op_sel_hi:[1,0]\n"
"v_pk_add_f32 v[76:77], v[62:63], v[72:73] op_sel_hi:[1,0]\n"
"v_pk_add_f32 v[62:63], v[66:67], v[72:73] op_sel_hi:[1,0]\n"
"v_pk_mul_f32 v[60:61], v[74:75], v[74:75]\n"
"v_pk_mul_f32 v[64:65], v[78:79], v[78:79]\n"
"v_pk_fma_f32 v[60:61], v[76:77], v[76:77], v[60:61]\n"
"v_pk_fma_f32 v[64:65], v[62:63], v[62:63], v[64:65]\n"
"v_pk_add_f32 v[60:61], v[60:61], v[64:65]\n"
"v_add_f32 v40, v60, v61\n"
"v_mov_b32 v60, v40\n"
"v_mfma_f32_16x16x32_bf16 a[88:91], v[176:179], a[96:99], a[88:91]\n"
"s_mul_i32 m0, s68, 0\n"
"v_permlane16_swap_b32 v40, v60\n"
"v_add_f32 v0, v40, v60\n"
"v_mov_b32 v40, v0\n"
"s_waitcnt vmcnt(7)\n"
"v_mfma_f32_16x16x32_bf16 v[68:71], v[172:175], v[92:95], v[68:71]\n"
"s_add_u32 m0, m0, s69\n"
"v_permlane32_swap_b32 v0, v40\n"
"buffer_load_dwordx4 v1, s[16:19], s45 offen offset:1024 lds\n"
"v_add_f32 v43, v0, v40\n"
"v_mfma_f32_16x16x32_bf16 v[84:87], v[168:171], v[92:95], v[84:87]\n"
"s_mul_i32 m0, s68, 0\n"
"s_add_u32 m0, m0, s69\n"
"v_mfma_f32_16x16x32_bf16 v[44:47], a[80:83], a[112:115], v[44:47]\n"
"v_and_b32 v57, s72, v52\n"
"v_lshlrev_b32 v58, 16, v53\n"
"v_mfma_f32_16x16x32_bf16 v[156:159], a[44:47], a[112:115], v[156:159]\n"
"v_and_b32 v59, s72, v53\n"
"v_lshlrev_b32 v60, 16, v54\n"
"v_mfma_f32_16x16x32_bf16 v[160:163], v[164:167], a[112:115], v[160:163]\n"
"buffer_store_dwordx2 v[42:43], v5, s[20:23], s0 offen sc1\n"
"v_mfma_f32_16x16x32_bf16 a[88:91], a[64:67], a[112:115], a[88:91]\n"
"v_and_b32 v61, s72, v54\n"
"v_lshlrev_b32 v62, 16, v55\n"
"buffer_load_dwordx4 v1, s[16:19], s45 offen offset:2048 lds\n"
"s_waitcnt vmcnt(9)\n"
"v_mfma_f32_16x16x32_bf16 v[68:71], a[80:83], v[96:99], v[68:71]\n"
"s_mul_i32 m0, s68, 0\n"
"s_add_u32 m0, m0, s69\n"
"v_mfma_f32_16x16x32_bf16 v[84:87], a[44:47], v[96:99], v[84:87]\n"
"v_and_b32 v63, s72, v55\n"
"v_mfma_f32_16x16x32_bf16 v[56:59], a[136:139], a[120:123], v[56:59]\n"
"s_nop 0\n"
"v_mfma_f32_16x16x32_bf16 v[60:63], a[132:135], a[120:123], v[60:63]\n"
"buffer_load_dwordx4 v1, s[16:19], s45 offen offset:3072 lds\n"
"v_mfma_f32_16x16x32_bf16 v[44:47], a[60:63], a[68:71], v[44:47]\n"
"s_mul_i32 m0, s68, 1\n"
"s_add_u32 m0, m0, s69\n"
"v_mfma_f32_16x16x32_bf16 v[156:159], a[76:79], a[68:71], v[156:159]\n"
"v_mfma_f32_16x16x32_bf16 v[160:163], a[104:107], a[68:71], v[160:163]\n"
"v_mfma_f32_16x16x32_bf16 a[88:91], a[52:55], a[68:71], a[88:91]\n"
"buffer_load_dwordx4 v2, s[16:19], s45 offen offset:0 lds\n"
"s_waitcnt vmcnt(10)\n"
"v_mfma_f32_16x16x32_bf16 v[68:71], a[60:63], v[88:91], v[68:71]\n"
"s_mul_i32 m0, s68, 1\n"
"s_add_u32 m0, m0, s69\n"
"v_mfma_f32_16x16x32_bf16 v[84:87], a[76:79], v[88:91], v[84:87]\n"
"ds_write_b128 v15, v[44:47] offset:32768\n"
"v_mfma_f32_16x16x32_bf16 v[56:59], a[208:211], v[92:95], v[56:59]\n"
"ds_write_b128 v15, v[156:159] offset:33792\n"
"buffer_load_dwordx4 v2, s[16:19], s45 offen offset:1024 lds\n"
"v_mfma_f32_16x16x32_bf16 v[60:63], v[176:179], v[92:95], v[60:63]\n"
"s_mul_i32 m0, s68, 1\n"
"s_add_u32 m0, m0, s69\n"
"v_mfma_f32_16x16x32_bf16 v[56:59], v[164:167], v[96:99], v[56:59]\n"
"ds_write_b128 v14, v[160:163] offset:32768\n"
"v_mfma_f32_16x16x32_bf16 v[60:63], a[64:67], v[96:99], v[60:63]\n"
"ds_write_b128 v14, a[88:91] offset:33792\n"
"buffer_load_dwordx4 v2, s[16:19], s45 offen offset:2048 lds\n"
"v_mfma_f32_16x16x32_bf16 v[56:59], a[104:107], v[88:91], v[56:59]\n"
"s_mul_i32 m0, s68, 1\n"
"s_add_u32 m0, m0, s69\n"
"v_mfma_f32_16x16x32_bf16 v[60:63], a[52:55], v[88:91], v[60:63]\n"
"ds_write_b128 v13, v[68:71] offset:32768\n"
"v_cmp_u_f32 vcc, v44, v68\n"
"ds_write_b128 v13, v[84:87] offset:33792\n"
"buffer_load_dwordx4 v2, s[16:19], s45 offen offset:3072 lds\n"
"s_mul_i32 m0, s68, 2\n"
"s_add_u32 m0, m0, s69\n"
"s_nop 0\n"
"buffer_load_dwordx4 v3, s[16:19], s45 offen offset:0 lds\n"
"s_mul_i32 m0, s68, 2\n"
"s_add_u32 m0, m0, s69\n"
"s_nop 0\n"
"buffer_load_dwordx4 v3, s[16:19], s45 offen offset:1024 lds\n"
"s_cbranch_vccnz RwoB%=\n"
"CwoB%=:\n"
"s_waitcnt lgkmcnt(0)\n"
"s_barrier\n"
"s_add_u32 s32, s50, 0x800000\n"
"ds_read_b128 v[44:47], v16 offset:32768\n"
"s_add_u32 s2, s49, 0x800000\n"
"s_add_u32 s0, s51, 0x900000\n"
"s_add_u32 s33, s52, 0xc00000\n"
"ds_read_b128 v[64:67], v16 offset:34816\n"
"s_add_u32 s3, s53, 0xd00000\n"
"v_mov_b32 v52, -1\n"
"v_mov_b32 v53, -1\n"
"ds_read_b128 v[80:83], v16 offset:36864\n"
"v_mov_b32 v54, -1\n"
"buffer_load_dwordx4 a[208:211], v1, s[20:23], s32 offen sc1\n"
"ds_read_b128 v[72:75], v16 offset:33792\n"
"v_mov_b32 v55, -1\n"
"s_add_u32 s1, s54, 0x10000\n"
"ds_read_b128 v[68:71], v16 offset:35840\n"
"ds_read_b128 v[76:79], v16 offset:37888\n"
"buffer_load_dwordx4 a[204:207], v1, s[20:23], s32 offen offset:1024 sc1\n"
"s_waitcnt lgkmcnt(5)\n"
"v_pk_add_f32 v[56:57], v[56:57], v[44:45]\n"
"v_pk_add_f32 v[58:59], v[58:59], v[46:47]\n"
"s_waitcnt lgkmcnt(3)\n"
"v_pk_add_f32 v[64:65], v[64:65], v[80:81]\n"
"v_pk_add_f32 v[66:67], v[66:67], v[82:83]\n"
"v_pk_add_f32 v[56:57], v[56:57], v[64:65]\n"
"v_pk_add_f32 v[58:59], v[58:59], v[66:67]\n"
"v_cvt_pk_bf16_f32 v44, v56, v57\n"
"s_waitcnt lgkmcnt(2)\n"
"v_pk_add_f32 v[60:61], v[60:61], v[72:73]\n"
"buffer_load_dwordx4 a[200:203], v1, s[20:23], s32 offen offset:2048 sc1\n"
"v_pk_add_f32 v[62:63], v[62:63], v[74:75]\n"
"v_cvt_pk_bf16_f32 v45, v58, v59\n"
"v_pk_add_f32 v[42:43], v[56:57], v[58:59]\n"
"s_waitcnt lgkmcnt(0)\n"
"v_pk_add_f32 v[68:69], v[68:69], v[76:77]\n"
"v_pk_add_f32 v[70:71], v[70:71], v[78:79]\n"
"v_pk_add_f32 v[60:61], v[60:61], v[68:69]\n"
"v_pk_add_f32 v[62:63], v[62:63], v[70:71]\n"
"v_cvt_pk_bf16_f32 v46, v60, v61\n"
"v_cvt_pk_bf16_f32 v47, v62, v63\n"
"v_pk_add_f32 v[64:65], v[60:61], v[62:63]\n"
"v_pk_add_f32 v[42:43], v[42:43], v[64:65]\n"
"v_add_f32 v0, v42, v43\n"
"buffer_store_dwordx4 v[44:47], v1, s[20:23], s33 offen sc1\n"
"v_mov_b32 v40, v0\n"
"s_nop 1\n"
"v_permlane16_swap_b32 v0, v40\n"
"buffer_load_dwordx4 v[76:79], v1, s[20:23], s32 offen offset:3072 sc1\n"
"v_add_f32 v42, v0, v40\n"
"v_mov_b32 v0, v42\n"
"s_nop 1\n"
"v_permlane32_swap_b32 v42, v0\n"
"v_add_f32 v64, v42, v0\n"
"v_mul_f32 v42, 0x3d000000, v64\n"
"v_mul_f32 v64, 0xbd000000, v64\n"
"v_pk_add_f32 v[66:67], v[56:57], v[64:65] op_sel_hi:[1,0]\n"
"v_pk_add_f32 v[70:71], v[60:61], v[64:65] op_sel_hi:[1,0]\n"
"v_pk_add_f32 v[68:69], v[58:59], v[64:65] op_sel_hi:[1,0]\n"
"v_pk_add_f32 v[56:57], v[62:63], v[64:65] op_sel_hi:[1,0]\n"
"buffer_load_dwordx4 a[104:107], v1, s[20:23], s2 offen sc1\n"
"v_pk_mul_f32 v[58:59], v[66:67], v[66:67]\n"
"v_pk_mul_f32 v[60:61], v[70:71], v[70:71]\n"
"v_pk_fma_f32 v[58:59], v[68:69], v[68:69], v[58:59]\n"
"v_pk_fma_f32 v[60:61], v[56:57], v[56:57], v[60:61]\n"
"v_pk_add_f32 v[58:59], v[58:59], v[60:61]\n"
"v_add_f32 v40, v58, v59\n"
"v_mov_b32 v56, v40\n"
"s_nop 1\n"
"v_permlane16_swap_b32 v40, v56\n"
"v_add_f32 v0, v40, v56\n"
"v_mov_b32 v40, v0\n"
"buffer_load_dwordx4 a[60:63], v1, s[20:23], s2 offen offset:1024 sc1\n"
"s_nop 0\n"
"v_permlane32_swap_b32 v0, v40\n"
"v_add_f32 v43, v0, v40\n"
"buffer_store_dwordx2 v[42:43], v5, s[20:23], s3 offen sc1\n"
"buffer_load_dwordx4 v[80:83], v1, s[20:23], s2 offen offset:2048 sc1\n"
"s_waitcnt vmcnt(8)\n"
"v_mfma_f32_16x16x32_bf16 v[64:67], a[124:127], a[208:211], 0\n"
"buffer_load_dwordx4 v[72:75], v1, s[20:23], s2 offen offset:3072 sc1\n"
"v_mfma_f32_16x16x32_bf16 a[64:67], a[116:119], a[208:211], 0\n"
"v_mfma_f32_16x16x32_bf16 a[52:55], a[92:95], a[208:211], 0\n"
"v_mfma_f32_16x16x32_bf16 a[44:47], a[108:111], a[208:211], 0\n"
"s_waitcnt vmcnt(8)\n"
"v_mfma_f32_16x16x32_bf16 v[64:67], a[164:167], a[204:207], v[64:67]\n"
"buffer_load_dwordx4 v[60:63], v1, s[20:23], s0 offen sc1\n"
"v_mfma_f32_16x16x32_bf16 a[64:67], a[168:171], a[204:207], a[64:67]\n"
"v_mfma_f32_16x16x32_bf16 a[52:55], a[100:103], a[204:207], a[52:55]\n"
"v_mfma_f32_16x16x32_bf16 a[44:47], a[84:87], a[204:207], a[44:47]\n"
"buffer_store_dwordx4 v[52:55], v1, s[4:7], s54 offen sc1\n"
"s_waitcnt vmcnt(9)\n"
"v_mfma_f32_16x16x32_bf16 v[64:67], a[180:183], a[200:203], v[64:67]\n"
"v_mfma_f32_16x16x32_bf16 a[64:67], a[184:187], a[200:203], a[64:67]\n"
"v_mfma_f32_16x16x32_bf16 a[52:55], a[172:175], a[200:203], a[52:55]\n"
"v_mfma_f32_16x16x32_bf16 a[44:47], a[176:179], a[200:203], a[44:47]\n"
"buffer_store_dwordx4 v[52:55], v1, s[4:7], s1 offen sc1\n"
"s_waitcnt vmcnt(8)\n"
"v_mfma_f32_16x16x32_bf16 v[64:67], a[192:195], v[76:79], v[64:67]\n"
"v_mfma_f32_16x16x32_bf16 a[64:67], a[196:199], v[76:79], a[64:67]\n"
"buffer_load_dwordx4 a[136:139], v2, s[4:7], s43 offen offset:3072\n"
"v_mfma_f32_16x16x32_bf16 a[52:55], a[188:191], v[76:79], a[52:55]\n"
"v_mfma_f32_16x16x32_bf16 a[44:47], v[100:103], v[76:79], a[44:47]\n"
"s_waitcnt vmcnt(8)\n"
"v_mfma_f32_16x16x32_bf16 v[68:71], a[124:127], a[104:107], 0\n"
"v_mfma_f32_16x16x32_bf16 v[76:79], a[116:119], a[104:107], 0\n"
"buffer_load_dwordx4 a[132:135], v3, s[8:11], s44 offen offset:3072\n"
"v_mfma_f32_16x16x32_bf16 v[52:55], a[92:95], a[104:107], 0\n"
"ds_write_b128 v15, v[64:67]\n"
"v_mfma_f32_16x16x32_bf16 v[56:59], a[108:111], a[104:107], 0\n"
"ds_write_b128 v15, a[64:67] offset:1024\n"
"s_waitcnt vmcnt(8)\n"
"v_mfma_f32_16x16x32_bf16 v[68:71], a[164:167], a[60:63], v[68:71]\n"
"buffer_load_dwordx4 a[64:67], v3, s[4:7], s43 offen\n"
"v_mfma_f32_16x16x32_bf16 v[76:79], a[168:171], a[60:63], v[76:79]\n"
"ds_write_b128 v14, a[52:55]\n"
"v_mfma_f32_16x16x32_bf16 v[52:55], a[100:103], a[60:63], v[52:55]\n"
"ds_write_b128 v14, a[44:47] offset:1024\n"
"v_mfma_f32_16x16x32_bf16 v[56:59], a[84:87], a[60:63], v[56:59]\n"
"buffer_load_dwordx4 a[80:83], v3, s[4:7], s43 offen offset:1024\n"
"s_waitcnt vmcnt(8)\n"
"v_mfma_f32_16x16x32_bf16 v[68:71], a[180:183], v[80:83], v[68:71]\n"
"v_mfma_f32_16x16x32_bf16 v[76:79], a[184:187], v[80:83], v[76:79]\n"
"v_mfma_f32_16x16x32_bf16 v[52:55], a[172:175], v[80:83], v[52:55]\n"
"v_mfma_f32_16x16x32_bf16 v[56:59], a[176:179], v[80:83], v[56:59]\n"
"buffer_load_dwordx4 a[76:79], v4, s[8:11], s44 offen\n"
"s_waitcnt vmcnt(8)\n"
"v_mfma_f32_16x16x32_bf16 v[68:71], a[192:195], v[72:75], v[68:71]\n"
"v_mfma_f32_16x16x32_bf16 v[76:79], a[196:199], v[72:75], v[76:79]\n"
"v_mfma_f32_16x16x32_bf16 v[52:55], a[188:191], v[72:75], v[52:55]\n"
"v_mfma_f32_16x16x32_bf16 v[56:59], v[100:103], v[72:75], v[56:59]\n"
"buffer_load_dwordx4 a[44:47], v3, s[4:7], s43 offen offset:2048\n"
"s_waitcnt vmcnt(8)\n"
"v_add_f32 v42, v60, v62\n"
"v_mov_b32 v43, v42\n"
"v_add_f32 v0, v61, v63\n"
"s_nop 0\n"
"v_permlane16_swap_b32 v42, v43\n"
"v_add_f32 v40, v42, v43\n"
"v_mov_b32 v42, v40\n"
"v_cmp_u_f32 vcc, v64, v68\n"
"ds_write_b128 v13, v[68:71]\n"
"buffer_load_dwordx4 a[96:99], v3, s[4:7], s43 offen offset:3072\n"
"v_permlane32_swap_b32 v40, v42\n"
"v_add_f32 v64, v40, v42\n"
"v_mul_f32 v42, 0x3e000000, v64\n"
"v_sub_f32 v40, v60, v42\n"
"v_sub_f32 v64, v62, v42\n"
"v_mul_f32 v60, v40, v40\n"
"v_fmac_f32 v60, v64, v64\n"
"v_fmamk_f32 v40, v60, 0x42000000, v0\n"
"v_mov_b32 v0, v40\n"
"ds_write_b128 v13, v[76:79] offset:1024\n"
"buffer_load_dwordx4 a[88:91], v4, s[8:11], s44 offen offset:1024\n"
"v_permlane16_swap_b32 v40, v0\n"
"v_add_f32 v60, v40, v0\n"
"v_mov_b32 v0, v60\n"
"s_nop 1\n"
"v_permlane32_swap_b32 v60, v0\n"
"v_add_f32 v43, v60, v0\n"
"v_cmp_u_f32 s[0:1], v43, v43\n"
"ds_write_b64 v17, v[42:43]\n"
"s_or_b64 vcc, vcc, s[0:1]\n"
"buffer_load_dwordx4 a[112:115], v4, s[4:7], s43 offen\n"
"buffer_load_dwordx4 a[52:55], v4, s[4:7], s43 offen offset:1024\n"
"s_cbranch_vccnz Rw1A%=\n"
"Cw1A%=:\n"
"s_waitcnt lgkmcnt(0)\n"
"s_barrier\n"
"s_add_u32 s33, s50, 0xc00000\n"
"ds_read_b128 v[84:87], v19\n"
"s_mov_b32 s0, 0xc00000\n"
"s_add_u32 s32, s49, 0xc00000\n"
"s_add_u32 s3, s51, 0xd00000\n"
"ds_read_b128 v[68:71], v16\n"
"s_add_u32 s1, s52, 0x1000000\n"
"v_mov_b32 v60, -1\n"
"v_mov_b32 v61, -1\n"
"ds_read_b128 v[64:67], v16 offset:2048\n"
"v_mov_b32 v62, -1\n"
"buffer_load_dwordx4 v[168:171], v1, s[20:23], s33 offen sc1\n"
"ds_read_b128 v[88:91], v16 offset:4096\n"
"v_mov_b32 v63, -1\n"
"s_add_u32 s2, s53, 0x100000\n"
"s_mul_i32 m0, s68, 2\n"
"ds_read_b128 v[92:95], v16 offset:1024\n"
"s_add_u32 m0, m0, s69\n"
"ds_read_b128 v[72:75], v16 offset:3072\n"
"ds_read_b128 v[96:99], v16 offset:5120\n"
"buffer_load_dwordx4 v[172:175], v1, s[20:23], s33 offen offset:1024 sc1\n"
"ds_read_b128 v[76:79], v21\n"
"s_waitcnt lgkmcnt(7)\n"
"v_sub_f32 v40, v84, v86\n"
"v_mul_f32 v0, v40, v40\n"
"ds_read_b128 v[80:83], v21 offset:16\n"
"v_add_f32 v40, v85, v87\n"
"v_fmamk_f32 v42, v0, 0x43000000, v40\n"
"v_mul_f32 v40, 0x3b000000, v42\n"
"v_add_f32 v0, 0x3727c5ac, v40\n"
"v_rsq_f32 v42, v0\n"
"v_add_f32 v0, v84, v86\n"
"buffer_load_dwordx4 v[176:179], v1, s[20:23], s33 offen offset:2048 sc1\n"
"v_mul_f32 v84, v42, v42\n"
"v_fmaak_f32 v85, v84, v40, 0x3727c5ac\n"
"v_rsq_f32 v40, v85\n"
"s_waitcnt lgkmcnt(7)\n"
"v_pk_add_f32 v[52:53], v[52:53], v[68:69]\n"
"s_waitcnt lgkmcnt(5)\n"
"v_pk_add_f32 v[64:65], v[64:65], v[88:89]\n"
"v_pk_add_f32 v[54:55], v[54:55], v[70:71]\n"
"v_pk_add_f32 v[66:67], v[66:67], v[90:91]\n"
"s_waitcnt lgkmcnt(4)\n"
"v_pk_add_f32 v[56:57], v[56:57], v[92:93]\n"
"s_waitcnt lgkmcnt(2)\n"
"v_pk_add_f32 v[72:73], v[72:73], v[96:97]\n"
"v_pk_add_f32 v[58:59], v[58:59], v[94:95]\n"
"v_pk_add_f32 v[74:75], v[74:75], v[98:99]\n"
"buffer_load_dwordx4 a[204:207], v1, s[20:23], s33 offen offset:3072 sc1\n"
"v_mul_f32 v70, 0.5, v0\n"
"v_pk_add_f32 v[52:53], v[52:53], v[64:65]\n"
"v_pk_add_f32 v[54:55], v[54:55], v[66:67]\n"
"v_pk_add_f32 v[56:57], v[56:57], v[72:73]\n"
"v_pk_add_f32 v[58:59], v[58:59], v[74:75]\n"
"v_mul_f32 v0, v42, v40\n"
"v_mul_f32 v68, -1.0, v70\n"
"v_mul_f32 v40, v0, v0\n"
"v_mul_f32 v64, 0xbdd2d3e8, v0\n"
"s_waitcnt lgkmcnt(1)\n"
"v_pk_fma_f32 v[70:71], v[76:77], v[68:69], v[52:53] op_sel_hi:[1,0,1]\n"
"v_pk_fma_f32 v[72:73], v[78:79], v[68:69], v[54:55] op_sel_hi:[1,0,1]\n"
"s_waitcnt lgkmcnt(0)\n"
"v_pk_fma_f32 v[52:53], v[80:81], v[68:69], v[56:57] op_sel_hi:[1,0,1]\n"
"v_pk_fma_f32 v[54:55], v[82:83], v[68:69], v[58:59] op_sel_hi:[1,0,1]\n"
"buffer_load_dwordx4 a[208:211], v1, s[20:23], s32 offen sc1\n"
"v_mul_f32 v66, v40, v64\n"
"v_mul_f32 v76, 0xc0135761, v0\n"
"v_pk_mul_f32 v[64:65], v[70:71], v[70:71]\n"
"v_pk_mul_f32 v[56:57], v[72:73], v[72:73]\n"
"v_pk_mul_f32 v[58:59], v[52:53], v[52:53]\n"
"v_pk_mul_f32 v[74:75], v[54:55], v[54:55]\n"
"v_pk_fma_f32 v[64:65], v[64:65], v[66:67], v[76:77] op_sel_hi:[1,0,0]\n"
"v_pk_fma_f32 v[56:57], v[56:57], v[66:67], v[76:77] op_sel_hi:[1,0,0]\n"
"v_pk_fma_f32 v[58:59], v[58:59], v[66:67], v[76:77] op_sel_hi:[1,0,0]\n"
"v_pk_fma_f32 v[74:75], v[74:75], v[66:67], v[76:77] op_sel_hi:[1,0,0]\n"
"v_pk_mul_f32 v[64:65], v[64:65], v[70:71]\n"
"v_pk_mul_f32 v[56:57], v[56:57], v[72:73]\n"
"v_pk_mul_f32 v[58:59], v[58:59], v[52:53]\n"
"buffer_load_dwordx4 a[200:203], v1, s[20:23], s32 offen offset:1024 sc1\n"
"v_pk_mul_f32 v[74:75], v[74:75], v[54:55]\n"
"v_rcp_f32 v66, v0\n"
"v_exp_f32 v64, v64\n"
"v_exp_f32 v65, v65\n"
"v_exp_f32 v56, v56\n"
"v_exp_f32 v57, v57\n"
"v_exp_f32 v58, v58\n"
"buffer_load_dwordx4 v[76:79], v1, s[20:23], s32 offen offset:2048 sc1\n"
"v_exp_f32 v59, v59\n"
"v_exp_f32 v74, v74\n"
"v_exp_f32 v75, v75\n"
"v_pk_fma_f32 v[64:65], v[64:65], v[66:67], v[66:67] op_sel_hi:[1,0,0]\n"
"v_pk_fma_f32 v[56:57], v[56:57], v[66:67], v[66:67] op_sel_hi:[1,0,0]\n"
"v_pk_fma_f32 v[58:59], v[58:59], v[66:67], v[66:67] op_sel_hi:[1,0,0]\n"
"v_pk_fma_f32 v[74:75], v[74:75], v[66:67], v[66:67] op_sel_hi:[1,0,0]\n"
"v_rcp_f32 v64, v64\n"
"v_rcp_f32 v65, v65\n"
"buffer_load_dwordx4 v[84:87], v1, s[20:23], s32 offen offset:3072 sc1\n"
"v_rcp_f32 v56, v56\n"
"v_rcp_f32 v57, v57\n"
"v_rcp_f32 v58, v58\n"
"v_rcp_f32 v59, v59\n"
"v_rcp_f32 v74, v74\n"
"v_rcp_f32 v75, v75\n"
"v_pk_mul_f32 v[70:71], v[70:71], v[64:65]\n"
"buffer_load_dwordx4 v[64:67], v1, s[20:23], s3 offen sc1\n"
"v_pk_mul_f32 v[72:73], v[72:73], v[56:57]\n"
"v_pk_mul_f32 v[52:53], v[52:53], v[58:59]\n"
"v_pk_mul_f32 v[54:55], v[54:55], v[74:75]\n"
"v_cvt_pk_bf16_f32 v56, v70, v71\n"
"v_cvt_pk_bf16_f32 v57, v72, v73\n"
"v_cvt_pk_bf16_f32 v58, v52, v53\n"
"v_cvt_pk_bf16_f32 v59, v54, v55\n"
"v_mul_f32 v54, v68, v42\n"
"buffer_store_dwordx4 v[56:59], v39, s[24:27], s0 offen sc1\n"
"buffer_store_dwordx4 v[56:59], v1, s[24:27], s1 offen sc1\n"
"s_waitcnt vmcnt(10)\n"
"v_mfma_f32_16x16x32_bf16 v[72:75], a[124:127], v[168:171], 0\n"
"buffer_store_dwordx4 v[60:63], v1, s[20:23], s52 offen sc1\n"
"v_mfma_f32_16x16x32_bf16 v[88:91], a[116:119], v[168:171], 0\n"
"v_mfma_f32_16x16x32_bf16 v[92:95], a[92:95], v[168:171], 0\n"
"v_mfma_f32_16x16x32_bf16 v[96:99], a[108:111], v[168:171], 0\n"
"s_waitcnt vmcnt(10)\n"
"v_mfma_f32_16x16x32_bf16 v[72:75], a[164:167], v[172:175], v[72:75]\n"
"buffer_store_dwordx2 v[60:61], v5, s[20:23], s2 offen sc1\n"
"v_mfma_f32_16x16x32_bf16 v[88:91], a[168:171], v[172:175], v[88:91]\n"
"v_mfma_f32_16x16x32_bf16 v[92:95], a[100:103], v[172:175], v[92:95]\n"
"v_mfma_f32_16x16x32_bf16 v[96:99], a[84:87], v[172:175], v[96:99]\n"
"s_waitcnt vmcnt(10)\n"
"v_mfma_f32_16x16x32_bf16 v[72:75], a[180:183], v[176:179], v[72:75]\n"
"buffer_load_dwordx4 a[104:107], v4, s[8:11], s44 offen offset:2048\n"
"v_mfma_f32_16x16x32_bf16 v[88:91], a[184:187], v[176:179], v[88:91]\n"
"s_waitcnt vmcnt(9)\n"
"v_mfma_f32_16x16x32_bf16 v[68:71], a[124:127], a[208:211], 0\n"
"v_mfma_f32_16x16x32_bf16 v[80:83], a[116:119], a[208:211], 0\n"
"v_mfma_f32_16x16x32_bf16 v[92:95], a[172:175], v[176:179], v[92:95]\n"
"buffer_load_dwordx4 a[60:63], v4, s[4:7], s43 offen offset:2048\n"
"v_mfma_f32_16x16x32_bf16 v[96:99], a[176:179], v[176:179], v[96:99]\n"
"v_mfma_f32_16x16x32_bf16 v[56:59], a[92:95], a[208:211], 0\n"
"s_waitcnt vmcnt(9)\n"
"v_mfma_f32_16x16x32_bf16 v[68:71], a[164:167], a[200:203], v[68:71]\n"
"v_mfma_f32_16x16x32_bf16 v[80:83], a[168:171], a[200:203], v[80:83]\n"
"buffer_load_dwordx4 a[68:71], v4, s[4:7], s43 offen offset:3072\n"
"v_mfma_f32_16x16x32_bf16 v[60:63], a[108:111], a[208:211], 0\n"
"v_mfma_f32_16x16x32_bf16 v[72:75], a[192:195], a[204:207], v[72:75]\n"
"s_waitcnt vmcnt(9)\n"
"v_mfma_f32_16x16x32_bf16 v[68:71], a[180:183], v[76:79], v[68:71]\n"
"v_mfma_f32_16x16x32_bf16 v[80:83], a[184:187], v[76:79], v[80:83]\n"
"buffer_load_dwordx4 a[120:123], v4, s[8:11], s44 offen offset:3072\n"
"v_mfma_f32_16x16x32_bf16 v[88:91], a[196:199], a[204:207], v[88:91]\n"
"v_mfma_f32_16x16x32_bf16 v[92:95], a[188:191], a[204:207], v[92:95]\n"
"v_mfma_f32_16x16x32_bf16 v[96:99], v[100:103], a[204:207], v[96:99]\n"
"s_waitcnt vmcnt(9)\n"
"v_mfma_f32_16x16x32_bf16 v[68:71], a[192:195], v[84:87], v[68:71]\n"
"buffer_load_dwordx4 v3, s[16:19], s45 offen offset:2048 lds\n"
"v_mfma_f32_16x16x32_bf16 v[80:83], a[196:199], v[84:87], v[80:83]\n"
"s_mul_i32 m0, s68, 2\n"
"s_add_u32 m0, m0, s69\n"
"v_mfma_f32_16x16x32_bf16 v[56:59], a[100:103], a[200:203], v[56:59]\n"
"ds_write_b128 v15, v[72:75] offset:32768\n"
"v_mfma_f32_16x16x32_bf16 v[60:63], a[84:87], a[200:203], v[60:63]\n"
"s_waitcnt vmcnt(9)\n"
"v_add_f32 v40, v64, v66\n"
"v_mov_b32 v52, v40\n"
"buffer_load_dwordx4 v3, s[16:19], s45 offen offset:3072 lds\n"
"v_mfma_f32_16x16x32_bf16 v[56:59], a[172:175], v[76:79], v[56:59]\n"
"v_permlane16_swap_b32 v40, v52\n"
"v_add_f32 v0, v40, v52\n"
"v_mfma_f32_16x16x32_bf16 v[60:63], a[176:179], v[76:79], v[60:63]\n"
"v_mov_b32 v40, v0\n"
"s_mul_i32 m0, s68, 3\n"
"v_mfma_f32_16x16x32_bf16 v[56:59], a[188:191], v[84:87], v[56:59]\n"
"v_permlane32_swap_b32 v0, v40\n"
"v_add_f32 v76, v0, v40\n"
"v_mfma_f32_16x16x32_bf16 v[60:63], v[100:103], v[84:87], v[60:63]\n"
"v_mul_f32 v52, 0x3e000000, v76\n"
"v_sub_f32 v76, v64, v52\n"
"v_sub_f32 v40, v66, v52\n"
"v_mul_f32 v0, v76, v76\n"
"s_add_u32 m0, m0, s69\n"
"v_fmac_f32 v0, v40, v40\n"
"buffer_load_dwordx4 v4, s[16:19], s45 offen offset:0 lds\n"
"v_add_f32 v76, v65, v67\n"
"v_fmamk_f32 v40, v0, 0x42000000, v76\n"
"s_mul_i32 m0, s68, 3\n"
"v_mov_b32 v64, v40\n"
"s_add_u32 m0, m0, s69\n"
"v_cmp_u_f32 vcc, v72, v68\n"
"v_permlane16_swap_b32 v40, v64\n"
"v_add_f32 v0, v40, v64\n"
"v_mov_b32 v40, v0\n"
"ds_write_b128 v15, v[88:91] offset:33792\n"
"buffer_load_dwordx4 v4, s[16:19], s45 offen offset:1024 lds\n"
"s_mul_i32 m0, s68, 3\n"
"s_add_u32 m0, m0, s69\n"
"v_permlane32_swap_b32 v0, v40\n"
"v_add_f32 v53, v0, v40\n"
"v_cmp_u_f32 s[0:1], v53, v53\n"
"ds_write_b64 v17, v[52:53] offset:512\n"
"ds_write_b128 v14, v[92:95] offset:32768\n"
"buffer_load_dwordx4 v4, s[16:19], s45 offen offset:2048 lds\n"
"s_mul_i32 m0, s68, 3\n"
"s_add_u32 m0, m0, s69\n"
"ds_write_b128 v14, v[96:99] offset:33792\n"
"s_or_b64 vcc, vcc, s[0:1]\n"
"ds_write_b128 v13, v[68:71] offset:32768\n"
"buffer_load_dwordx4 v4, s[16:19], s45 offen offset:3072 lds\n"
"ds_write_b128 v13, v[80:83] offset:33792\n"
"s_cbranch_vccnz Rw1B%=\n"
"Cw1B%=:\n"
"s_waitcnt lgkmcnt(0)\n"
"s_barrier\n"
"s_add_u32 s33, s50, 0xfb8000\n"
"ds_read_b128 v[80:83], v19 offset:512\n"
"s_mov_b32 s1, 0x1000000\n"
"s_add_u32 s0, s49, 0xfb8000\n"
"s_add_u32 s2, s52, 0x1400000\n"
"ds_read_b128 v[84:87], v16 offset:32768\n"
"v_mov_b32 v76, -1\n"
"buffer_load_dwordx4 a[188:191], v38, s[24:27], s33 offen sc1\n"
"ds_read_b128 v[72:75], v16 offset:34816\n"
"v_mov_b32 v77, -1\n"
"v_mov_b32 v78, -1\n"
"v_mov_b32 v79, -1\n"
"ds_read_b128 v[100:103], v16 offset:36864\n"
"s_add_u32 s32, s52, 0x1000000\n"
"s_add_u32 s3, s53, 0x1100000\n"
"v_lshlrev_b32 v64, 16, v48\n"
"ds_read_b128 v[104:107], v16 offset:33792\n"
"v_and_b32 v65, s72, v48\n"
"v_lshlrev_b32 v66, 16, v49\n"
"v_and_b32 v67, s72, v49\n"
"ds_read_b128 v[88:91], v16 offset:35840\n"
"buffer_load_dwordx4 a[184:187], v38, s[24:27], s33 offen offset:1024 sc1\n"
"ds_read_b128 v[108:111], v16 offset:37888\n"
"v_lshlrev_b32 v68, 16, v50\n"
"s_waitcnt lgkmcnt(6)\n"
"v_sub_f32 v40, v80, v82\n"
"v_mul_f32 v0, v40, v40\n"
"ds_read_b128 v[92:95], v21\n"
"v_add_f32 v52, v81, v83\n"
"v_fmamk_f32 v40, v0, 0x43000000, v52\n"
"ds_read_b128 v[96:99], v21 offset:16\n"
"v_mul_f32 v0, 0x3b000000, v40\n"
"v_add_f32 v40, 0x3727c5ac, v0\n"
"v_rsq_f32 v52, v40\n"
"buffer_load_dwordx4 a[180:183], v38, s[24:27], s33 offen offset:2048 sc1\n"
"v_mul_f32 v40, v52, v52\n"
"v_fmaak_f32 v112, v40, v0, 0x3727c5ac\n"
"v_rsq_f32 v40, v112\n"
"v_add_f32 v0, v80, v82\n"
"s_waitcnt lgkmcnt(7)\n"
"v_pk_add_f32 v[56:57], v[56:57], v[84:85]\n"
"s_waitcnt lgkmcnt(5)\n"
"v_pk_add_f32 v[72:73], v[72:73], v[100:101]\n"
"v_pk_add_f32 v[58:59], v[58:59], v[86:87]\n"
"v_pk_add_f32 v[74:75], v[74:75], v[102:103]\n"
"s_waitcnt lgkmcnt(4)\n"
"v_pk_add_f32 v[60:61], v[60:61], v[104:105]\n"
"s_waitcnt lgkmcnt(2)\n"
"v_pk_add_f32 v[88:89], v[88:89], v[108:109]\n"
"v_pk_add_f32 v[62:63], v[62:63], v[106:107]\n"
"buffer_load_dwordx4 v[104:107], v38, s[24:27], s33 offen offset:3072 sc1\n"
"v_pk_add_f32 v[90:91], v[90:91], v[110:111]\n"
"v_mul_f32 v80, 0.5, v0\n"
"v_pk_add_f32 v[56:57], v[56:57], v[72:73]\n"
"v_pk_add_f32 v[58:59], v[58:59], v[74:75]\n"
"v_pk_add_f32 v[60:61], v[60:61], v[88:89]\n"
"v_pk_add_f32 v[62:63], v[62:63], v[90:91]\n"
"v_mul_f32 v0, v52, v40\n"
"v_mul_f32 v72, -1.0, v80\n"
"v_mul_f32 v40, v0, v0\n"
"v_mul_f32 v82, 0xbdd2d3e8, v0\n"
"s_waitcnt lgkmcnt(1)\n"
"v_pk_fma_f32 v[74:75], v[92:93], v[72:73], v[56:57] op_sel_hi:[1,0,1]\n"
"v_pk_fma_f32 v[80:81], v[94:95], v[72:73], v[58:59] op_sel_hi:[1,0,1]\n"
"s_waitcnt lgkmcnt(0)\n"
"v_pk_fma_f32 v[56:57], v[96:97], v[72:73], v[60:61] op_sel_hi:[1,0,1]\n"
"buffer_store_dwordx4 v[76:79], v1, s[16:19], s32 offen sc1\n"
"v_pk_fma_f32 v[58:59], v[98:99], v[72:73], v[62:63] op_sel_hi:[1,0,1]\n"
"v_mul_f32 v86, v40, v82\n"
"v_mul_f32 v88, 0xc0135761, v0\n"
"v_pk_mul_f32 v[84:85], v[74:75], v[74:75]\n"
"v_pk_mul_f32 v[60:61], v[80:81], v[80:81]\n"
"v_pk_mul_f32 v[62:63], v[56:57], v[56:57]\n"
"v_pk_mul_f32 v[82:83], v[58:59], v[58:59]\n"
"v_pk_fma_f32 v[84:85], v[84:85], v[86:87], v[88:89] op_sel_hi:[1,0,0]\n"
"v_pk_fma_f32 v[60:61], v[60:61], v[86:87], v[88:89] op_sel_hi:[1,0,0]\n"
"v_pk_fma_f32 v[62:63], v[62:63], v[86:87], v[88:89] op_sel_hi:[1,0,0]\n"
"v_pk_fma_f32 v[82:83], v[82:83], v[86:87], v[88:89] op_sel_hi:[1,0,0]\n"
"v_pk_mul_f32 v[84:85], v[84:85], v[74:75]\n"
"v_pk_mul_f32 v[60:61], v[60:61], v[80:81]\n"
"buffer_store_dwordx2 v[76:77], v5, s[16:19], s3 offen sc1\n"
"v_pk_mul_f32 v[62:63], v[62:63], v[56:57]\n"
"v_pk_mul_f32 v[82:83], v[82:83], v[58:59]\n"
"v_rcp_f32 v76, v0\n"
"v_exp_f32 v84, v84\n"
"v_exp_f32 v85, v85\n"
"v_exp_f32 v60, v60\n"
"v_exp_f32 v61, v61\n"
"v_exp_f32 v62, v62\n"
"buffer_load_dwordx4 a[124:127], v1, s[4:7], s46 offen offset:3072\n"
"v_exp_f32 v63, v63\n"
"v_exp_f32 v82, v82\n"
"v_exp_f32 v83, v83\n"
"v_pk_fma_f32 v[84:85], v[84:85], v[76:77], v[76:77] op_sel_hi:[1,0,0]\n"
"v_pk_fma_f32 v[60:61], v[60:61], v[76:77], v[76:77] op_sel_hi:[1,0,0]\n"
"v_pk_fma_f32 v[62:63], v[62:63], v[76:77], v[76:77] op_sel_hi:[1,0,0]\n"
"v_pk_fma_f32 v[82:83], v[82:83], v[76:77], v[76:77] op_sel_hi:[1,0,0]\n"
"v_rcp_f32 v84, v84\n"
"v_rcp_f32 v85, v85\n"
"buffer_load_dwordx4 v[88:91], v8, s[24:27], s0 offen sc1\n"
"v_rcp_f32 v60, v60\n"
"v_rcp_f32 v61, v61\n"
"v_rcp_f32 v62, v62\n"
"v_rcp_f32 v63, v63\n"
"v_rcp_f32 v82, v82\n"
"v_rcp_f32 v83, v83\n"
"v_pk_mul_f32 v[74:75], v[74:75], v[84:85]\n"
"buffer_load_dwordx4 v[76:79], v8, s[24:27], s0 offen offset:1024 sc1\n"
"v_pk_mul_f32 v[80:81], v[80:81], v[60:61]\n"
"v_pk_mul_f32 v[56:57], v[56:57], v[62:63]\n"
"v_pk_mul_f32 v[58:59], v[58:59], v[82:83]\n"
"v_cvt_pk_bf16_f32 v60, v74, v75\n"
"v_cvt_pk_bf16_f32 v61, v80, v81\n"
"v_cvt_pk_bf16_f32 v62, v56, v57\n"
"v_cvt_pk_bf16_f32 v63, v58, v59\n"
"v_and_b32 v69, s72, v50\n"
"v_lshlrev_b32 v70, 16, v51\n"
"v_and_b32 v71, s72, v51\n"
"v_fma_f32 v64, v64, v42, v54\n"
"v_fma_f32 v65, v65, v42, v54\n"
"v_fma_f32 v66, v66, v42, v54\n"
"buffer_store_dwordx4 v[60:63], v39, s[24:27], s1 offen sc1\n"
"buffer_store_dwordx4 v[60:63], v1, s[24:27], s2 offen sc1\n"
"s_waitcnt vmcnt(10)\n"
"v_mfma_f32_16x16x32_bf16 v[56:59], a[212:215], a[188:191], 0\n"
"v_fma_f32 v67, v67, v42, v54\n"
"v_fma_f32 v68, v68, v42, v54\n"
"buffer_load_dwordx4 v[84:87], v8, s[24:27], s0 offen offset:2048 sc1\n"
"v_mfma_f32_16x16x32_bf16 v[108:111], a[216:219], a[188:191], 0\n"
"v_fma_f32 v69, v69, v42, v54\n"
"v_fma_f32 v70, v70, v42, v54\n"
"v_mfma_f32_16x16x32_bf16 v[92:95], a[144:147], a[188:191], 0\n"
"v_fma_f32 v71, v71, v42, v54\n"
"v_mul_f32 v60, v72, v52\n"
"v_mfma_f32_16x16x32_bf16 v[96:99], a[140:143], a[188:191], 0\n"
"s_waitcnt vmcnt(10)\n"
"v_mfma_f32_16x16x32_bf16 v[56:59], a[228:231], a[184:187], v[56:59]\n"
"buffer_load_dwordx4 v[80:83], v8, s[24:27], s0 offen offset:3072 sc1\n"
"v_mfma_f32_16x16x32_bf16 v[108:111], a[232:235], a[184:187], v[108:111]\n"
"v_mfma_f32_16x16x32_bf16 v[92:95], a[220:223], a[184:187], v[92:95]\n"
"v_mfma_f32_16x16x32_bf16 v[96:99], a[224:227], a[184:187], v[96:99]\n"
"s_waitcnt vmcnt(10)\n"
"v_mfma_f32_16x16x32_bf16 v[56:59], v[200:203], a[180:183], v[56:59]\n"
"buffer_load_dwordx4 a[116:119], v2, s[4:7], s46 offen\n"
"v_mfma_f32_16x16x32_bf16 v[108:111], v[196:199], a[180:183], v[108:111]\n"
"v_mfma_f32_16x16x32_bf16 v[92:95], a[236:239], a[180:183], v[92:95]\n"
"v_mfma_f32_16x16x32_bf16 v[96:99], v[204:207], a[180:183], v[96:99]\n"
"s_waitcnt vmcnt(10)\n"
"v_mfma_f32_16x16x32_bf16 v[56:59], v[184:187], v[104:107], v[56:59]\n"
"buffer_load_dwordx4 a[108:111], v2, s[4:7], s46 offen offset:1024\n"
"v_mfma_f32_16x16x32_bf16 v[108:111], v[180:183], v[104:107], v[108:111]\n"
"v_mfma_f32_16x16x32_bf16 v[92:95], v[192:195], v[104:107], v[92:95]\n"
"v_mfma_f32_16x16x32_bf16 v[96:99], v[188:191], v[104:107], v[96:99]\n"
"buffer_load_dwordx4 a[100:103], v2, s[4:7], s46 offen offset:2048\n"
"s_nop 2\n"
"ds_write_b128 v15, v[56:59]\n"
"s_waitcnt vmcnt(8)\n"
"v_mfma_f32_16x16x32_bf16 v[72:75], a[212:215], v[88:91], 0\n"
"buffer_load_dwordx4 a[92:95], v2, s[4:7], s46 offen offset:3072\n"
"v_mfma_f32_16x16x32_bf16 a[180:183], a[216:219], v[88:91], 0\n"
"ds_write_b128 v15, v[108:111] offset:1024\n"
"v_mfma_f32_16x16x32_bf16 v[64:67], a[144:147], v[88:91], v[64:67]\n"
"ds_write_b128 v14, v[92:95]\n"
"s_waitcnt vmcnt(8)\n"
"v_mfma_f32_16x16x32_bf16 v[72:75], a[228:231], v[76:79], v[72:75]\n"
"buffer_load_dwordx4 a[84:87], v3, s[4:7], s46 offen\n"
"v_mfma_f32_16x16x32_bf16 a[180:183], a[232:235], v[76:79], a[180:183]\n"
"ds_write_b128 v14, v[96:99] offset:1024\n"
"v_mfma_f32_16x16x32_bf16 v[68:71], a[140:143], v[88:91], v[68:71]\n"
"v_mfma_f32_16x16x32_bf16 v[64:67], a[220:223], v[76:79], v[64:67]\n"
"v_mfma_f32_16x16x32_bf16 v[68:71], a[224:227], v[76:79], v[68:71]\n"
"buffer_load_dwordx4 a[164:167], v3, s[4:7], s46 offen offset:1024\n"
"s_waitcnt vmcnt(7)\n"
"v_mfma_f32_16x16x32_bf16 v[72:75], v[200:203], v[84:87], v[72:75]\n"
"v_mfma_f32_16x16x32_bf16 a[180:183], v[196:199], v[84:87], a[180:183]\n"
"buffer_load_dwordx4 a[168:171], v3, s[4:7], s46 offen offset:2048\n"
"v_mfma_f32_16x16x32_bf16 v[64:67], a[236:239], v[84:87], v[64:67]\n"
"v_mfma_f32_16x16x32_bf16 v[68:71], v[204:207], v[84:87], v[68:71]\n"
"s_waitcnt vmcnt(7)\n"
"v_mfma_f32_16x16x32_bf16 v[72:75], v[184:187], v[80:83], v[72:75]\n"
"v_mfma_f32_16x16x32_bf16 a[180:183], v[180:183], v[80:83], a[180:183]\n"
"buffer_load_dwordx4 a[172:175], v3, s[4:7], s46 offen offset:3072\n"
"v_mfma_f32_16x16x32_bf16 v[64:67], v[192:195], v[80:83], v[64:67]\n"
"v_mfma_f32_16x16x32_bf16 v[68:71], v[188:191], v[80:83], v[68:71]\n"
"s_nop 3\n"
"ds_write_b128 v13, v[72:75]\n"
"buffer_load_dwordx4 a[176:179], v4, s[4:7], s46 offen\n"
"v_cmp_u_f32 vcc, v56, v72\n"
"ds_write_b128 v13, a[180:183] offset:1024\n"
"s_cbranch_vccnz Rw2A%=\n"
"Cw2A%=:\n"
"s_waitcnt lgkmcnt(0)\n"
"s_barrier\n"
"s_add_u32 s33, s50, 0x13b8000\n"
"ds_read_b128 v[96:99], v16\n"
"buffer_load_dwordx4 v[176:179], v38, s[24:27], s33 offen sc1\n"
"ds_read_b128 v[56:59], v16 offset:2048\n"
"s_add_u32 s0, s49, 0x13b8000\n"
"s_add_u32 s3, s52, 0x1800000\n"
"s_add_u32 s1, s53, 0x1900000\n"
"ds_read_b128 v[92:95], v16 offset:4096\n"
"v_mov_b32 v80, -1\n"
"v_mov_b32 v81, -1\n"
"v_mov_b32 v82, -1\n"
"ds_read_b128 v[100:103], v16 offset:1024\n"
"v_mov_b32 v83, -1\n"
"s_add_u32 s32, s52, 0x1400000\n"
"s_add_u32 s2, s53, 0x1500000\n"
"ds_read_b128 v[84:87], v16 offset:3072\n"
"buffer_load_dwordx4 v[112:115], v38, s[24:27], s33 offen offset:1024 sc1\n"
"ds_read_b128 v[88:91], v16 offset:5120\n"
"v_lshlrev_b32 v72, 16, v44\n"
"v_and_b32 v73, s72, v44\n"
"v_lshlrev_b32 v74, 16, v45\n"
"v_and_b32 v75, s72, v45\n"
"v_lshlrev_b32 v76, 16, v46\n"
"v_and_b32 v77, s72, v46\n"
"s_waitcnt lgkmcnt(5)\n"
"v_pk_add_f32 v[64:65], v[64:65], v[96:97]\n"
"v_pk_add_f32 v[66:67], v[66:67], v[98:99]\n"
"v_lshlrev_b32 v78, 16, v47\n"
"v_and_b32 v79, s72, v47\n"
"v_fma_f32 v72, v72, v52, v60\n"
"v_fma_f32 v73, v73, v52, v60\n"
"buffer_load_dwordx4 v[104:107], v38, s[24:27], s33 offen offset:2048 sc1\n"
"s_waitcnt lgkmcnt(3)\n"
"v_pk_add_f32 v[56:57], v[56:57], v[92:93]\n"
"v_pk_add_f32 v[58:59], v[58:59], v[94:95]\n"
"v_pk_add_f32 v[64:65], v[64:65], v[56:57]\n"
"s_waitcnt lgkmcnt(2)\n"
"v_pk_add_f32 v[68:69], v[68:69], v[100:101]\n"
"v_pk_add_f32 v[70:71], v[70:71], v[102:103]\n"
"v_pk_add_f32 v[66:67], v[66:67], v[58:59]\n"
"v_cvt_pk_bf16_f32 v56, v64, v65\n"
"v_cvt_pk_bf16_f32 v57, v66, v67\n"
"v_pk_add_f32 v[42:43], v[64:65], v[66:67]\n"
"v_fma_f32 v74, v74, v52, v60\n"
"v_fma_f32 v75, v75, v52, v60\n"
"s_waitcnt lgkmcnt(0)\n"
"v_pk_add_f32 v[84:85], v[84:85], v[88:89]\n"
"buffer_load_dwordx4 v[108:111], v38, s[24:27], s33 offen offset:3072 sc1\n"
"v_pk_add_f32 v[86:87], v[86:87], v[90:91]\n"
"v_pk_add_f32 v[68:69], v[68:69], v[84:85]\n"
"v_pk_add_f32 v[70:71], v[70:71], v[86:87]\n"
"v_cvt_pk_bf16_f32 v58, v68, v69\n"
"v_cvt_pk_bf16_f32 v59, v70, v71\n"
"v_pk_add_f32 v[54:55], v[68:69], v[70:71]\n"
"v_pk_add_f32 v[42:43], v[42:43], v[54:55]\n"
"v_add_f32 v0, v42, v43\n"
"v_mov_b32 v40, v0\n"
"v_fma_f32 v76, v76, v52, v60\n"
"v_fma_f32 v77, v77, v52, v60\n"
"v_permlane16_swap_b32 v0, v40\n"
"buffer_store_dwordx4 v[56:59], v1, s[24:27], s3 offen sc1\n"
"v_add_f32 v42, v0, v40\n"
"v_mov_b32 v0, v42\n"
"v_fma_f32 v78, v78, v52, v60\n"
"buffer_store_dwordx4 v[80:83], v1, s[16:19], s32 offen sc1\n"
"v_permlane32_swap_b32 v42, v0\n"
"v_add_f32 v54, v42, v0\n"
"v_mul_f32 v42, 0x3d000000, v54\n"
"v_mul_f32 v54, 0xbd000000, v54\n"
"v_pk_add_f32 v[62:63], v[64:65], v[54:55] op_sel_hi:[1,0]\n"
"v_pk_add_f32 v[86:87], v[68:69], v[54:55] op_sel_hi:[1,0]\n"
"v_pk_add_f32 v[84:85], v[66:67], v[54:55] op_sel_hi:[1,0]\n"
"v_pk_add_f32 v[64:65], v[70:71], v[54:55] op_sel_hi:[1,0]\n"
"v_pk_mul_f32 v[54:55], v[62:63], v[62:63]\n"
"v_pk_mul_f32 v[62:63], v[86:87], v[86:87]\n"
"v_pk_fma_f32 v[54:55], v[84:85], v[84:85], v[54:55]\n"
"v_pk_fma_f32 v[62:63], v[64:65], v[64:65], v[62:63]\n"
"buffer_store_dwordx2 v[80:81], v5, s[16:19], s2 offen sc1\n"
"v_pk_add_f32 v[54:55], v[54:55], v[62:63]\n"
"v_add_f32 v0, v54, v55\n"
"v_mov_b32 v40, v0\n"
"v_fma_f32 v79, v79, v52, v60\n"
"s_nop 0\n"
"v_permlane16_swap_b32 v0, v40\n"
"v_add_f32 v54, v0, v40\n"
"v_mov_b32 v0, v54\n"
"s_nop 1\n"
"v_permlane32_swap_b32 v54, v0\n"
"v_add_f32 v43, v54, v0\n"
"buffer_store_dwordx2 v[42:43], v5, s[24:27], s1 offen sc1\n"
"buffer_load_dwordx4 a[180:183], v4, s[4:7], s46 offen offset:1024\n"
"s_waitcnt vmcnt(8)\n"
"v_mfma_f32_16x16x32_bf16 v[64:67], a[212:215], v[176:179], 0\n"
"v_mfma_f32_16x16x32_bf16 v[96:99], a[216:219], v[176:179], 0\n"
"buffer_load_dwordx4 v[92:95], v8, s[24:27], s0 offen sc1\n"
"v_mfma_f32_16x16x32_bf16 v[68:71], a[144:147], v[176:179], 0\n"
"v_mfma_f32_16x16x32_bf16 v[80:83], a[140:143], v[176:179], 0\n"
"s_waitcnt vmcnt(8)\n"
"v_mfma_f32_16x16x32_bf16 v[64:67], a[228:231], v[112:115], v[64:67]\n"
"buffer_load_dwordx4 v[100:103], v8, s[24:27], s0 offen offset:1024 sc1\n"
"v_mfma_f32_16x16x32_bf16 v[96:99], a[232:235], v[112:115], v[96:99]\n"
"v_mfma_f32_16x16x32_bf16 v[68:71], a[220:223], v[112:115], v[68:71]\n"
"v_mfma_f32_16x16x32_bf16 v[80:83], a[224:227], v[112:115], v[80:83]\n"
"s_waitcnt vmcnt(8)\n"
"v_mfma_f32_16x16x32_bf16 v[64:67], v[200:203], v[104:107], v[64:67]\n"
"buffer_load_dwordx4 v[88:91], v8, s[24:27], s0 offen offset:2048 sc1\n"
"v_mfma_f32_16x16x32_bf16 v[96:99], v[196:199], v[104:107], v[96:99]\n"
"v_mfma_f32_16x16x32_bf16 v[68:71], a[236:239], v[104:107], v[68:71]\n"
"v_mfma_f32_16x16x32_bf16 v[80:83], v[204:207], v[104:107], v[80:83]\n"
"buffer_load_dwordx4 v[84:87], v8, s[24:27], s0 offen offset:3072 sc1\n"
"s_waitcnt vmcnt(9)\n"
"v_mfma_f32_16x16x32_bf16 v[64:67], v[184:187], v[108:111], v[64:67]\n"
"v_mfma_f32_16x16x32_bf16 v[96:99], v[180:183], v[108:111], v[96:99]\n"
"v_mfma_f32_16x16x32_bf16 v[68:71], v[192:195], v[108:111], v[68:71]\n"
"v_mfma_f32_16x16x32_bf16 v[80:83], v[188:191], v[108:111], v[80:83]\n"
"buffer_load_dwordx4 a[184:187], v4, s[4:7], s46 offen offset:2048\n"
"s_nop 3\n"
"ds_write_b128 v15, v[64:67] offset:32768\n"
"ds_write_b128 v15, v[96:99] offset:33792\n"
"buffer_load_dwordx4 a[188:191], v4, s[4:7], s46 offen offset:3072\n"
"ds_write_b128 v14, v[68:71] offset:32768\n"
"ds_write_b128 v14, v[80:83] offset:33792\n"
"buffer_load_dwordx4 a[192:195], v1, s[4:7], s47 offen\n"
"buffer_load_dwordx4 a[196:199], v1, s[4:7], s47 offen offset:1024\n"
"s_waitcnt vmcnt(7)\n"
"v_mfma_f32_16x16x32_bf16 v[68:71], a[212:215], v[92:95], 0\n"
"v_mfma_f32_16x16x32_bf16 v[80:83], a[216:219], v[92:95], 0\n"
"v_mfma_f32_16x16x32_bf16 v[72:75], a[144:147], v[92:95], v[72:75]\n"
"buffer_load_dwordx4 a[200:203], v1, s[4:7], s47 offen offset:2048\n"
"v_mfma_f32_16x16x32_bf16 v[76:79], a[140:143], v[92:95], v[76:79]\n"
"s_waitcnt vmcnt(7)\n"
"v_mfma_f32_16x16x32_bf16 v[68:71], a[228:231], v[100:103], v[68:71]\n"
"v_mfma_f32_16x16x32_bf16 v[80:83], a[232:235], v[100:103], v[80:83]\n"
"v_mfma_f32_16x16x32_bf16 v[72:75], a[220:223], v[100:103], v[72:75]\n"
"buffer_load_dwordx4 a[204:207], v1, s[4:7], s47 offen offset:3072\n"
"v_mfma_f32_16x16x32_bf16 v[76:79], a[224:227], v[100:103], v[76:79]\n"
"s_waitcnt vmcnt(7)\n"
"v_mfma_f32_16x16x32_bf16 v[68:71], v[200:203], v[88:91], v[68:71]\n"
"v_mfma_f32_16x16x32_bf16 v[80:83], v[196:199], v[88:91], v[80:83]\n"
"buffer_load_dwordx4 a[208:211], v2, s[4:7], s47 offen\n"
"v_mfma_f32_16x16x32_bf16 v[72:75], a[236:239], v[88:91], v[72:75]\n"
"v_mfma_f32_16x16x32_bf16 v[76:79], v[204:207], v[88:91], v[76:79]\n"
"s_waitcnt vmcnt(7)\n"
"v_mfma_f32_16x16x32_bf16 v[68:71], v[184:187], v[84:87], v[68:71]\n"
"v_mfma_f32_16x16x32_bf16 v[80:83], v[180:183], v[84:87], v[80:83]\n"
"v_mfma_f32_16x16x32_bf16 v[72:75], v[192:195], v[84:87], v[72:75]\n"
"v_mfma_f32_16x16x32_bf16 v[76:79], v[188:191], v[84:87], v[76:79]\n"
"s_nop 4\n"
"ds_write_b128 v13, v[68:71] offset:32768\n"
"v_cmp_u_f32 vcc, v64, v68\n"
"ds_write_b128 v13, v[80:83] offset:33792\n"
"s_cbranch_vccnz Rw2B%=\n"
"Cw2B%=:\n"
"s_waitcnt lgkmcnt(0)\n"
"s_barrier\n"
"s_add_u32 s32, s48, 0x1800000\n"
"buffer_load_dwordx4 v[188:191], v1, s[24:27], s32 offen sc1\n"
"ds_read_b128 v[52:55], v16 offset:32768\n"
"s_add_u32 s2, s48, 0x1808000\n"
"s_add_u32 s0, s51, 0x1900000\n"
"s_add_u32 s3, s53, 0x100000\n"
"ds_read_b128 v[60:63], v16 offset:34816\n"
"v_mov_b32 v64, -1\n"
"v_mov_b32 v65, -1\n"
"v_mov_b32 v66, -1\n"
"ds_read_b128 v[84:87], v16 offset:36864\n"
"v_mov_b32 v67, -1\n"
"s_add_u32 s1, s52, 0x1800000\n"
"s_mul_i32 m0, s68, 4\n"
"ds_read_b128 v[88:91], v16 offset:33792\n"
"buffer_load_dwordx4 v[192:195], v1, s[24:27], s32 offen offset:1024 sc1\n"
"ds_read_b128 v[68:71], v16 offset:35840\n"
"s_add_u32 m0, m0, s69\n"
"ds_read_b128 v[80:83], v16 offset:37888\n"
"s_waitcnt lgkmcnt(5)\n"
"v_pk_add_f32 v[72:73], v[72:73], v[52:53]\n"
"buffer_load_dwordx4 v[196:199], v1, s[24:27], s32 offen offset:2048 sc1\n"
"v_pk_add_f32 v[74:75], v[74:75], v[54:55]\n"
"s_waitcnt lgkmcnt(3)\n"
"v_pk_add_f32 v[60:61], v[60:61], v[84:85]\n"
"v_pk_add_f32 v[62:63], v[62:63], v[86:87]\n"
"v_pk_add_f32 v[72:73], v[72:73], v[60:61]\n"
"v_pk_add_f32 v[74:75], v[74:75], v[62:63]\n"
"s_waitcnt lgkmcnt(2)\n"
"v_pk_add_f32 v[76:77], v[76:77], v[88:89]\n"
"v_pk_add_f32 v[78:79], v[78:79], v[90:91]\n"
"v_cvt_pk_bf16_f32 v52, v72, v73\n"
"v_cvt_pk_bf16_f32 v53, v74, v75\n"
"v_pk_add_f32 v[42:43], v[72:73], v[74:75]\n"
"buffer_load_dwordx4 v[200:203], v1, s[24:27], s32 offen offset:3072 sc1\n"
"s_waitcnt lgkmcnt(0)\n"
"v_pk_add_f32 v[68:69], v[68:69], v[80:81]\n"
"v_pk_add_f32 v[70:71], v[70:71], v[82:83]\n"
"v_pk_add_f32 v[76:77], v[76:77], v[68:69]\n"
"v_pk_add_f32 v[78:79], v[78:79], v[70:71]\n"
"v_cvt_pk_bf16_f32 v54, v76, v77\n"
"v_cvt_pk_bf16_f32 v55, v78, v79\n"
"v_pk_add_f32 v[60:61], v[76:77], v[78:79]\n"
"v_pk_add_f32 v[42:43], v[42:43], v[60:61]\n"
"v_add_f32 v0, v42, v43\n"
"v_mov_b32 v42, v0\n"
"s_nop 1\n"
"v_permlane16_swap_b32 v0, v42\n"
"buffer_store_dwordx4 v[52:55], v1, s[20:23], s52 offen sc1\n"
"v_add_f32 v40, v0, v42\n"
"v_mov_b32 v0, v40\n"
"buffer_load_dwordx4 v[204:207], v1, s[24:27], s2 offen sc1\n"
"s_nop 0\n"
"v_permlane32_swap_b32 v40, v0\n"
"v_add_f32 v60, v40, v0\n"
"v_mul_f32 v42, 0x3d000000, v60\n"
"v_mul_f32 v60, 0xbd000000, v60\n"
"v_pk_add_f32 v[68:69], v[72:73], v[60:61] op_sel_hi:[1,0]\n"
"v_pk_add_f32 v[80:81], v[76:77], v[60:61] op_sel_hi:[1,0]\n"
"v_pk_add_f32 v[70:71], v[74:75], v[60:61] op_sel_hi:[1,0]\n"
"v_pk_add_f32 v[62:63], v[78:79], v[60:61] op_sel_hi:[1,0]\n"
"v_pk_mul_f32 v[60:61], v[68:69], v[68:69]\n"
"v_pk_mul_f32 v[68:69], v[80:81], v[80:81]\n"
"v_pk_fma_f32 v[60:61], v[70:71], v[70:71], v[60:61]\n"
"buffer_load_dwordx4 a[144:147], v1, s[24:27], s2 offen offset:1024 sc1\n"
"v_pk_fma_f32 v[68:69], v[62:63], v[62:63], v[68:69]\n"
"v_pk_add_f32 v[60:61], v[60:61], v[68:69]\n"
"v_add_f32 v40, v60, v61\n"
"v_mov_b32 v60, v40\n"
"s_nop 1\n"
"v_permlane16_swap_b32 v40, v60\n"
"v_add_f32 v0, v40, v60\n"
"v_mov_b32 v40, v0\n"
"s_nop 1\n"
"v_permlane32_swap_b32 v0, v40\n"
"buffer_load_dwordx4 a[140:143], v1, s[24:27], s2 offen offset:2048 sc1\n"
"v_add_f32 v43, v0, v40\n"
"buffer_store_dwordx2 v[42:43], v5, s[20:23], s3 offen sc1\n"
"s_waitcnt vmcnt(8)\n"
"v_mfma_f32_16x16x32_bf16 v[80:83], a[72:75], v[188:191], 0\n"
"ds_write_b128 v9, v[188:191]\n"
"buffer_load_dwordx4 v[88:91], v1, s[24:27], s2 offen offset:3072 sc1\n"
"v_mfma_f32_16x16x32_bf16 v[76:79], a[48:51], v[188:191], 0\n"
"v_mfma_f32_16x16x32_bf16 v[84:87], a[16:19], v[188:191], 0\n"
"s_waitcnt vmcnt(8)\n"
"v_mfma_f32_16x16x32_bf16 v[80:83], a[40:43], v[192:195], v[80:83]\n"
"ds_write_b128 v9, v[192:195] offset:1024\n"
"buffer_load_dwordx4 v[60:63], v1, s[24:27], s0 offen sc1\n"
"v_mfma_f32_16x16x32_bf16 v[76:79], a[32:35], v[192:195], v[76:79]\n"
"v_mfma_f32_16x16x32_bf16 v[84:87], a[20:23], v[192:195], v[84:87]\n"
"s_waitcnt vmcnt(8)\n"
"v_mfma_f32_16x16x32_bf16 v[80:83], a[0:3], v[196:199], v[80:83]\n"
"ds_write_b128 v9, v[196:199] offset:2048\n"
"buffer_store_dwordx4 v[64:67], v1, s[16:19], s1 offen sc1\n"
"v_mfma_f32_16x16x32_bf16 v[76:79], a[4:7], v[196:199], v[76:79]\n"
"v_mfma_f32_16x16x32_bf16 v[84:87], a[24:27], v[196:199], v[84:87]\n"
"s_waitcnt vmcnt(8)\n"
"v_mfma_f32_16x16x32_bf16 v[80:83], a[8:11], v[200:203], v[80:83]\n"
"ds_write_b128 v9, v[200:203] offset:3072\n"
"buffer_load_dwordx4 a[212:215], v2, s[4:7], s47 offen offset:1024\n"
"v_mfma_f32_16x16x32_bf16 v[76:79], a[12:15], v[200:203], v[76:79]\n"
"v_mfma_f32_16x16x32_bf16 v[84:87], a[28:31], v[200:203], v[84:87]\n"
"buffer_load_dwordx4 a[216:219], v2, s[4:7], s47 offen offset:2048\n"
"s_waitcnt vmcnt(8)\n"
"v_mfma_f32_16x16x32_bf16 v[64:67], a[72:75], v[204:207], 0\n"
"ds_write_b128 v9, v[204:207] offset:16384\n"
"v_mfma_f32_16x16x32_bf16 v[68:71], a[48:51], v[204:207], 0\n"
"v_mfma_f32_16x16x32_bf16 v[72:75], a[16:19], v[204:207], 0\n"
"buffer_load_dwordx4 a[220:223], v2, s[4:7], s47 offen offset:3072\n"
"s_waitcnt vmcnt(8)\n"
"v_mfma_f32_16x16x32_bf16 v[64:67], a[40:43], a[144:147], v[64:67]\n"
"ds_write_b128 v9, a[144:147] offset:17408\n"
"v_mfma_f32_16x16x32_bf16 v[68:71], a[32:35], a[144:147], v[68:71]\n"
"v_mfma_f32_16x16x32_bf16 v[72:75], a[20:23], a[144:147], v[72:75]\n"
"buffer_load_dwordx4 a[224:227], v3, s[12:15], s47 offen\n"
"s_waitcnt vmcnt(8)\n"
"v_mfma_f32_16x16x32_bf16 v[64:67], a[0:3], a[140:143], v[64:67]\n"
"ds_write_b128 v9, a[140:143] offset:18432\n"
"v_mfma_f32_16x16x32_bf16 v[68:71], a[4:7], a[140:143], v[68:71]\n"
"v_mfma_f32_16x16x32_bf16 v[72:75], a[24:27], a[140:143], v[72:75]\n"
"buffer_load_dwordx4 a[228:231], v3, s[12:15], s47 offen offset:1024\n"
"s_waitcnt vmcnt(7)\n"
"v_mfma_f32_16x16x32_bf16 v[64:67], a[8:11], v[88:91], v[64:67]\n"
"buffer_load_dwordx4 a[232:235], v3, s[12:15], s47 offen offset:2048\n"
"v_mfma_f32_16x16x32_bf16 v[68:71], a[12:15], v[88:91], v[68:71]\n"
"ds_write_b128 v9, v[88:91] offset:19456\n"
"v_mfma_f32_16x16x32_bf16 v[72:75], a[28:31], v[88:91], v[72:75]\n"
"s_nop 3\n"
"v_cmp_u_f32 vcc, v80, v64\n"
"buffer_load_dwordx4 a[236:239], v3, s[12:15], s47 offen offset:3072\n"
"s_waitcnt vmcnt(8)\n"
"v_add_f32 v40, v60, v62\n"
"v_mov_b32 v42, v40\n"
"v_add_f32 v0, v61, v63\n"
"s_nop 0\n"
"v_permlane16_swap_b32 v40, v42\n"
"v_add_f32 v43, v40, v42\n"
"v_mov_b32 v40, v43\n"
"s_nop 1\n"
"v_permlane32_swap_b32 v43, v40\n"
"v_add_f32 v88, v43, v40\n"
"v_mul_f32 v42, 0x3e000000, v88\n"
"buffer_load_dwordx4 v1, s[16:19], s46 offen offset:0 lds\n"
"v_sub_f32 v40, v60, v42\n"
"v_sub_f32 v88, v62, v42\n"
"v_mul_f32 v60, v40, v40\n"
"v_fmac_f32 v60, v88, v88\n"
"v_fmamk_f32 v40, v60, 0x42000000, v0\n"
"v_mov_b32 v60, v40\n"
"s_mul_i32 m0, s68, 4\n"
"s_add_u32 m0, m0, s69\n"
"v_permlane16_swap_b32 v40, v60\n"
"v_add_f32 v0, v40, v60\n"
"v_mov_b32 v40, v0\n"
"buffer_load_dwordx4 v1, s[16:19], s46 offen offset:1024 lds\n"
"s_mul_i32 m0, s68, 4\n"
"v_permlane32_swap_b32 v0, v40\n"
"v_add_f32 v43, v0, v40\n"
"s_add_u32 m0, m0, s69\n"
"v_cmp_u_f32 s[0:1], v43, v43\n"
"ds_write_b64 v17, v[42:43]\n"
"s_or_b64 vcc, vcc, s[0:1]\n"
"buffer_load_dwordx4 v1, s[16:19], s46 offen offset:2048 lds\n"
"s_cbranch_vccnz Rw3A%=\n"
"Cw3A%=:\n"
"s_waitcnt lgkmcnt(0)\n"
"s_barrier\n"
"ds_read_b128 a[144:147], v10\n"
"s_add_u32 s3, s48, 0x8000\n"
"s_add_u32 s2, s51, 0x100000\n"
"s_add_u32 s1, s52, 0x1c00000\n"
"ds_read_b128 a[140:143], v10 offset:1024\n"
"s_add_u32 s0, s54, 0x10000\n"
"v_mov_b32 v88, -1\n"
"v_mov_b32 v89, -1\n"
"ds_read_b128 v[180:183], v10 offset:2048\n"
"v_mov_b32 v90, -1\n"
"v_mov_b32 v91, -1\n"
"buffer_load_dwordx4 v[184:187], v1, s[20:23], s48 offen sc1\n"
"ds_read_b128 v[192:195], v10 offset:3072\n"
"ds_read_b128 v[60:63], v18\n"
"ds_read_b128 v[92:95], v18 offset:256\n"
"ds_read_b128 v[100:103], v23\n"
"buffer_load_dwordx4 v[188:191], v1, s[20:23], s48 offen offset:1024 sc1\n"
"s_waitcnt lgkmcnt(6)\n"
"v_mfma_f32_16x16x32_bf16 v[80:83], a[56:59], a[144:147], v[80:83]\n"
"ds_read_b128 v[196:199], v11\n"
"v_mfma_f32_16x16x32_bf16 v[84:87], v[216:219], a[144:147], v[84:87]\n"
"ds_read_b128 v[104:107], v22\n"
"v_mfma_f32_16x16x32_bf16 v[76:79], a[152:155], a[144:147], v[76:79]\n"
"ds_read_b128 v[96:99], v22 offset:16\n"
"s_waitcnt lgkmcnt(8)\n"
"v_mfma_f32_16x16x32_bf16 v[80:83], v[208:211], a[140:143], v[80:83]\n"
"buffer_load_dwordx4 v[200:203], v1, s[20:23], s48 offen offset:2048 sc1\n"
"v_mfma_f32_16x16x32_bf16 v[84:87], v[224:227], a[140:143], v[84:87]\n"
"ds_read_b128 v[204:207], v11 offset:1024\n"
"s_waitcnt lgkmcnt(6)\n"
"v_sub_f32 v40, v60, v62\n"
"v_mfma_f32_16x16x32_bf16 v[76:79], v[228:231], a[140:143], v[76:79]\n"
"s_waitcnt lgkmcnt(5)\n"
"v_sub_f32 v42, v92, v94\n"
"v_mul_f32 v0, v40, v40\n"
"v_mfma_f32_16x16x32_bf16 v[80:83], v[212:215], v[180:183], v[80:83]\n"
"ds_read_b128 a[144:147], v11 offset:2048\n"
"v_add_f32 v40, v61, v63\n"
"v_mfma_f32_16x16x32_bf16 v[84:87], a[148:151], v[180:183], v[84:87]\n"
"buffer_load_dwordx4 a[140:143], v1, s[20:23], s48 offen offset:3072 sc1\n"
"v_mfma_f32_16x16x32_bf16 v[76:79], v[232:235], v[180:183], v[76:79]\n"
"v_mul_f32 v43, v42, v42\n"
"v_add_f32 v42, v93, v95\n"
"v_mfma_f32_16x16x32_bf16 v[80:83], v[220:223], v[192:195], v[80:83]\n"
"ds_read_b128 v[112:115], v11 offset:3072\n"
"v_fmamk_f32 v108, v0, 0x43000000, v40\n"
"v_mfma_f32_16x16x32_bf16 v[84:87], v[240:243], v[192:195], v[84:87]\n"
"v_fmamk_f32 v0, v43, 0x43000000, v42\n"
"v_mul_f32 v40, 0x3b000000, v108\n"
"v_mfma_f32_16x16x32_bf16 v[76:79], a[160:163], v[192:195], v[76:79]\n"
"buffer_load_dwordx4 v[192:195], v1, s[20:23], s3 offen sc1\n"
"s_waitcnt lgkmcnt(5)\n"
"v_mfma_f32_16x16x32_bf16 v[80:83], v[236:239], v[196:199], v[80:83]\n"
"ds_read_b128 v[168:171], v12\n"
"v_mul_f32 v43, 0x3b000000, v0\n"
"v_mfma_f32_16x16x32_bf16 v[84:87], v[248:251], v[196:199], v[84:87]\n"
"v_add_f32 v40, 0x3727c5ac, v40\n"
"v_add_f32 v43, 0x3727c5ac, v43\n"
"v_mfma_f32_16x16x32_bf16 v[76:79], v[252:255], v[196:199], v[76:79]\n"
"v_add_f32 v108, v60, v62\n"
"v_add_f32 v42, v92, v94\n"
"s_waitcnt lgkmcnt(3)\n"
"v_mfma_f32_16x16x32_bf16 v[80:83], v[244:247], v[204:207], v[80:83]\n"
"buffer_load_dwordx4 v[196:199], v1, s[20:23], s3 offen offset:1024 sc1\n"
"v_mfma_f32_16x16x32_bf16 v[84:87], a[252:255], v[204:207], v[84:87]\n"
"ds_read_b128 v[172:175], v12 offset:1024\n"
"v_rsq_f32 v0, v40\n"
"v_mfma_f32_16x16x32_bf16 v[76:79], a[248:251], v[204:207], v[76:79]\n"
"v_rsq_f32 v40, v43\n"
"s_waitcnt lgkmcnt(3)\n"
"v_mfma_f32_16x16x32_bf16 v[80:83], a[156:159], a[144:147], v[80:83]\n"
"ds_read_b128 v[176:179], v12 offset:2048\n"
"v_mul_f32 v43, 0.5, v108\n"
"buffer_load_dwordx4 v[204:207], v1, s[20:23], s3 offen offset:2048 sc1\n"
"v_mfma_f32_16x16x32_bf16 v[84:87], a[36:39], a[144:147], v[84:87]\n"
"v_mul_f32 v60, 0.5, v42\n"
"v_mul_f32 v108, -1.0, v43\n"
"v_mfma_f32_16x16x32_bf16 v[76:79], a[240:243], a[144:147], v[76:79]\n"
"v_mul_f32 v118, -1.0, v60\n"
"v_mul_f32 v42, v0, v0\n"
"s_waitcnt lgkmcnt(3)\n"
"v_mfma_f32_16x16x32_bf16 v[80:83], a[244:247], v[112:115], v[80:83]\n"
"ds_read_b128 v[180:183], v12 offset:3072\n"
"v_mul_f32 v43, 0xbdd2d3e8, v0\n"
"v_mfma_f32_16x16x32_bf16 v[84:87], a[132:135], v[112:115], v[84:87]\n"
"buffer_load_dwordx4 a[144:147], v1, s[20:23], s3 offen offset:3072 sc1\n"
"v_mfma_f32_16x16x32_bf16 v[76:79], a[136:139], v[112:115], v[76:79]\n"
"v_mul_f32 v60, v40, v40\n"
"v_mul_f32 v61, 0xbdd2d3e8, v40\n"
"s_waitcnt lgkmcnt(3)\n"
"v_mfma_f32_16x16x32_bf16 v[80:83], a[64:67], v[168:171], v[80:83]\n"
"ds_read_b128 v[164:167], v10 offset:16384\n"
"v_mul_f32 v120, v42, v43\n"
"v_mfma_f32_16x16x32_bf16 v[84:87], a[76:79], v[168:171], v[84:87]\n"
"v_mul_f32 v122, 0xc0135761, v0\n"
"v_mul_f32 v114, v60, v61\n"
"v_mfma_f32_16x16x32_bf16 v[76:79], a[80:83], v[168:171], v[76:79]\n"
"buffer_load_dwordx4 v[60:63], v1, s[20:23], s2 offen sc1\n"
"s_waitcnt lgkmcnt(3)\n"
"v_mfma_f32_16x16x32_bf16 v[80:83], a[44:47], v[172:175], v[80:83]\n"
"ds_read_b128 v[156:159], v10 offset:17408\n"
"v_mul_f32 v116, 0xc0135761, v40\n"
"v_mfma_f32_16x16x32_bf16 v[84:87], a[88:91], v[172:175], v[84:87]\n"
"v_rcp_f32 v94, v0\n"
"v_mfma_f32_16x16x32_bf16 v[76:79], a[96:99], v[172:175], v[76:79]\n"
"v_rcp_f32 v92, v40\n"
"s_waitcnt lgkmcnt(3)\n"
"v_mfma_f32_16x16x32_bf16 v[80:83], a[112:115], v[176:179], v[80:83]\n"
"buffer_store_dwordx4 v[88:91], v1, s[16:19], s1 offen sc1\n"
"v_mfma_f32_16x16x32_bf16 v[84:87], a[104:107], v[176:179], v[84:87]\n"
"ds_read_b128 v[168:171], v10 offset:18432\n"
"v_mfma_f32_16x16x32_bf16 v[76:79], a[52:55], v[176:179], v[76:79]\n"
"s_waitcnt lgkmcnt(3)\n"
"v_mfma_f32_16x16x32_bf16 v[80:83], a[60:63], v[180:183], v[80:83]\n"
"ds_read_b128 v[172:175], v10 offset:19456\n"
"v_mfma_f32_16x16x32_bf16 v[84:87], a[120:123], v[180:183], v[84:87]\n"
"v_mfma_f32_16x16x32_bf16 v[76:79], a[68:71], v[180:183], v[76:79]\n"
"s_waitcnt lgkmcnt(3)\n"
"v_mfma_f32_16x16x32_bf16 v[64:67], a[56:59], v[164:167], v[64:67]\n"
"ds_read_b128 v[176:179], v11 offset:16384\n"
"s_waitcnt lgkmcnt(3)\n"
"v_mfma_f32_16x16x32_bf16 v[64:67], v[208:211], v[156:159], v[64:67]\n"
"ds_read_b128 v[180:183], v11 offset:17408\n"
"v_mfma_f32_16x16x32_bf16 v[72:75], v[216:219], v[164:167], v[72:75]\n"
"v_fma_f32 v42, v104, v108, v80\n"
"v_fma_f32 v43, v105, v108, v81\n"
"s_waitcnt lgkmcnt(3)\n"
"v_mfma_f32_16x16x32_bf16 v[64:67], v[212:215], v[168:171], v[64:67]\n"
"ds_read_b128 v[160:163], v11 offset:18432\n"
"v_fma_f32 v88, v100, v108, v84\n"
"v_mfma_f32_16x16x32_bf16 v[68:71], a[152:155], v[164:167], v[68:71]\n"
"v_fma_f32 v89, v101, v108, v85\n"
"v_fma_f32 v90, v102, v108, v86\n"
"s_waitcnt lgkmcnt(3)\n"
"v_mfma_f32_16x16x32_bf16 v[64:67], v[220:223], v[172:175], v[64:67]\n"
"ds_read_b128 v[164:167], v11 offset:19456\n"
"v_fma_f32 v91, v103, v108, v87\n"
"v_mfma_f32_16x16x32_bf16 v[72:75], v[224:227], v[156:159], v[72:75]\n"
"v_fma_f32 v84, v106, v108, v82\n"
"v_fma_f32 v85, v107, v108, v83\n"
"v_mfma_f32_16x16x32_bf16 v[68:71], v[228:231], v[156:159], v[68:71]\n"
"v_fma_f32 v80, v96, v108, v76\n"
"v_fma_f32 v81, v97, v108, v77\n"
"v_mfma_f32_16x16x32_bf16 v[72:75], a[148:151], v[168:171], v[72:75]\n"
"v_fma_f32 v82, v98, v108, v78\n"
"v_fma_f32 v83, v99, v108, v79\n"
"v_mfma_f32_16x16x32_bf16 v[68:71], v[232:235], v[168:171], v[68:71]\n"
"v_mul_f32 v86, v88, v88\n"
"v_mul_f32 v87, v89, v89\n"
"s_waitcnt lgkmcnt(3)\n"
"v_mfma_f32_16x16x32_bf16 v[64:67], v[236:239], v[176:179], v[64:67]\n"
"ds_read_b128 v[168:171], v12 offset:16384\n"
"v_mul_f32 v108, v90, v90\n"
"v_mfma_f32_16x16x32_bf16 v[72:75], v[240:243], v[172:175], v[72:75]\n"
"v_mul_f32 v109, v91, v91\n"
"v_mul_f32 v76, v42, v42\n"
"v_mfma_f32_16x16x32_bf16 v[68:71], a[160:163], v[172:175], v[68:71]\n"
"v_mul_f32 v77, v43, v43\n"
"v_mul_f32 v78, v84, v84\n"
"s_waitcnt lgkmcnt(3)\n"
"v_mfma_f32_16x16x32_bf16 v[64:67], v[244:247], v[180:183], v[64:67]\n"
"ds_read_b128 v[172:175], v12 offset:17408\n"
"v_mul_f32 v79, v85, v85\n"
"v_mfma_f32_16x16x32_bf16 v[72:75], v[248:251], v[176:179], v[72:75]\n"
"v_mul_f32 v110, v80, v80\n"
"v_mul_f32 v111, v81, v81\n"
"v_mfma_f32_16x16x32_bf16 v[68:71], v[252:255], v[176:179], v[68:71]\n"
"v_mul_f32 v112, v82, v82\n"
"v_mul_f32 v113, v83, v83\n"
"s_waitcnt lgkmcnt(3)\n"
"v_mfma_f32_16x16x32_bf16 v[64:67], a[156:159], v[160:163], v[64:67]\n"
"ds_read_b128 v[176:179], v12 offset:18432\n"
"v_fma_f32 v86, v86, v120, v122\n"
"v_mfma_f32_16x16x32_bf16 v[72:75], a[252:255], v[180:183], v[72:75]\n"
"v_fma_f32 v87, v87, v120, v122\n"
"v_fma_f32 v108, v108, v120, v122\n"
"v_mfma_f32_16x16x32_bf16 v[68:71], a[248:251], v[180:183], v[68:71]\n"
"v_fma_f32 v109, v109, v120, v122\n"
"v_fma_f32 v76, v76, v120, v122\n"
"s_waitcnt lgkmcnt(3)\n"
"v_mfma_f32_16x16x32_bf16 v[64:67], a[244:247], v[164:167], v[64:67]\n"
"ds_read_b128 v[180:183], v12 offset:19456\n"
"v_fma_f32 v77, v77, v120, v122\n"
"v_mfma_f32_16x16x32_bf16 v[72:75], a[36:39], v[160:163], v[72:75]\n"
"v_fma_f32 v78, v78, v120, v122\n"
"v_fma_f32 v79, v79, v120, v122\n"
"v_mfma_f32_16x16x32_bf16 v[68:71], a[240:243], v[160:163], v[68:71]\n"
"v_fma_f32 v110, v110, v120, v122\n"
"v_fma_f32 v111, v111, v120, v122\n"
"v_mfma_f32_16x16x32_bf16 v[72:75], a[132:135], v[164:167], v[72:75]\n"
"v_fma_f32 v112, v112, v120, v122\n"
"v_fma_f32 v113, v113, v120, v122\n"
"v_mfma_f32_16x16x32_bf16 v[68:71], a[136:139], v[164:167], v[68:71]\n"
"v_mul_f32 v86, v86, v88\n"
"v_mul_f32 v87, v87, v89\n"
"s_waitcnt lgkmcnt(3)\n"
"v_mfma_f32_16x16x32_bf16 v[72:75], a[76:79], v[168:171], v[72:75]\n"
"v_mul_f32 v108, v108, v90\n"
"v_mul_f32 v109, v109, v91\n"
"v_mfma_f32_16x16x32_bf16 v[64:67], a[64:67], v[168:171], v[64:67]\n"
"v_mul_f32 v76, v76, v42\n"
"v_mul_f32 v77, v77, v43\n"
"v_mfma_f32_16x16x32_bf16 v[68:71], a[80:83], v[168:171], v[68:71]\n"
"v_mul_f32 v78, v78, v84\n"
"v_mul_f32 v79, v79, v85\n"
"s_waitcnt lgkmcnt(2)\n"
"v_mfma_f32_16x16x32_bf16 v[72:75], a[88:91], v[172:175], v[72:75]\n"
"v_mul_f32 v110, v110, v80\n"
"v_mul_f32 v111, v111, v81\n"
"v_mfma_f32_16x16x32_bf16 v[64:67], a[44:47], v[172:175], v[64:67]\n"
"v_mul_f32 v112, v112, v82\n"
"v_mul_f32 v113, v113, v83\n"
"v_mfma_f32_16x16x32_bf16 v[68:71], a[96:99], v[172:175], v[68:71]\n"
"v_exp_f32 v86, v86\n"
"s_waitcnt lgkmcnt(1)\n"
"v_mfma_f32_16x16x32_bf16 v[72:75], a[104:107], v[176:179], v[72:75]\n"
"v_exp_f32 v87, v87\n"
"v_mfma_f32_16x16x32_bf16 v[64:67], a[112:115], v[176:179], v[64:67]\n"
"v_exp_f32 v108, v108\n"
"v_mfma_f32_16x16x32_bf16 v[68:71], a[52:55], v[176:179], v[68:71]\n"
"v_exp_f32 v109, v109\n"
"s_waitcnt lgkmcnt(0)\n"
"v_mfma_f32_16x16x32_bf16 v[72:75], a[120:123], v[180:183], v[72:75]\n"
"v_exp_f32 v76, v76\n"
"v_mfma_f32_16x16x32_bf16 v[64:67], a[60:63], v[180:183], v[64:67]\n"
"v_exp_f32 v77, v77\n"
"v_mfma_f32_16x16x32_bf16 v[68:71], a[68:71], v[180:183], v[68:71]\n"
"v_exp_f32 v78, v78\n"
"v_exp_f32 v79, v79\n"
"v_exp_f32 v110, v110\n"
"v_exp_f32 v111, v111\n"
"v_fma_f32 v120, v100, v118, v72\n"
"v_fma_f32 v121, v101, v118, v73\n"
"v_fma_f32 v122, v102, v118, v74\n"
"v_fma_f32 v123, v103, v118, v75\n"
"v_fma_f32 v72, v104, v118, v64\n"
"v_fma_f32 v73, v105, v118, v65\n"
"v_fma_f32 v74, v106, v118, v66\n"
"v_fma_f32 v75, v107, v118, v67\n"
"v_fma_f32 v64, v96, v118, v68\n"
"v_fma_f32 v65, v97, v118, v69\n"
"v_fma_f32 v66, v98, v118, v70\n"
"v_fma_f32 v67, v99, v118, v71\n"
"v_mul_f32 v100, v120, v120\n"
"v_mul_f32 v101, v121, v121\n"
"v_mul_f32 v102, v122, v122\n"
"v_mul_f32 v103, v123, v123\n"
"v_mul_f32 v68, v72, v72\n"
"v_mul_f32 v69, v73, v73\n"
"v_mul_f32 v70, v74, v74\n"
"v_mul_f32 v71, v75, v75\n"
"v_mul_f32 v96, v64, v64\n"
"v_mul_f32 v97, v65, v65\n"
"v_mul_f32 v98, v66, v66\n"
"v_mul_f32 v99, v67, v67\n"
"v_fma_f32 v100, v100, v114, v116\n"
"v_fma_f32 v101, v101, v114, v116\n"
"v_fma_f32 v102, v102, v114, v116\n"
"v_fma_f32 v103, v103, v114, v116\n"
"v_fma_f32 v68, v68, v114, v116\n"
"v_fma_f32 v69, v69, v114, v116\n"
"v_fma_f32 v70, v70, v114, v116\n"
"v_fma_f32 v71, v71, v114, v116\n"
"v_fma_f32 v96, v96, v114, v116\n"
"v_fma_f32 v97, v97, v114, v116\n"
"v_fma_f32 v98, v98, v114, v116\n"
"v_fma_f32 v99, v99, v114, v116\n"
"v_mul_f32 v100, v100, v120\n"
"v_mul_f32 v101, v101, v121\n"
"v_mul_f32 v102, v102, v122\n"
"v_mul_f32 v103, v103, v123\n"
"v_mul_f32 v68, v68, v72\n"
"v_mul_f32 v69, v69, v73\n"
"v_mul_f32 v70, v70, v74\n"
"v_mul_f32 v71, v71, v75\n"
"v_mul_f32 v96, v96, v64\n"
"v_mul_f32 v97, v97, v65\n"
"v_mul_f32 v98, v98, v66\n"
"v_mul_f32 v99, v99, v67\n"
"v_exp_f32 v100, v100\n"
"v_exp_f32 v101, v101\n"
"v_exp_f32 v102, v102\n"
"v_exp_f32 v103, v103\n"
"v_exp_f32 v112, v112\n"
"v_exp_f32 v113, v113\n"
"v_exp_f32 v68, v68\n"
"v_exp_f32 v69, v69\n"
"v_exp_f32 v70, v70\n"
"v_exp_f32 v71, v71\n"
"v_exp_f32 v96, v96\n"
"v_exp_f32 v97, v97\n"
"v_exp_f32 v98, v98\n"
"v_exp_f32 v99, v99\n"
"v_fma_f32 v86, v86, v94, v94\n"
"v_fma_f32 v87, v87, v94, v94\n"
"v_fma_f32 v108, v108, v94, v94\n"
"v_fma_f32 v109, v109, v94, v94\n"
"v_fma_f32 v100, v100, v92, v92\n"
"v_fma_f32 v101, v101, v92, v92\n"
"v_fma_f32 v102, v102, v92, v92\n"
"v_fma_f32 v103, v103, v92, v92\n"
"v_fma_f32 v76, v76, v94, v94\n"
"v_fma_f32 v77, v77, v94, v94\n"
"v_fma_f32 v78, v78, v94, v94\n"
"v_fma_f32 v79, v79, v94, v94\n"
"v_fma_f32 v110, v110, v94, v94\n"
"v_fma_f32 v111, v111, v94, v94\n"
"v_fma_f32 v112, v112, v94, v94\n"
"v_fma_f32 v113, v113, v94, v94\n"
"v_fma_f32 v68, v68, v92, v92\n"
"v_fma_f32 v69, v69, v92, v92\n"
"v_fma_f32 v70, v70, v92, v92\n"
"v_fma_f32 v71, v71, v92, v92\n"
"v_fma_f32 v96, v96, v92, v92\n"
"v_fma_f32 v97, v97, v92, v92\n"
"v_fma_f32 v98, v98, v92, v92\n"
"v_fma_f32 v99, v99, v92, v92\n"
"v_rcp_f32 v86, v86\n"
"v_rcp_f32 v87, v87\n"
"v_rcp_f32 v108, v108\n"
"v_rcp_f32 v109, v109\n"
"v_rcp_f32 v100, v100\n"
"v_rcp_f32 v101, v101\n"
"v_rcp_f32 v102, v102\n"
"v_rcp_f32 v103, v103\n"
"v_rcp_f32 v76, v76\n"
"v_rcp_f32 v77, v77\n"
"v_rcp_f32 v78, v78\n"
"v_rcp_f32 v79, v79\n"
"v_rcp_f32 v110, v110\n"
"v_rcp_f32 v111, v111\n"
"v_rcp_f32 v112, v112\n"
"v_rcp_f32 v113, v113\n"
"v_rcp_f32 v68, v68\n"
"v_rcp_f32 v69, v69\n"
"v_rcp_f32 v70, v70\n"
"v_rcp_f32 v71, v71\n"
"v_rcp_f32 v96, v96\n"
"v_rcp_f32 v97, v97\n"
"v_rcp_f32 v98, v98\n"
"v_rcp_f32 v99, v99\n"
"v_mul_f32 v88, v88, v86\n"
"v_mul_f32 v89, v89, v87\n"
"v_mul_f32 v90, v90, v108\n"
"v_mul_f32 v91, v91, v109\n"
"v_mul_f32 v120, v120, v100\n"
"v_mul_f32 v121, v121, v101\n"
"v_mul_f32 v122, v122, v102\n"
"v_mul_f32 v123, v123, v103\n"
"v_mul_f32 v42, v42, v76\n"
"v_mul_f32 v43, v43, v77\n"
"v_mul_f32 v84, v84, v78\n"
"v_mul_f32 v85, v85, v79\n"
"v_mul_f32 v80, v80, v110\n"
"v_mul_f32 v81, v81, v111\n"
"v_mul_f32 v82, v82, v112\n"
"v_mul_f32 v83, v83, v113\n"
"v_mul_f32 v72, v72, v68\n"
"v_mul_f32 v73, v73, v69\n"
"v_mul_f32 v74, v74, v70\n"
"v_mul_f32 v75, v75, v71\n"
"v_mul_f32 v64, v64, v96\n"
"v_mul_f32 v65, v65, v97\n"
"v_mul_f32 v66, v66, v98\n"
"v_mul_f32 v67, v67, v99\n"
"v_cvt_pk_bf16_f32 v68, v88, v89\n"
"v_cvt_pk_bf16_f32 v69, v90, v91\n"
"v_cvt_pk_bf16_f32 v70, v120, v121\n"
"v_cvt_pk_bf16_f32 v71, v122, v123\n"
"v_cvt_pk_bf16_f32 v76, v42, v43\n"
"v_cvt_pk_bf16_f32 v77, v84, v85\n"
"v_cvt_pk_bf16_f32 v78, v80, v81\n"
"v_cvt_pk_bf16_f32 v79, v82, v83\n"
"buffer_store_dwordx4 v[76:79], v1, s[4:7], s54 offen sc1\n"
"s_nop 1\n"
"v_cvt_pk_bf16_f32 v76, v72, v73\n"
"v_cvt_pk_bf16_f32 v77, v74, v75\n"
"v_cvt_pk_bf16_f32 v78, v64, v65\n"
"v_cvt_pk_bf16_f32 v79, v66, v67\n"
"buffer_store_dwordx4 v[76:79], v1, s[4:7], s0 offen sc1\n"
"v_cndmask_b32 v64, v68, v70, s[74:75]\n"
"v_cndmask_b32 v66, v70, v68, s[74:75]\n"
"v_cndmask_b32 v65, v69, v71, s[74:75]\n"
"v_cndmask_b32 v67, v71, v69, s[74:75]\n"
"buffer_store_dwordx4 v[64:67], v1, s[8:11], s55 offen sc1\n"
"s_waitcnt vmcnt(12)\n"
"v_mfma_f32_16x16x32_bf16 v[88:91], a[72:75], v[184:187], 0\n"
"s_waitcnt vmcnt(4)\n"
"v_add_f32 v40, v60, v62\n"
"v_mov_b32 v43, v40\n"
"v_mfma_f32_16x16x32_bf16 v[76:79], a[72:75], v[192:195], 0\n"
"v_add_f32 v0, v61, v63\n"
"v_permlane16_swap_b32 v40, v43\n"
"v_mfma_f32_16x16x32_bf16 v[88:91], a[40:43], v[188:191], v[88:91]\n"
"v_add_f32 v42, v40, v43\n"
"v_mov_b32 v40, v42\n"
"v_mfma_f32_16x16x32_bf16 v[76:79], a[40:43], v[196:199], v[76:79]\n"
"ds_write_b128 v9, v[184:187] offset:32768\n"
"v_mfma_f32_16x16x32_bf16 v[88:91], a[0:3], v[200:203], v[88:91]\n"
"v_permlane32_swap_b32 v42, v40\n"
"v_add_f32 v64, v42, v40\n"
"v_mfma_f32_16x16x32_bf16 v[76:79], a[0:3], v[204:207], v[76:79]\n"
"v_mul_f32 v42, 0x3e000000, v64\n"
"v_sub_f32 v40, v60, v42\n"
"v_mfma_f32_16x16x32_bf16 v[92:95], a[48:51], v[184:187], 0\n"
"v_sub_f32 v64, v62, v42\n"
"v_mul_f32 v60, v40, v40\n"
"v_mfma_f32_16x16x32_bf16 v[96:99], a[16:19], v[184:187], 0\n"
"v_fmac_f32 v60, v64, v64\n"
"v_fmamk_f32 v40, v60, 0x42000000, v0\n"
"v_mfma_f32_16x16x32_bf16 v[80:83], a[48:51], v[192:195], 0\n"
"v_mov_b32 v60, v40\n"
"ds_write_b128 v9, v[188:191] offset:33792\n"
"v_mfma_f32_16x16x32_bf16 v[84:87], a[16:19], v[192:195], 0\n"
"v_permlane16_swap_b32 v40, v60\n"
"v_add_f32 v0, v40, v60\n"
"v_mfma_f32_16x16x32_bf16 v[88:91], a[8:11], a[140:143], v[88:91]\n"
"v_mov_b32 v40, v0\n"
"ds_write_b128 v9, v[200:203] offset:34816\n"
"v_mfma_f32_16x16x32_bf16 v[76:79], a[8:11], a[144:147], v[76:79]\n"
"v_permlane32_swap_b32 v0, v40\n"
"v_add_f32 v43, v0, v40\n"
"v_mfma_f32_16x16x32_bf16 v[92:95], a[32:35], v[188:191], v[92:95]\n"
"v_cmp_u_f32 s[0:1], v43, v43\n"
"ds_write_b128 v9, a[140:143] offset:35840\n"
"v_mfma_f32_16x16x32_bf16 v[96:99], a[20:23], v[188:191], v[96:99]\n"
"ds_write_b128 v9, v[192:195] offset:49152\n"
"v_mfma_f32_16x16x32_bf16 v[80:83], a[32:35], v[196:199], v[80:83]\n"
"v_cmp_u_f32 vcc, v88, v76\n"
"ds_write_b128 v9, v[196:199] offset:50176\n"
"v_mfma_f32_16x16x32_bf16 v[84:87], a[20:23], v[196:199], v[84:87]\n"
"ds_write_b128 v9, v[204:207] offset:51200\n"
"v_mfma_f32_16x16x32_bf16 v[92:95], a[4:7], v[200:203], v[92:95]\n"
"ds_write_b128 v9, a[144:147] offset:52224\n"
"v_mfma_f32_16x16x32_bf16 v[96:99], a[24:27], v[200:203], v[96:99]\n"
"ds_write_b64 v17, v[42:43] offset:512\n"
"v_mfma_f32_16x16x32_bf16 v[80:83], a[4:7], v[204:207], v[80:83]\n"
"s_or_b64 vcc, vcc, s[0:1]\n"
"v_mfma_f32_16x16x32_bf16 v[84:87], a[24:27], v[204:207], v[84:87]\n"
"v_mfma_f32_16x16x32_bf16 v[92:95], a[12:15], a[140:143], v[92:95]\n"
"v_mfma_f32_16x16x32_bf16 v[96:99], a[28:31], a[140:143], v[96:99]\n"
"v_mfma_f32_16x16x32_bf16 v[80:83], a[12:15], a[144:147], v[80:83]\n"
"v_mfma_f32_16x16x32_bf16 v[84:87], a[28:31], a[144:147], v[84:87]\n"
"s_cbranch_vccnz Rw3B%=\n"
"Cw3B%=:\n"
"s_waitcnt lgkmcnt(0)\n"
"s_barrier\n"
"buffer_load_dwordx4 a[0:3], v1, s[16:19], s36 offen\n"
"ds_read_b128 a[12:15], v10 offset:32768\n"
"s_add_u32 s32, s54, 0x400000\n"
"s_add_u32 s3, s54, 0x410000\n"
"s_add_u32 s2, s55, 0x100000\n"
"ds_read_b128 a[28:31], v10 offset:33792\n"
"s_add_u32 s0, s57, 0x400000\n"
"s_add_u32 s1, s56, 0x400000\n"
"v_lshlrev_b32 v60, 16, v48\n"
"ds_read_b128 a[16:19], v10 offset:34816\n"
"v_and_b32 v61, s72, v48\n"
"v_lshlrev_b32 v62, 16, v49\n"
"v_and_b32 v63, s72, v49\n"
"ds_read_b128 a[20:23], v10 offset:35840\n"
"v_lshlrev_b32 v64, 16, v50\n"
"buffer_load_dwordx4 a[4:7], v1, s[16:19], s36 offen offset:1024\n"
"ds_read_b128 v[100:103], v18 offset:512\n"
"v_and_b32 v65, s72, v50\n"
"v_lshlrev_b32 v66, 16, v51\n"
"v_and_b32 v67, s72, v51\n"
"ds_read_b128 v[104:107], v18 offset:768\n"
"v_lshlrev_b32 v68, 16, v56\n"
"v_and_b32 v69, s72, v56\n"
"v_lshlrev_b32 v70, 16, v57\n"
"ds_read_b128 v[116:119], v23\n"
"v_and_b32 v71, s72, v57\n"
"s_waitcnt lgkmcnt(6)\n"
"v_mfma_f32_16x16x32_bf16 v[88:91], a[56:59], a[12:15], v[88:91]\n"
"ds_read_b128 a[24:27], v11 offset:32768\n"
"buffer_load_dwordx4 a[8:11], v1, s[16:19], s36 offen offset:2048\n"
"v_mfma_f32_16x16x32_bf16 v[96:99], v[216:219], a[12:15], v[96:99]\n"
"ds_read_b128 v[128:131], v22\n"
"v_lshlrev_b32 v72, 16, v58\n"
"v_mfma_f32_16x16x32_bf16 v[92:95], a[152:155], a[12:15], v[92:95]\n"
"ds_read_b128 v[120:123], v22 offset:16\n"
"v_and_b32 v73, s72, v58\n"
"s_waitcnt lgkmcnt(8)\n"
"v_mfma_f32_16x16x32_bf16 v[88:91], v[208:211], a[28:31], v[88:91]\n"
"ds_read_b128 a[48:51], v11 offset:33792\n"
"s_waitcnt lgkmcnt(6)\n"
"v_sub_f32 v40, v100, v102\n"
"v_mfma_f32_16x16x32_bf16 v[96:99], v[224:227], a[28:31], v[96:99]\n"
"buffer_load_dwordx4 a[12:15], v1, s[16:19], s36 offen offset:3072\n"
"v_mfma_f32_16x16x32_bf16 v[92:95], v[228:231], a[28:31], v[92:95]\n"
"s_waitcnt lgkmcnt(5)\n"
"v_sub_f32 v43, v104, v106\n"
"v_mul_f32 v0, v40, v40\n"
"v_mfma_f32_16x16x32_bf16 v[88:91], v[212:215], a[16:19], v[88:91]\n"
"ds_read_b128 a[28:31], v11 offset:34816\n"
"v_add_f32 v40, v101, v103\n"
"v_mfma_f32_16x16x32_bf16 v[96:99], a[148:151], a[16:19], v[96:99]\n"
"v_mul_f32 v42, v43, v43\n"
"v_add_f32 v43, v105, v107\n"
"v_mfma_f32_16x16x32_bf16 v[92:95], v[232:235], a[16:19], v[92:95]\n"
"buffer_load_dwordx4 a[16:19], v2, s[16:19], s36 offen\n"
"v_mfma_f32_16x16x32_bf16 v[88:91], v[220:223], a[20:23], v[88:91]\n"
"ds_read_b128 a[32:35], v11 offset:35840\n"
"v_fmamk_f32 v108, v0, 0x43000000, v40\n"
"v_mfma_f32_16x16x32_bf16 v[96:99], v[240:243], a[20:23], v[96:99]\n"
"v_fmamk_f32 v0, v42, 0x43000000, v43\n"
"v_mul_f32 v40, 0x3b000000, v108\n"
"v_mfma_f32_16x16x32_bf16 v[92:95], a[160:163], a[20:23], v[92:95]\n"
"v_mul_f32 v42, 0x3b000000, v0\n"
"v_add_f32 v40, 0x3727c5ac, v40\n"
"s_waitcnt lgkmcnt(5)\n"
"v_mfma_f32_16x16x32_bf16 v[88:91], v[236:239], a[24:27], v[88:91]\n"
"buffer_load_dwordx4 a[20:23], v2, s[16:19], s36 offen offset:1024\n"
"v_mfma_f32_16x16x32_bf16 v[96:99], v[248:251], a[24:27], v[96:99]\n"
"ds_read_b128 a[40:43], v12 offset:32768\n"
"v_add_f32 v42, 0x3727c5ac, v42\n"
"v_mfma_f32_16x16x32_bf16 v[92:95], v[252:255], a[24:27], v[92:95]\n"
"v_add_f32 v108, v100, v102\n"
"v_add_f32 v43, v104, v106\n"
"s_waitcnt lgkmcnt(3)\n"
"v_mfma_f32_16x16x32_bf16 v[88:91], v[244:247], a[48:51], v[88:91]\n"
"ds_read_b128 a[72:75], v12 offset:33792\n"
"v_rsq_f32 v0, v40\n"
"v_mfma_f32_16x16x32_bf16 v[96:99], a[252:255], a[48:51], v[96:99]\n"
"buffer_load_dwordx4 a[24:27], v2, s[16:19], s36 offen offset:2048\n"
"v_mfma_f32_16x16x32_bf16 v[92:95], a[248:251], a[48:51], v[92:95]\n"
"v_rsq_f32 v40, v42\n"
"s_waitcnt lgkmcnt(3)\n"
"v_mfma_f32_16x16x32_bf16 v[88:91], a[156:159], a[28:31], v[88:91]\n"
"ds_read_b128 a[48:51], v12 offset:34816\n"
"v_mul_f32 v100, 0.5, v108\n"
"v_mfma_f32_16x16x32_bf16 v[96:99], a[36:39], a[28:31], v[96:99]\n"
"v_mul_f32 v42, 0.5, v43\n"
"v_mul_f32 v114, -1.0, v100\n"
"v_mfma_f32_16x16x32_bf16 v[92:95], a[240:243], a[28:31], v[92:95]\n"
"buffer_load_dwordx4 a[28:31], v2, s[16:19], s36 offen offset:3072\n"
"s_waitcnt lgkmcnt(3)\n"
"v_mfma_f32_16x16x32_bf16 v[88:91], a[244:247], a[32:35], v[88:91]\n"
"ds_read_b128 a[144:147], v12 offset:35840\n"
"v_mul_f32 v146, -1.0, v42\n"
"v_mfma_f32_16x16x32_bf16 v[96:99], a[132:135], a[32:35], v[96:99]\n"
"v_mul_f32 v42, v0, v0\n"
"v_mul_f32 v100, 0xbdd2d3e8, v0\n"
"v_mfma_f32_16x16x32_bf16 v[92:95], a[136:139], a[32:35], v[92:95]\n"
"v_mul_f32 v43, v40, v40\n"
"v_mul_f32 v101, 0xbdd2d3e8, v40\n"
"s_waitcnt lgkmcnt(3)\n"
"v_mfma_f32_16x16x32_bf16 v[88:91], a[64:67], a[40:43], v[88:91]\n"
"buffer_load_dwordx4 a[32:35], v3, s[16:19], s36 offen\n"
"v_mfma_f32_16x16x32_bf16 v[96:99], a[76:79], a[40:43], v[96:99]\n"
"ds_read_b128 v[196:199], v10 offset:49152\n"
"v_mul_f32 v138, v42, v100\n"
"v_mfma_f32_16x16x32_bf16 v[92:95], a[80:83], a[40:43], v[92:95]\n"
"v_mul_f32 v142, 0xc0135761, v0\n"
"v_mul_f32 v140, v43, v101\n"
"s_waitcnt lgkmcnt(3)\n"
"v_mfma_f32_16x16x32_bf16 v[88:91], a[44:47], a[72:75], v[88:91]\n"
"ds_read_b128 v[104:107], v10 offset:50176\n"
"v_mul_f32 v144, 0xc0135761, v40\n"
"v_mfma_f32_16x16x32_bf16 v[96:99], a[88:91], a[72:75], v[96:99]\n"
"buffer_load_dwordx4 a[40:43], v3, s[16:19], s36 offen offset:2048\n"
"v_mfma_f32_16x16x32_bf16 v[92:95], a[96:99], a[72:75], v[92:95]\n"
"v_rcp_f32 v112, v0\n"
"s_waitcnt lgkmcnt(3)\n"
"v_mfma_f32_16x16x32_bf16 v[88:91], a[112:115], a[48:51], v[88:91]\n"
"ds_read_b128 v[200:203], v10 offset:51200\n"
"v_rcp_f32 v110, v40\n"
"v_mfma_f32_16x16x32_bf16 v[96:99], a[104:107], a[48:51], v[96:99]\n"
"ds_read_b128 a[72:75], v28 offset:2048\n"
"v_lshlrev_b32 v74, 16, v59\n"
"v_mfma_f32_16x16x32_bf16 v[92:95], a[52:55], a[48:51], v[92:95]\n"
"buffer_load_dwordx4 a[48:51], v4, s[16:19], s36 offen\n"
"s_waitcnt lgkmcnt(4)\n"
"v_mfma_f32_16x16x32_bf16 v[88:91], a[60:63], a[144:147], v[88:91]\n"
"ds_read_b128 v[204:207], v10 offset:52224\n"
"v_and_b32 v75, s72, v59\n"
"v_mfma_f32_16x16x32_bf16 v[96:99], a[120:123], a[144:147], v[96:99]\n"
"ds_read_b128 a[140:143], v28\n"
"v_add_f32 v60, v60, v68\n"
"v_mfma_f32_16x16x32_bf16 v[92:95], a[68:71], a[144:147], v[92:95]\n"
"ds_read_b128 a[144:147], v28 offset:1024\n"
"v_add_f32 v61, v61, v69\n"
"s_waitcnt lgkmcnt(6)\n"
"v_mfma_f32_16x16x32_bf16 v[76:79], a[56:59], v[196:199], v[76:79]\n"
"buffer_load_dwordx4 a[56:59], v4, s[16:19], s36 offen offset:2048\n"
"s_waitcnt lgkmcnt(5)\n"
"v_mfma_f32_16x16x32_bf16 v[76:79], v[208:211], v[104:107], v[76:79]\n"
"ds_read_b128 v[208:211], v11 offset:49152\n"
"v_fma_f32 v42, v116, v114, v96\n"
"s_waitcnt lgkmcnt(5)\n"
"v_mfma_f32_16x16x32_bf16 v[76:79], v[212:215], v[200:203], v[76:79]\n"
"ds_read_b128 v[212:215], v11 offset:50176\n"
"v_fma_f32 v43, v117, v114, v97\n"
"v_mfma_f32_16x16x32_bf16 v[84:87], v[216:219], v[196:199], v[84:87]\n"
"ds_read_b128 v[216:219], v11 offset:51200\n"
"v_fma_f32 v100, v118, v114, v98\n"
"v_mfma_f32_16x16x32_bf16 v[80:83], a[152:155], v[196:199], v[80:83]\n"
"buffer_load_dwordx4 a[152:155], v1, s[4:7], s57 offen sc1\n"
"s_waitcnt lgkmcnt(5)\n"
"v_mfma_f32_16x16x32_bf16 v[76:79], v[220:223], v[204:207], v[76:79]\n"
"ds_read_b128 v[220:223], v11 offset:52224\n"
"v_fma_f32 v101, v119, v114, v99\n"
"v_mfma_f32_16x16x32_bf16 v[84:87], v[224:227], v[104:107], v[84:87]\n"
"v_fma_f32 v102, v128, v114, v88\n"
"v_fma_f32 v103, v129, v114, v89\n"
"v_mfma_f32_16x16x32_bf16 v[80:83], v[228:231], v[104:107], v[80:83]\n"
"v_fma_f32 v104, v130, v114, v90\n"
"v_fma_f32 v105, v131, v114, v91\n"
"v_mfma_f32_16x16x32_bf16 v[84:87], a[148:151], v[200:203], v[84:87]\n"
"buffer_load_dwordx4 a[148:151], v1, s[4:7], s56 offen sc1\n"
"v_mfma_f32_16x16x32_bf16 v[80:83], v[232:235], v[200:203], v[80:83]\n"
"v_fma_f32 v106, v120, v114, v92\n"
"v_fma_f32 v107, v121, v114, v93\n"
"s_waitcnt lgkmcnt(3)\n"
"v_mfma_f32_16x16x32_bf16 v[76:79], v[236:239], v[208:211], v[76:79]\n"
"ds_read_b128 v[236:239], v12 offset:49152\n"
"v_fma_f32 v108, v122, v114, v94\n"
"v_mfma_f32_16x16x32_bf16 v[84:87], v[240:243], v[204:207], v[84:87]\n"
"v_fma_f32 v109, v123, v114, v95\n"
"v_mul_f32 v114, v42, v42\n"
"v_mfma_f32_16x16x32_bf16 v[80:83], a[160:163], v[204:207], v[80:83]\n"
"buffer_load_dwordx4 a[160:163], v1, s[4:7], s57 offen offset:1024 sc1\n"
"s_waitcnt lgkmcnt(3)\n"
"v_mfma_f32_16x16x32_bf16 v[76:79], v[244:247], v[212:215], v[76:79]\n"
"ds_read_b128 v[244:247], v12 offset:50176\n"
"v_mul_f32 v115, v43, v43\n"
"v_mfma_f32_16x16x32_bf16 v[84:87], v[248:251], v[208:211], v[84:87]\n"
"v_mul_f32 v132, v100, v100\n"
"v_mul_f32 v133, v101, v101\n"
"v_mfma_f32_16x16x32_bf16 v[80:83], v[252:255], v[208:211], v[80:83]\n"
"v_mul_f32 v124, v102, v102\n"
"v_mul_f32 v125, v103, v103\n"
"s_waitcnt lgkmcnt(3)\n"
"v_mfma_f32_16x16x32_bf16 v[76:79], a[156:159], v[216:219], v[76:79]\n"
"buffer_load_dwordx4 a[156:159], v1, s[4:7], s56 offen offset:1024 sc1\n"
"v_mfma_f32_16x16x32_bf16 v[84:87], a[252:255], v[212:215], v[84:87]\n"
"ds_read_b128 a[252:255], v12 offset:51200\n"
"v_mul_f32 v126, v104, v104\n"
"v_mfma_f32_16x16x32_bf16 v[80:83], a[248:251], v[212:215], v[80:83]\n"
"v_mul_f32 v127, v105, v105\n"
"v_mul_f32 v134, v106, v106\n"
"s_waitcnt lgkmcnt(3)\n"
"v_mfma_f32_16x16x32_bf16 v[76:79], a[244:247], v[220:223], v[76:79]\n"
"ds_read_b128 a[244:247], v12 offset:52224\n"
"v_mul_f32 v135, v107, v107\n"
"v_mfma_f32_16x16x32_bf16 v[84:87], a[36:39], v[216:219], v[84:87]\n"
"buffer_load_dwordx4 a[36:39], v1, s[4:7], s57 offen offset:2048 sc1\n"
"v_mfma_f32_16x16x32_bf16 v[80:83], a[240:243], v[216:219], v[80:83]\n"
"v_mul_f32 v136, v108, v108\n"
"v_mul_f32 v137, v109, v109\n"
"v_mfma_f32_16x16x32_bf16 v[84:87], a[132:135], v[220:223], v[84:87]\n"
"v_fma_f32 v114, v114, v138, v142\n"
"v_fma_f32 v115, v115, v138, v142\n"
"v_mfma_f32_16x16x32_bf16 v[80:83], a[136:139], v[220:223], v[80:83]\n"
"v_fma_f32 v132, v132, v138, v142\n"
"v_fma_f32 v133, v133, v138, v142\n"
"s_waitcnt lgkmcnt(3)\n"
"v_mfma_f32_16x16x32_bf16 v[84:87], a[76:79], v[236:239], v[84:87]\n"
"buffer_load_dwordx4 a[76:79], v1, s[4:7], s56 offen offset:2048 sc1\n"
"v_mfma_f32_16x16x32_bf16 v[76:79], a[64:67], v[236:239], v[76:79]\n"
"v_fma_f32 v124, v124, v138, v142\n"
"v_fma_f32 v125, v125, v138, v142\n"
"v_mfma_f32_16x16x32_bf16 v[80:83], a[80:83], v[236:239], v[80:83]\n"
"v_fma_f32 v126, v126, v138, v142\n"
"v_fma_f32 v127, v127, v138, v142\n"
"s_waitcnt lgkmcnt(2)\n"
"v_mfma_f32_16x16x32_bf16 v[84:87], a[88:91], v[244:247], v[84:87]\n"
"v_fma_f32 v134, v134, v138, v142\n"
"v_fma_f32 v135, v135, v138, v142\n"
"v_mfma_f32_16x16x32_bf16 v[76:79], a[44:47], v[244:247], v[76:79]\n"
"buffer_load_dwordx4 a[80:83], v1, s[4:7], s57 offen offset:3072 sc1\n"
"v_mfma_f32_16x16x32_bf16 v[80:83], a[96:99], v[244:247], v[80:83]\n"
"v_fma_f32 v136, v136, v138, v142\n"
"v_fma_f32 v137, v137, v138, v142\n"
"s_waitcnt lgkmcnt(1)\n"
"v_mfma_f32_16x16x32_bf16 v[84:87], a[104:107], a[252:255], v[84:87]\n"
"v_mul_f32 v114, v114, v42\n"
"v_mul_f32 v115, v115, v43\n"
"v_mfma_f32_16x16x32_bf16 v[76:79], a[112:115], a[252:255], v[76:79]\n"
"v_mul_f32 v132, v132, v100\n"
"v_mul_f32 v133, v133, v101\n"
"v_mfma_f32_16x16x32_bf16 v[80:83], a[52:55], a[252:255], v[80:83]\n"
"buffer_load_dwordx4 a[96:99], v1, s[4:7], s56 offen offset:3072 sc1\n"
"s_waitcnt lgkmcnt(0)\n"
"v_mfma_f32_16x16x32_bf16 v[84:87], a[120:123], a[244:247], v[84:87]\n"
"v_mul_f32 v124, v124, v102\n"
"v_mul_f32 v125, v125, v103\n"
"v_mfma_f32_16x16x32_bf16 v[76:79], a[60:63], a[244:247], v[76:79]\n"
"v_mul_f32 v126, v126, v104\n"
"v_mul_f32 v127, v127, v105\n"
"v_mfma_f32_16x16x32_bf16 v[80:83], a[68:71], a[244:247], v[80:83]\n"
"v_mul_f32 v134, v134, v106\n"
"v_mul_f32 v135, v135, v107\n"
"v_fma_f32 v138, v116, v146, v84\n"
"buffer_load_dwordx4 a[88:91], v2, s[4:7], s57 offen sc1\n"
"v_fma_f32 v139, v117, v146, v85\n"
"v_fma_f32 v142, v118, v146, v86\n"
"v_fma_f32 v143, v119, v146, v87\n"
"v_fma_f32 v116, v128, v146, v76\n"
"v_fma_f32 v117, v129, v146, v77\n"
"v_fma_f32 v118, v130, v146, v78\n"
"v_fma_f32 v119, v131, v146, v79\n"
"v_fma_f32 v128, v120, v146, v80\n"
"v_fma_f32 v129, v121, v146, v81\n"
"v_fma_f32 v130, v122, v146, v82\n"
"v_fma_f32 v131, v123, v146, v83\n"
"v_mul_f32 v150, v138, v138\n"
"v_mul_f32 v151, v139, v139\n"
"buffer_load_dwordx4 a[112:115], v2, s[4:7], s56 offen sc1\n"
"v_mul_f32 v152, v142, v142\n"
"v_mul_f32 v153, v143, v143\n"
"v_mul_f32 v146, v116, v116\n"
"v_mul_f32 v147, v117, v117\n"
"v_mul_f32 v148, v118, v118\n"
"v_mul_f32 v149, v119, v119\n"
"v_mul_f32 v120, v128, v128\n"
"v_mul_f32 v121, v129, v129\n"
"v_mul_f32 v122, v130, v130\n"
"v_mul_f32 v123, v131, v131\n"
"v_fma_f32 v150, v150, v140, v144\n"
"v_fma_f32 v151, v151, v140, v144\n"
"v_fma_f32 v152, v152, v140, v144\n"
"buffer_load_dwordx4 a[44:47], v2, s[4:7], s57 offen offset:1024 sc1\n"
"v_fma_f32 v153, v153, v140, v144\n"
"v_fma_f32 v146, v146, v140, v144\n"
"v_fma_f32 v147, v147, v140, v144\n"
"v_fma_f32 v148, v148, v140, v144\n"
"v_fma_f32 v149, v149, v140, v144\n"
"v_fma_f32 v120, v120, v140, v144\n"
"v_fma_f32 v121, v121, v140, v144\n"
"v_fma_f32 v122, v122, v140, v144\n"
"v_fma_f32 v123, v123, v140, v144\n"
"v_mul_f32 v150, v150, v138\n"
"v_mul_f32 v151, v151, v139\n"
"v_mul_f32 v152, v152, v142\n"
"v_mul_f32 v153, v153, v143\n"
"buffer_load_dwordx4 a[104:107], v2, s[4:7], s57 offen offset:2048 sc1\n"
"v_mul_f32 v136, v136, v108\n"
"v_mul_f32 v137, v137, v109\n"
"v_mul_f32 v146, v146, v116\n"
"v_mul_f32 v147, v147, v117\n"
"v_mul_f32 v148, v148, v118\n"
"v_mul_f32 v149, v149, v119\n"
"v_mul_f32 v120, v120, v128\n"
"v_mul_f32 v121, v121, v129\n"
"v_mul_f32 v122, v122, v130\n"
"v_mul_f32 v123, v123, v131\n"
"v_exp_f32 v114, v114\n"
"v_exp_f32 v115, v115\n"
"buffer_load_dwordx4 a[52:55], v2, s[4:7], s57 offen offset:3072 sc1\n"
"v_exp_f32 v132, v132\n"
"v_exp_f32 v133, v133\n"
"v_exp_f32 v150, v150\n"
"v_exp_f32 v151, v151\n"
"v_exp_f32 v152, v152\n"
"v_exp_f32 v153, v153\n"
"v_exp_f32 v124, v124\n"
"buffer_load_dwordx4 v[76:79], v1, s[4:7], s58 offen sc1\n"
"v_exp_f32 v125, v125\n"
"v_exp_f32 v126, v126\n"
"v_exp_f32 v127, v127\n"
"v_exp_f32 v134, v134\n"
"v_exp_f32 v135, v135\n"
"v_exp_f32 v136, v136\n"
"v_exp_f32 v137, v137\n"
"buffer_load_dwordx4 v[84:87], v1, s[4:7], s58 offen offset:1024 sc1\n"
"v_exp_f32 v146, v146\n"
"v_exp_f32 v147, v147\n"
"v_exp_f32 v148, v148\n"
"v_exp_f32 v149, v149\n"
"v_exp_f32 v120, v120\n"
"v_exp_f32 v121, v121\n"
"v_exp_f32 v122, v122\n"
"buffer_load_dwordx4 v[80:83], v1, s[4:7], s58 offen offset:2048 sc1\n"
"v_exp_f32 v123, v123\n"
"v_fma_f32 v114, v114, v112, v112\n"
"v_fma_f32 v115, v115, v112, v112\n"
"v_fma_f32 v132, v132, v112, v112\n"
"v_fma_f32 v133, v133, v112, v112\n"
"v_fma_f32 v150, v150, v110, v110\n"
"v_fma_f32 v151, v151, v110, v110\n"
"v_fma_f32 v152, v152, v110, v110\n"
"v_fma_f32 v153, v153, v110, v110\n"
"v_fma_f32 v124, v124, v112, v112\n"
"v_fma_f32 v125, v125, v112, v112\n"
"v_fma_f32 v126, v126, v112, v112\n"
"buffer_load_dwordx4 v[88:91], v1, s[4:7], s58 offen offset:3072 sc1\n"
"v_fma_f32 v127, v127, v112, v112\n"
"v_fma_f32 v134, v134, v112, v112\n"
"v_fma_f32 v135, v135, v112, v112\n"
"v_fma_f32 v136, v136, v112, v112\n"
"v_fma_f32 v137, v137, v112, v112\n"
"v_fma_f32 v146, v146, v110, v110\n"
"v_fma_f32 v147, v147, v110, v110\n"
"v_fma_f32 v148, v148, v110, v110\n"
"v_fma_f32 v149, v149, v110, v110\n"
"v_fma_f32 v120, v120, v110, v110\n"
"v_fma_f32 v121, v121, v110, v110\n"
"v_fma_f32 v122, v122, v110, v110\n"
"v_fma_f32 v123, v123, v110, v110\n"
"buffer_load_dwordx4 v[92:95], v7, s[4:7], s58 offen sc1\n"
"v_rcp_f32 v114, v114\n"
"v_rcp_f32 v115, v115\n"
"v_rcp_f32 v132, v132\n"
"v_rcp_f32 v133, v133\n"
"v_rcp_f32 v150, v150\n"
"v_rcp_f32 v151, v151\n"
"v_rcp_f32 v152, v152\n"
"buffer_load_dwordx4 v[96:99], v7, s[4:7], s58 offen offset:1024 sc1\n"
"v_rcp_f32 v153, v153\n"
"v_rcp_f32 v124, v124\n"
"v_rcp_f32 v125, v125\n"
"v_rcp_f32 v126, v126\n"
"v_rcp_f32 v127, v127\n"
"v_rcp_f32 v134, v134\n"
"v_rcp_f32 v135, v135\n"
"buffer_load_dwordx4 a[120:123], v2, s[4:7], s56 offen offset:1024 sc1\n"
"v_rcp_f32 v136, v136\n"
"v_rcp_f32 v137, v137\n"
"v_rcp_f32 v146, v146\n"
"v_rcp_f32 v147, v147\n"
"v_rcp_f32 v148, v148\n"
"v_rcp_f32 v149, v149\n"
"v_rcp_f32 v120, v120\n"
"buffer_load_dwordx4 a[60:63], v2, s[4:7], s56 offen offset:2048 sc1\n"
"v_rcp_f32 v121, v121\n"
"v_rcp_f32 v122, v122\n"
"v_rcp_f32 v123, v123\n"
"v_mul_f32 v42, v42, v114\n"
"v_mul_f32 v43, v43, v115\n"
"v_mul_f32 v100, v100, v132\n"
"v_mul_f32 v101, v101, v133\n"
"v_mul_f32 v138, v138, v150\n"
"v_mul_f32 v139, v139, v151\n"
"v_mul_f32 v142, v142, v152\n"
"buffer_load_dwordx4 a[68:71], v2, s[4:7], s56 offen offset:3072 sc1\n"
"v_mul_f32 v143, v143, v153\n"
"v_mul_f32 v102, v102, v124\n"
"v_mul_f32 v103, v103, v125\n"
"v_mul_f32 v104, v104, v126\n"
"v_mul_f32 v105, v105, v127\n"
"v_mul_f32 v106, v106, v134\n"
"v_mul_f32 v107, v107, v135\n"
"v_mul_f32 v108, v108, v136\n"
"v_mul_f32 v109, v109, v137\n"
"v_mul_f32 v116, v116, v146\n"
"v_mul_f32 v117, v117, v147\n"
"v_mul_f32 v118, v118, v148\n"
"v_mul_f32 v119, v119, v149\n"
"buffer_load_dwordx4 a[64:67], v1, s[16:19], s37 offen\n"
"v_mul_f32 v128, v128, v120\n"
"v_mul_f32 v129, v129, v121\n"
"v_mul_f32 v130, v130, v122\n"
"v_mul_f32 v131, v131, v123\n"
"v_cvt_pk_bf16_f32 v110, v42, v43\n"
"v_cvt_pk_bf16_f32 v111, v100, v101\n"
"v_cvt_pk_bf16_f32 v42, v138, v139\n"
"v_cvt_pk_bf16_f32 v43, v142, v143\n"
"v_cvt_pk_bf16_f32 v112, v102, v103\n"
"v_cvt_pk_bf16_f32 v113, v104, v105\n"
"v_cvt_pk_bf16_f32 v114, v106, v107\n"
"v_cvt_pk_bf16_f32 v115, v108, v109\n"
"v_cvt_pk_bf16_f32 v100, v116, v117\n"
"v_cvt_pk_bf16_f32 v101, v118, v119\n"
"buffer_store_dwordx4 v[112:115], v1, s[4:7], s32 offen sc1\n"
"v_cvt_pk_bf16_f32 v102, v128, v129\n"
"v_cvt_pk_bf16_f32 v103, v130, v131\n"
"v_cndmask_b32 v104, v110, v42, s[74:75]\n"
"v_cndmask_b32 v106, v42, v110, s[74:75]\n"
"buffer_store_dwordx4 v[100:103], v1, s[4:7], s3 offen sc1\n"
"v_cndmask_b32 v105, v111, v43, s[74:75]\n"
"v_cndmask_b32 v107, v43, v111, s[74:75]\n"
"s_waitcnt vmcnt(23)\n"
"v_mfma_f32_16x16x32_bf16 v[100:103], a[72:75], a[148:151], 0\n"
"buffer_store_dwordx4 v[104:107], v1, s[8:11], s2 offen sc1\n"
"s_nop 1\n"
"v_mfma_f32_16x16x32_bf16 v[104:107], a[72:75], a[152:155], 0\n"
"ds_read_b128 v[128:131], v28 offset:3072\n"
"v_add_f32 v62, v62, v70\n"
"buffer_load_dwordx4 a[72:75], v1, s[16:19], s37 offen offset:2048\n"
"v_mfma_f32_16x16x32_bf16 v[108:111], a[140:143], a[152:155], 0\n"
"v_add_f32 v63, v63, v71\n"
"v_add_f32 v64, v64, v72\n"
"v_mfma_f32_16x16x32_bf16 v[68:71], a[144:147], a[152:155], 0\n"
"v_add_f32 v65, v65, v73\n"
"v_add_f32 v66, v66, v74\n"
"v_mfma_f32_16x16x32_bf16 v[60:63], a[140:143], a[148:151], v[60:63]\n"
"v_add_f32 v67, v67, v75\n"
"ds_read_b128 v[72:75], v28 offset:6144\n"
"s_nop 0\n"
"v_mfma_f32_16x16x32_bf16 v[64:67], a[144:147], a[148:151], v[64:67]\n"
"ds_read_b128 v[124:127], v28 offset:7168\n"
"ds_read_b128 v[132:135], v28 offset:4096\n"
"s_waitcnt lgkmcnt(3)\n"
"v_mfma_f32_16x16x32_bf16 a[144:147], v[128:131], a[148:151], 0\n"
"ds_read_b128 v[144:147], v28 offset:5120\n"
"v_mfma_f32_16x16x32_bf16 a[148:151], v[128:131], a[152:155], 0\n"
"ds_read_b128 v[148:151], v28 offset:10240\n"
"ds_read_b128 v[120:123], v28 offset:8192\n"
"ds_read_b128 v[136:139], v28 offset:9216\n"
"s_waitcnt vmcnt(23) lgkmcnt(6)\n"
"v_mfma_f32_16x16x32_bf16 v[100:103], v[72:75], a[156:159], v[100:103]\n"
"ds_read_b128 v[140:143], v28 offset:11264\n"
"s_waitcnt lgkmcnt(6)\n"
"v_mfma_f32_16x16x32_bf16 a[144:147], v[124:127], a[156:159], a[144:147]\n"
"ds_read_b128 v[116:119], v28 offset:14336\n"
"s_waitcnt lgkmcnt(6)\n"
"v_mfma_f32_16x16x32_bf16 v[60:63], v[132:135], a[156:159], v[60:63]\n"
"ds_read_b128 v[112:115], v28 offset:15360\n"
"v_mfma_f32_16x16x32_bf16 v[104:107], v[72:75], a[160:163], v[104:107]\n"
"ds_read_b128 a[140:143], v28 offset:12288\n"
"s_waitcnt lgkmcnt(7)\n"
"v_mfma_f32_16x16x32_bf16 v[64:67], v[144:147], a[156:159], v[64:67]\n"
"ds_read_b128 v[72:75], v28 offset:13312\n"
"s_waitcnt vmcnt(21) lgkmcnt(7)\n"
"v_mfma_f32_16x16x32_bf16 v[100:103], v[148:151], a[76:79], v[100:103]\n"
"ds_read_b128 v[128:131], v28 offset:18432\n"
"s_waitcnt lgkmcnt(7)\n"
"v_mfma_f32_16x16x32_bf16 v[60:63], v[120:123], a[76:79], v[60:63]\n"
"ds_read_b128 a[152:155], v28 offset:16384\n"
"s_waitcnt lgkmcnt(7)\n"
"v_mfma_f32_16x16x32_bf16 v[64:67], v[136:139], a[76:79], v[64:67]\n"
"ds_read_b128 a[156:159], v28 offset:17408\n"
"v_mfma_f32_16x16x32_bf16 a[148:151], v[124:127], a[160:163], a[148:151]\n"
"v_mfma_f32_16x16x32_bf16 v[108:111], v[132:135], a[160:163], v[108:111]\n"
"v_mfma_f32_16x16x32_bf16 v[68:71], v[144:147], a[160:163], v[68:71]\n"
"v_mfma_f32_16x16x32_bf16 v[104:107], v[148:151], a[36:39], v[104:107]\n"
"s_waitcnt lgkmcnt(7)\n"
"v_mfma_f32_16x16x32_bf16 a[148:151], v[140:143], a[36:39], a[148:151]\n"
"v_mfma_f32_16x16x32_bf16 v[108:111], v[120:123], a[36:39], v[108:111]\n"
"v_mfma_f32_16x16x32_bf16 v[68:71], v[136:139], a[36:39], v[68:71]\n"
"v_mfma_f32_16x16x32_bf16 a[144:147], v[140:143], a[76:79], a[144:147]\n"
"s_waitcnt vmcnt(20) lgkmcnt(6)\n"
"v_mfma_f32_16x16x32_bf16 v[104:107], v[116:119], a[80:83], v[104:107]\n"
"s_waitcnt lgkmcnt(5)\n"
"v_mfma_f32_16x16x32_bf16 a[148:151], v[112:115], a[80:83], a[148:151]\n"
"s_waitcnt vmcnt(12)\n"
"v_swap_b32 v78, v84\n"
"s_waitcnt lgkmcnt(4)\n"
"v_mfma_f32_16x16x32_bf16 v[108:111], a[140:143], a[80:83], v[108:111]\n"
"v_swap_b32 v79, v85\n"
"s_waitcnt lgkmcnt(3)\n"
"v_mfma_f32_16x16x32_bf16 v[68:71], v[72:75], a[80:83], v[68:71]\n"
"v_mfma_f32_16x16x32_bf16 v[100:103], v[116:119], a[96:99], v[100:103]\n"
"v_mfma_f32_16x16x32_bf16 a[144:147], v[112:115], a[96:99], a[144:147]\n"
"s_waitcnt lgkmcnt(2)\n"
"v_mfma_f32_16x16x32_bf16 v[104:107], v[128:131], a[88:91], v[104:107]\n"
"v_mfma_f32_16x16x32_bf16 a[148:151], a[124:127], a[88:91], a[148:151]\n"
"s_waitcnt lgkmcnt(1)\n"
"v_mfma_f32_16x16x32_bf16 v[108:111], a[152:155], a[88:91], v[108:111]\n"
"s_waitcnt lgkmcnt(0)\n"
"v_mfma_f32_16x16x32_bf16 v[68:71], a[156:159], a[88:91], v[68:71]\n"
"v_mfma_f32_16x16x32_bf16 v[100:103], v[128:131], a[112:115], v[100:103]\n"
"v_mfma_f32_16x16x32_bf16 a[144:147], a[124:127], a[112:115], a[144:147]\n"
"s_waitcnt vmcnt(10)\n"
"v_swap_b32 v82, v88\n"
"v_swap_b32 v83, v89\n"
"v_mfma_f32_16x16x32_bf16 v[104:107], a[100:103], a[44:47], v[104:107]\n"
"v_mfma_f32_16x16x32_bf16 a[148:151], a[92:95], a[44:47], a[148:151]\n"
"v_mfma_f32_16x16x32_bf16 v[108:111], a[116:119], a[44:47], v[108:111]\n"
"v_mfma_f32_16x16x32_bf16 v[68:71], a[108:111], a[44:47], v[68:71]\n"
"v_mfma_f32_16x16x32_bf16 v[100:103], a[200:203], v[76:79], v[100:103]\n"
"v_mfma_f32_16x16x32_bf16 a[144:147], a[204:207], v[76:79], a[144:147]\n"
"v_mfma_f32_16x16x32_bf16 v[60:63], a[140:143], a[96:99], v[60:63]\n"
"v_mfma_f32_16x16x32_bf16 v[64:67], v[72:75], a[96:99], v[64:67]\n"
"v_mfma_f32_16x16x32_bf16 v[104:107], a[168:171], a[104:107], v[104:107]\n"
"s_waitcnt vmcnt(8)\n"
"v_swap_b32 v94, v96\n"
"v_mfma_f32_16x16x32_bf16 a[148:151], a[172:175], a[104:107], a[148:151]\n"
"v_swap_b32 v95, v97\n"
"v_mfma_f32_16x16x32_bf16 v[108:111], a[84:87], a[104:107], v[108:111]\n"
"v_mfma_f32_16x16x32_bf16 v[68:71], a[164:167], a[104:107], v[68:71]\n"
"v_mfma_f32_16x16x32_bf16 v[100:103], a[216:219], v[80:83], v[100:103]\n"
"v_mfma_f32_16x16x32_bf16 a[144:147], a[220:223], v[80:83], a[144:147]\n"
"v_mfma_f32_16x16x32_bf16 v[60:63], a[152:155], a[112:115], v[60:63]\n"
"v_mfma_f32_16x16x32_bf16 v[64:67], a[156:159], a[112:115], v[64:67]\n"
"v_mfma_f32_16x16x32_bf16 v[104:107], a[184:187], a[52:55], v[104:107]\n"
"v_mfma_f32_16x16x32_bf16 a[148:151], a[188:191], a[52:55], a[148:151]\n"
"v_mfma_f32_16x16x32_bf16 v[108:111], a[176:179], a[52:55], v[108:111]\n"
"v_mfma_f32_16x16x32_bf16 v[68:71], a[180:183], a[52:55], v[68:71]\n"
"v_mfma_f32_16x16x32_bf16 v[100:103], a[232:235], v[92:95], v[100:103]\n"
"v_mfma_f32_16x16x32_bf16 a[144:147], a[236:239], v[92:95], a[144:147]\n"
"v_mfma_f32_16x16x32_bf16 v[60:63], a[192:195], v[76:79], v[60:63]\n"
"v_mfma_f32_16x16x32_bf16 v[64:67], a[196:199], v[76:79], v[64:67]\n"
"v_mfma_f32_16x16x32_bf16 v[104:107], a[200:203], v[84:87], v[104:107]\n"
"v_mfma_f32_16x16x32_bf16 a[148:151], a[204:207], v[84:87], a[148:151]\n"
"v_mfma_f32_16x16x32_bf16 v[108:111], a[192:195], v[84:87], v[108:111]\n"
"v_mfma_f32_16x16x32_bf16 v[68:71], a[196:199], v[84:87], v[68:71]\n"
"s_waitcnt vmcnt(7)\n"
"v_mfma_f32_16x16x32_bf16 v[100:103], a[100:103], a[120:123], v[100:103]\n"
"v_mfma_f32_16x16x32_bf16 a[144:147], a[92:95], a[120:123], a[144:147]\n"
"v_mfma_f32_16x16x32_bf16 v[60:63], a[208:211], v[80:83], v[60:63]\n"
"v_mfma_f32_16x16x32_bf16 v[64:67], a[212:215], v[80:83], v[64:67]\n"
"v_mfma_f32_16x16x32_bf16 v[104:107], a[216:219], v[88:91], v[104:107]\n"
"v_mfma_f32_16x16x32_bf16 a[148:151], a[220:223], v[88:91], a[148:151]\n"
"v_mfma_f32_16x16x32_bf16 v[108:111], a[208:211], v[88:91], v[108:111]\n"
"v_mfma_f32_16x16x32_bf16 v[68:71], a[212:215], v[88:91], v[68:71]\n"
"s_waitcnt vmcnt(6)\n"
"v_mfma_f32_16x16x32_bf16 v[100:103], a[168:171], a[60:63], v[100:103]\n"
"v_mfma_f32_16x16x32_bf16 a[144:147], a[172:175], a[60:63], a[144:147]\n"
"v_mfma_f32_16x16x32_bf16 v[60:63], a[224:227], v[92:95], v[60:63]\n"
"v_mfma_f32_16x16x32_bf16 v[64:67], a[228:231], v[92:95], v[64:67]\n"
"v_mfma_f32_16x16x32_bf16 v[104:107], a[232:235], v[96:99], v[104:107]\n"
"v_mfma_f32_16x16x32_bf16 a[148:151], a[236:239], v[96:99], a[148:151]\n"
"v_mfma_f32_16x16x32_bf16 v[108:111], a[224:227], v[96:99], v[108:111]\n"
"v_mfma_f32_16x16x32_bf16 v[68:71], a[228:231], v[96:99], v[68:71]\n"
"s_waitcnt vmcnt(5)\n"
"v_mfma_f32_16x16x32_bf16 v[100:103], a[184:187], a[68:71], v[100:103]\n"
"v_mfma_f32_16x16x32_bf16 a[144:147], a[188:191], a[68:71], a[144:147]\n"
"v_mfma_f32_16x16x32_bf16 v[60:63], a[116:119], a[120:123], v[60:63]\n"
"v_mfma_f32_16x16x32_bf16 v[64:67], a[108:111], a[120:123], v[64:67]\n"
"ds_write_b128 v15, v[104:107]\n"
"v_mfma_f32_16x16x32_bf16 v[60:63], a[84:87], a[60:63], v[60:63]\n"
"ds_write_b128 v15, a[148:151] offset:1024\n"
"v_mfma_f32_16x16x32_bf16 v[64:67], a[164:167], a[60:63], v[64:67]\n"
"ds_write_b128 v14, v[108:111]\n"
"v_mfma_f32_16x16x32_bf16 v[60:63], a[176:179], a[68:71], v[60:63]\n"
"ds_write_b128 v14, v[68:71] offset:1024\n"
"v_mfma_f32_16x16x32_bf16 v[64:67], a[180:183], a[68:71], v[64:67]\n"
"ds_write_b128 v13, v[100:103]\n"
"v_cmp_u_f32 vcc, v104, v100\n"
"ds_write_b128 v13, a[144:147] offset:1024\n"
"s_sleep 1\n"
"buffer_load_dwordx4 v[104:107], v1, s[4:7], s0 offen sc1\n"
"buffer_load_dwordx4 a[76:79], v1, s[4:7], s1 offen sc1\n"
"buffer_load_dwordx4 v[112:115], v1, s[4:7], s0 offen offset:1024 sc1\n"
"buffer_load_dwordx4 v[108:111], v1, s[4:7], s1 offen offset:1024 sc1\n"
"buffer_load_dwordx4 v[100:103], v1, s[4:7], s0 offen offset:2048 sc1\n"
"buffer_load_dwordx4 v[152:155], v1, s[4:7], s1 offen offset:2048 sc1\n"
"buffer_load_dwordx4 v[124:127], v1, s[4:7], s0 offen offset:3072 sc1\n"
"buffer_load_dwordx4 v[148:151], v1, s[4:7], s1 offen offset:3072 sc1\n"
"s_cbranch_vccnz Rw4A%=\n"
"Cw4A%=:\n"
"s_waitcnt lgkmcnt(0)\n"
"s_barrier\n"
"ds_read_b128 v[72:75], v16\n"
"s_add_u32 s34, s57, 0x400000\n"
"s_add_u32 s3, s56, 0x400000\n"
"s_add_u32 s32, s58, 0x100000\n"
"ds_read_b128 v[48:51], v16 offset:2048\n"
"s_add_u32 s35, s52, 0x1000000\n"
"s_add_u32 s33, s53, 0x1100000\n"
"s_add_u32 s2, s48, 0x1000000\n"
"ds_read_b128 v[84:87], v16 offset:4096\n"
"s_add_u32 s0, s48, 0x1008000\n"
"buffer_load_dwordx4 v[132:135], v2, s[4:7], s34 offen sc1\n"
"ds_read_b128 v[80:83], v16 offset:1024\n"
"s_add_u32 s1, s51, 0x1100000\n"
"v_lshlrev_b32 v56, 16, v44\n"
"v_and_b32 v57, s72, v44\n"
"ds_read_b128 v[76:79], v16 offset:3072\n"
"v_lshlrev_b32 v58, 16, v45\n"
"v_and_b32 v59, s72, v45\n"
"v_lshlrev_b32 v68, 16, v46\n"
"ds_read_b128 v[88:91], v16 offset:5120\n"
"v_and_b32 v69, s72, v46\n"
"v_lshlrev_b32 v70, 16, v47\n"
"v_and_b32 v71, s72, v47\n"
"ds_read_b128 a[52:55], v28 offset:2048\n"
"buffer_load_dwordx4 v[136:139], v2, s[4:7], s3 offen sc1\n"
"s_waitcnt lgkmcnt(6)\n"
"v_pk_add_f32 v[60:61], v[60:61], v[72:73]\n"
"v_pk_add_f32 v[62:63], v[62:63], v[74:75]\n"
"ds_read_b128 a[68:71], v28\n"
"v_lshlrev_b32 v72, 16, v52\n"
"v_and_b32 v73, s72, v52\n"
"v_lshlrev_b32 v74, 16, v53\n"
"s_waitcnt lgkmcnt(5)\n"
"v_pk_add_f32 v[48:49], v[48:49], v[84:85]\n"
"v_pk_add_f32 v[50:51], v[50:51], v[86:87]\n"
"v_pk_add_f32 v[60:61], v[60:61], v[48:49]\n"
"v_pk_add_f32 v[62:63], v[62:63], v[50:51]\n"
"v_cvt_pk_bf16_f32 v48, v60, v61\n"
"s_waitcnt lgkmcnt(4)\n"
"v_pk_add_f32 v[64:65], v[64:65], v[80:81]\n"
"buffer_load_dwordx4 v[144:147], v2, s[4:7], s34 offen offset:1024 sc1\n"
"v_pk_add_f32 v[66:67], v[66:67], v[82:83]\n"
"v_cvt_pk_bf16_f32 v49, v62, v63\n"
"v_pk_add_f32 v[42:43], v[60:61], v[62:63]\n"
"s_waitcnt lgkmcnt(2)\n"
"v_pk_add_f32 v[76:77], v[76:77], v[88:89]\n"
"v_pk_add_f32 v[78:79], v[78:79], v[90:91]\n"
"v_pk_add_f32 v[64:65], v[64:65], v[76:77]\n"
"v_pk_add_f32 v[66:67], v[66:67], v[78:79]\n"
"v_cvt_pk_bf16_f32 v50, v64, v65\n"
"v_cvt_pk_bf16_f32 v51, v66, v67\n"
"v_pk_add_f32 v[76:77], v[64:65], v[66:67]\n"
"v_pk_add_f32 v[42:43], v[42:43], v[76:77]\n"
"v_add_f32 v0, v42, v43\n"
"buffer_store_dwordx4 v[48:51], v1, s[16:19], s35 offen sc1\n"
"v_mov_b32 v40, v0\n"
"ds_read_b128 a[44:47], v28 offset:1024\n"
"v_and_b32 v75, s72, v53\n"
"v_permlane16_swap_b32 v0, v40\n"
"buffer_load_dwordx4 v[120:123], v2, s[4:7], s34 offen offset:2048 sc1\n"
"v_add_f32 v42, v0, v40\n"
"v_mov_b32 v0, v42\n"
"ds_read_b128 a[96:99], v28 offset:3072\n"
"v_lshlrev_b32 v76, 16, v54\n"
"v_permlane32_swap_b32 v42, v0\n"
"v_add_f32 v80, v42, v0\n"
"v_mul_f32 v42, 0x3d000000, v80\n"
"v_mul_f32 v80, 0xbd000000, v80\n"
"v_pk_add_f32 v[82:83], v[60:61], v[80:81] op_sel_hi:[1,0]\n"
"v_pk_add_f32 v[86:87], v[64:65], v[80:81] op_sel_hi:[1,0]\n"
"v_pk_add_f32 v[84:85], v[62:63], v[80:81] op_sel_hi:[1,0]\n"
"v_pk_add_f32 v[60:61], v[66:67], v[80:81] op_sel_hi:[1,0]\n"
"buffer_load_dwordx4 v[140:143], v2, s[4:7], s34 offen offset:3072 sc1\n"
"v_pk_mul_f32 v[62:63], v[82:83], v[82:83]\n"
"v_pk_mul_f32 v[64:65], v[86:87], v[86:87]\n"
"v_pk_fma_f32 v[62:63], v[84:85], v[84:85], v[62:63]\n"
"v_pk_fma_f32 v[64:65], v[60:61], v[60:61], v[64:65]\n"
"v_pk_add_f32 v[62:63], v[62:63], v[64:65]\n"
"v_add_f32 v0, v62, v63\n"
"v_mov_b32 v60, v0\n"
"v_and_b32 v77, s72, v54\n"
"v_lshlrev_b32 v78, 16, v55\n"
"v_permlane16_swap_b32 v0, v60\n"
"v_add_f32 v40, v0, v60\n"
"v_mov_b32 v0, v40\n"
"buffer_load_dwordx4 v[84:87], v1, s[4:7], s32 offen sc1\n"
"v_and_b32 v79, s72, v55\n"
"v_permlane32_swap_b32 v40, v0\n"
"v_add_f32 v43, v40, v0\n"
"v_add_f32 v56, v56, v72\n"
"v_add_f32 v57, v57, v73\n"
"v_add_f32 v58, v58, v74\n"
"v_add_f32 v59, v59, v75\n"
"v_add_f32 v68, v68, v76\n"
"v_add_f32 v69, v69, v77\n"
"v_add_f32 v70, v70, v78\n"
"v_add_f32 v71, v71, v79\n"
"ds_read_b128 a[104:107], v28 offset:6144\n"
"buffer_store_dwordx2 v[42:43], v5, s[16:19], s33 offen sc1\n"
"ds_read_b128 a[112:115], v28 offset:7168\n"
"buffer_load_dwordx4 v[88:91], v1, s[4:7], s32 offen offset:1024 sc1\n"
"ds_read_b128 a[120:123], v28 offset:4096\n"
"ds_read_b128 a[60:63], v28 offset:5120\n"
"s_waitcnt vmcnt(16) lgkmcnt(7)\n"
"v_mfma_f32_16x16x32_bf16 v[60:63], a[52:55], v[104:107], 0\n"
"s_waitcnt lgkmcnt(4)\n"
"v_mfma_f32_16x16x32_bf16 v[116:119], a[96:99], v[104:107], 0\n"
"buffer_load_dwordx4 v[80:83], v1, s[4:7], s32 offen offset:2048 sc1\n"
"v_mfma_f32_16x16x32_bf16 v[128:131], a[68:71], v[104:107], 0\n"
"v_mfma_f32_16x16x32_bf16 v[96:99], a[44:47], v[104:107], 0\n"
"s_waitcnt vmcnt(16)\n"
"v_mfma_f32_16x16x32_bf16 v[92:95], a[52:55], a[76:79], 0\n"
"ds_read_b128 a[52:55], v28 offset:10240\n"
"buffer_load_dwordx4 v[76:79], v1, s[4:7], s32 offen offset:3072 sc1\n"
"v_mfma_f32_16x16x32_bf16 v[56:59], a[68:71], a[76:79], v[56:59]\n"
"ds_read_b128 a[68:71], v28 offset:8192\n"
"v_mfma_f32_16x16x32_bf16 v[68:71], a[44:47], a[76:79], v[68:71]\n"
"ds_read_b128 a[44:47], v28 offset:9216\n"
"v_mfma_f32_16x16x32_bf16 v[104:107], a[96:99], a[76:79], 0\n"
"ds_read_b128 a[96:99], v28 offset:11264\n"
"s_waitcnt vmcnt(16) lgkmcnt(7)\n"
"v_mfma_f32_16x16x32_bf16 v[60:63], a[104:107], v[112:115], v[60:63]\n"
"buffer_load_dwordx4 v[72:75], v7, s[4:7], s32 offen sc1\n"
"s_waitcnt lgkmcnt(6)\n"
"v_mfma_f32_16x16x32_bf16 v[116:119], a[112:115], v[112:115], v[116:119]\n"
"s_waitcnt lgkmcnt(5)\n"
"v_mfma_f32_16x16x32_bf16 v[128:131], a[120:123], v[112:115], v[128:131]\n"
"s_waitcnt lgkmcnt(4)\n"
"v_mfma_f32_16x16x32_bf16 v[96:99], a[60:63], v[112:115], v[96:99]\n"
"buffer_load_dwordx4 v[64:67], v7, s[4:7], s32 offen offset:1024 sc1\n"
"s_waitcnt vmcnt(17)\n"
"v_mfma_f32_16x16x32_bf16 v[92:95], a[104:107], v[108:111], v[92:95]\n"
"ds_read_b128 a[104:107], v28 offset:14336\n"
"v_mfma_f32_16x16x32_bf16 v[104:107], a[112:115], v[108:111], v[104:107]\n"
"ds_read_b128 a[112:115], v28 offset:15360\n"
"v_mfma_f32_16x16x32_bf16 v[56:59], a[120:123], v[108:111], v[56:59]\n"
"ds_read_b128 a[120:123], v28 offset:12288\n"
"v_mfma_f32_16x16x32_bf16 v[68:71], a[60:63], v[108:111], v[68:71]\n"
"buffer_load_dwordx4 v[112:115], v2, s[4:7], s3 offen offset:1024 sc1\n"
"s_waitcnt vmcnt(17) lgkmcnt(6)\n"
"v_mfma_f32_16x16x32_bf16 v[60:63], a[52:55], v[100:103], v[60:63]\n"
"ds_read_b128 a[60:63], v28 offset:13312\n"
"s_waitcnt lgkmcnt(4)\n"
"v_mfma_f32_16x16x32_bf16 v[116:119], a[96:99], v[100:103], v[116:119]\n"
"v_mfma_f32_16x16x32_bf16 v[128:131], a[68:71], v[100:103], v[128:131]\n"
"v_mfma_f32_16x16x32_bf16 v[96:99], a[44:47], v[100:103], v[96:99]\n"
"buffer_load_dwordx4 v[108:111], v2, s[4:7], s3 offen offset:2048 sc1\n"
"s_waitcnt vmcnt(17)\n"
"v_mfma_f32_16x16x32_bf16 v[92:95], a[52:55], v[152:155], v[92:95]\n"
"ds_read_b128 a[52:55], v28 offset:18432\n"
"v_mfma_f32_16x16x32_bf16 v[56:59], a[68:71], v[152:155], v[56:59]\n"
"ds_read_b128 a[68:71], v28 offset:16384\n"
"v_mfma_f32_16x16x32_bf16 v[68:71], a[44:47], v[152:155], v[68:71]\n"
"ds_read_b128 a[76:79], v28 offset:17408\n"
"v_mfma_f32_16x16x32_bf16 v[104:107], a[96:99], v[152:155], v[104:107]\n"
"buffer_load_dwordx4 v[100:103], v2, s[4:7], s3 offen offset:3072 sc1\n"
"s_waitcnt vmcnt(17) lgkmcnt(6)\n"
"v_mfma_f32_16x16x32_bf16 v[60:63], a[104:107], v[124:127], v[60:63]\n"
"s_waitcnt lgkmcnt(5)\n"
"v_mfma_f32_16x16x32_bf16 v[116:119], a[112:115], v[124:127], v[116:119]\n"
"s_waitcnt lgkmcnt(4)\n"
"v_mfma_f32_16x16x32_bf16 v[128:131], a[120:123], v[124:127], v[128:131]\n"
"buffer_store_dwordx4 v[48:51], v35, s[28:31], s67 offen\n"
"buffer_load_dwordx4 a[80:83], v2, s[16:19], s37 offen\n"
"s_waitcnt lgkmcnt(3)\n"
"v_mfma_f32_16x16x32_bf16 v[96:99], a[60:63], v[124:127], v[96:99]\n"
"s_waitcnt vmcnt(18)\n"
"v_mfma_f32_16x16x32_bf16 v[92:95], a[104:107], v[148:151], v[92:95]\n"
"v_mfma_f32_16x16x32_bf16 v[104:107], a[112:115], v[148:151], v[104:107]\n"
"s_waitcnt vmcnt(17) lgkmcnt(2)\n"
"v_mfma_f32_16x16x32_bf16 v[60:63], a[52:55], v[132:135], v[60:63]\n"
"buffer_load_dwordx4 a[88:91], v2, s[16:19], s37 offen offset:2048\n"
"v_mfma_f32_16x16x32_bf16 v[116:119], a[124:127], v[132:135], v[116:119]\n"
"s_waitcnt lgkmcnt(1)\n"
"v_mfma_f32_16x16x32_bf16 v[128:131], a[68:71], v[132:135], v[128:131]\n"
"s_waitcnt lgkmcnt(0)\n"
"v_mfma_f32_16x16x32_bf16 v[96:99], a[76:79], v[132:135], v[96:99]\n"
"s_waitcnt vmcnt(17)\n"
"v_mfma_f32_16x16x32_bf16 v[92:95], a[52:55], v[136:139], v[92:95]\n"
"buffer_load_dwordx4 a[96:99], v3, s[16:19], s37 offen\n"
"v_mfma_f32_16x16x32_bf16 v[104:107], a[124:127], v[136:139], v[104:107]\n"
"s_waitcnt vmcnt(17)\n"
"v_mfma_f32_16x16x32_bf16 v[60:63], a[100:103], v[144:147], v[60:63]\n"
"v_mfma_f32_16x16x32_bf16 v[116:119], a[92:95], v[144:147], v[116:119]\n"
"v_mfma_f32_16x16x32_bf16 v[128:131], a[116:119], v[144:147], v[128:131]\n"
"buffer_load_dwordx4 a[104:107], v3, s[16:19], s37 offen offset:2048\n"
"v_mfma_f32_16x16x32_bf16 v[96:99], a[108:111], v[144:147], v[96:99]\n"
"v_mfma_f32_16x16x32_bf16 v[56:59], a[120:123], v[148:151], v[56:59]\n"
"v_mfma_f32_16x16x32_bf16 v[68:71], a[60:63], v[148:151], v[68:71]\n"
"s_waitcnt vmcnt(16)\n"
"v_mfma_f32_16x16x32_bf16 v[60:63], a[168:171], v[120:123], v[60:63]\n"
"buffer_load_dwordx4 a[112:115], v4, s[16:19], s37 offen\n"
"v_mfma_f32_16x16x32_bf16 v[116:119], a[172:175], v[120:123], v[116:119]\n"
"v_mfma_f32_16x16x32_bf16 v[128:131], a[84:87], v[120:123], v[128:131]\n"
"v_mfma_f32_16x16x32_bf16 v[96:99], a[164:167], v[120:123], v[96:99]\n"
"v_mfma_f32_16x16x32_bf16 v[56:59], a[68:71], v[136:139], v[56:59]\n"
"buffer_load_dwordx4 a[120:123], v4, s[16:19], s37 offen offset:2048\n"
"v_mfma_f32_16x16x32_bf16 v[68:71], a[76:79], v[136:139], v[68:71]\n"
"s_waitcnt vmcnt(17)\n"
"v_mfma_f32_16x16x32_bf16 v[60:63], a[184:187], v[140:143], v[60:63]\n"
"v_mfma_f32_16x16x32_bf16 v[116:119], a[188:191], v[140:143], v[116:119]\n"
"v_mfma_f32_16x16x32_bf16 v[128:131], a[176:179], v[140:143], v[128:131]\n"
"buffer_load_dwordx4 a[36:39], v3, s[16:19], s36 offen offset:1024\n"
"v_mfma_f32_16x16x32_bf16 v[96:99], a[180:183], v[140:143], v[96:99]\n"
"buffer_load_dwordx4 a[44:47], v3, s[16:19], s36 offen offset:3072\n"
"s_waitcnt vmcnt(16)\n"
"v_swap_b32 v86, v88\n"
"v_swap_b32 v87, v89\n"
"s_nop 1\n"
"v_mfma_f32_16x16x32_bf16 v[92:95], a[200:203], v[84:87], v[92:95]\n"
"buffer_load_dwordx4 a[52:55], v4, s[16:19], s36 offen offset:1024\n"
"v_mfma_f32_16x16x32_bf16 v[104:107], a[204:207], v[84:87], v[104:107]\n"
"v_mfma_f32_16x16x32_bf16 v[56:59], a[192:195], v[84:87], v[56:59]\n"
"v_mfma_f32_16x16x32_bf16 v[68:71], a[196:199], v[84:87], v[68:71]\n"
"v_mfma_f32_16x16x32_bf16 v[60:63], a[200:203], v[88:91], v[60:63]\n"
"buffer_load_dwordx4 a[60:63], v4, s[16:19], s36 offen offset:3072\n"
"v_mfma_f32_16x16x32_bf16 v[116:119], a[204:207], v[88:91], v[116:119]\n"
"v_mfma_f32_16x16x32_bf16 v[128:131], a[192:195], v[88:91], v[128:131]\n"
"v_mfma_f32_16x16x32_bf16 v[96:99], a[196:199], v[88:91], v[96:99]\n"
"s_waitcnt vmcnt(16)\n"
"v_swap_b32 v82, v76\n"
"v_swap_b32 v83, v77\n"
"buffer_load_dwordx4 a[68:71], v1, s[16:19], s37 offen offset:1024\n"
"s_nop 0\n"
"v_mfma_f32_16x16x32_bf16 v[92:95], a[216:219], v[80:83], v[92:95]\n"
"v_mfma_f32_16x16x32_bf16 v[104:107], a[220:223], v[80:83], v[104:107]\n"
"v_mfma_f32_16x16x32_bf16 v[56:59], a[208:211], v[80:83], v[56:59]\n"
"v_mfma_f32_16x16x32_bf16 v[68:71], a[212:215], v[80:83], v[68:71]\n"
"buffer_load_dwordx4 a[76:79], v1, s[16:19], s37 offen offset:3072\n"
"v_mfma_f32_16x16x32_bf16 v[60:63], a[216:219], v[76:79], v[60:63]\n"
"v_mfma_f32_16x16x32_bf16 v[116:119], a[220:223], v[76:79], v[116:119]\n"
"v_mfma_f32_16x16x32_bf16 v[128:131], a[208:211], v[76:79], v[128:131]\n"
"v_mfma_f32_16x16x32_bf16 v[96:99], a[212:215], v[76:79], v[96:99]\n"
"s_waitcnt vmcnt(16)\n"
"v_swap_b32 v74, v64\n"
"v_swap_b32 v75, v65\n"
"s_nop 1\n"
"v_mfma_f32_16x16x32_bf16 v[92:95], a[232:235], v[72:75], v[92:95]\n"
"v_mfma_f32_16x16x32_bf16 v[104:107], a[236:239], v[72:75], v[104:107]\n"
"v_mfma_f32_16x16x32_bf16 v[56:59], a[224:227], v[72:75], v[56:59]\n"
"v_mfma_f32_16x16x32_bf16 v[68:71], a[228:231], v[72:75], v[68:71]\n"
"buffer_load_dwordx4 a[132:135], v1, s[16:19], s2 offen sc1\n"
"s_waitcnt vmcnt(16)\n"
"v_mfma_f32_16x16x32_bf16 v[92:95], a[100:103], v[112:115], v[92:95]\n"
"v_mfma_f32_16x16x32_bf16 v[104:107], a[92:95], v[112:115], v[104:107]\n"
"v_mfma_f32_16x16x32_bf16 v[60:63], a[232:235], v[64:67], v[60:63]\n"
"v_mfma_f32_16x16x32_bf16 v[116:119], a[236:239], v[64:67], v[116:119]\n"
"buffer_load_dwordx4 a[136:139], v1, s[16:19], s2 offen offset:1024 sc1\n"
"s_waitcnt vmcnt(16)\n"
"v_mfma_f32_16x16x32_bf16 v[92:95], a[168:171], v[108:111], v[92:95]\n"
"v_mfma_f32_16x16x32_bf16 v[104:107], a[172:175], v[108:111], v[104:107]\n"
"v_mfma_f32_16x16x32_bf16 v[128:131], a[224:227], v[64:67], v[128:131]\n"
"v_mfma_f32_16x16x32_bf16 v[96:99], a[228:231], v[64:67], v[96:99]\n"
"buffer_load_dwordx4 a[140:143], v1, s[16:19], s2 offen offset:2048 sc1\n"
"v_mfma_f32_16x16x32_bf16 v[56:59], a[116:119], v[112:115], v[56:59]\n"
"ds_write_b128 v15, v[60:63] offset:32768\n"
"s_waitcnt vmcnt(16)\n"
"v_mfma_f32_16x16x32_bf16 v[92:95], a[184:187], v[100:103], v[92:95]\n"
"ds_write_b128 v15, v[116:119] offset:33792\n"
"v_mfma_f32_16x16x32_bf16 v[104:107], a[188:191], v[100:103], v[104:107]\n"
"buffer_load_dwordx4 a[144:147], v1, s[16:19], s2 offen offset:3072 sc1\n"
"v_mfma_f32_16x16x32_bf16 v[68:71], a[108:111], v[112:115], v[68:71]\n"
"ds_write_b128 v14, v[128:131] offset:32768\n"
"v_mfma_f32_16x16x32_bf16 v[56:59], a[84:87], v[108:111], v[56:59]\n"
"ds_write_b128 v14, v[96:99] offset:33792\n"
"v_mfma_f32_16x16x32_bf16 v[68:71], a[164:167], v[108:111], v[68:71]\n"
"buffer_load_dwordx4 a[148:151], v1, s[16:19], s0 offen sc1\n"
"v_mfma_f32_16x16x32_bf16 v[56:59], a[176:179], v[100:103], v[56:59]\n"
"ds_write_b128 v13, v[92:95] offset:32768\n"
"v_mfma_f32_16x16x32_bf16 v[68:71], a[180:183], v[100:103], v[68:71]\n"
"ds_write_b128 v13, v[104:107] offset:33792\n"
"v_cmp_u_f32 vcc, v60, v92\n"
"buffer_load_dwordx4 a[152:155], v1, s[16:19], s0 offen offset:1024 sc1\n"
"buffer_load_dwordx4 a[156:159], v1, s[16:19], s0 offen offset:2048 sc1\n"
"buffer_load_dwordx4 a[160:163], v1, s[16:19], s0 offen offset:3072 sc1\n"
"buffer_load_dwordx4 v[60:63], v1, s[16:19], s1 offen sc1\n"
"s_cbranch_vccnz Rw4B%=\n"
"Cw4B%=:\n"
"s_waitcnt lgkmcnt(0)\n"
"s_barrier\n"
"ds_read_b128 v[52:55], v16 offset:32768\n"
"s_add_u32 s2, s52, 0x1400000\n"
"s_add_u32 s0, s53, 0x1500000\n"
"s_add_u32 s1, s67, 0x100000\n"
"ds_read_b128 v[72:75], v16 offset:34816\n"
"ds_read_b128 v[80:83], v16 offset:36864\n"
"ds_read_b128 v[76:79], v16 offset:33792\n"
"ds_read_b128 v[44:47], v16 offset:35840\n"
"ds_read_b128 v[64:67], v16 offset:37888\n"
"s_waitcnt lgkmcnt(5)\n"
"v_pk_add_f32 v[56:57], v[56:57], v[52:53]\n"
"v_pk_add_f32 v[58:59], v[58:59], v[54:55]\n"
"s_waitcnt lgkmcnt(3)\n"
"v_pk_add_f32 v[72:73], v[72:73], v[80:81]\n"
"v_pk_add_f32 v[74:75], v[74:75], v[82:83]\n"
"v_pk_add_f32 v[56:57], v[56:57], v[72:73]\n"
"v_pk_add_f32 v[58:59], v[58:59], v[74:75]\n"
"s_waitcnt lgkmcnt(2)\n"
"v_pk_add_f32 v[68:69], v[68:69], v[76:77]\n"
"v_pk_add_f32 v[70:71], v[70:71], v[78:79]\n"
"v_pk_add_f32 v[42:43], v[56:57], v[58:59]\n"
"v_cvt_pk_bf16_f32 v52, v56, v57\n"
"v_cvt_pk_bf16_f32 v53, v58, v59\n"
"s_waitcnt lgkmcnt(0)\n"
"v_pk_add_f32 v[44:45], v[44:45], v[64:65]\n"
"v_pk_add_f32 v[46:47], v[46:47], v[66:67]\n"
"v_pk_add_f32 v[68:69], v[68:69], v[44:45]\n"
"v_pk_add_f32 v[70:71], v[70:71], v[46:47]\n"
"v_pk_add_f32 v[44:45], v[68:69], v[70:71]\n"
"v_pk_add_f32 v[42:43], v[42:43], v[44:45]\n"
"v_add_f32 v0, v42, v43\n"
"v_mov_b32 v40, v0\n"
"v_cvt_pk_bf16_f32 v54, v68, v69\n"
"v_cvt_pk_bf16_f32 v55, v70, v71\n"
"buffer_store_dwordx4 v[52:55], v1, s[16:19], s2 offen sc1\n"
"v_permlane16_swap_b32 v0, v40\n"
"v_add_f32 v42, v0, v40\n"
"v_mov_b32 v0, v42\n"
"buffer_store_dwordx4 v[52:55], v35, s[28:31], s1 offen\n"
"buffer_load_dwordx4 a[84:87], v2, s[16:19], s37 offen offset:1024\n"
"v_permlane32_swap_b32 v42, v0\n"
"v_add_f32 v44, v42, v0\n"
"v_mul_f32 v42, 0x3d000000, v44\n"
"v_mul_f32 v44, 0xbd000000, v44\n"
"v_pk_add_f32 v[46:47], v[56:57], v[44:45] op_sel_hi:[1,0]\n"
"v_pk_add_f32 v[66:67], v[68:69], v[44:45] op_sel_hi:[1,0]\n"
"v_pk_add_f32 v[64:65], v[58:59], v[44:45] op_sel_hi:[1,0]\n"
"v_pk_add_f32 v[56:57], v[70:71], v[44:45] op_sel_hi:[1,0]\n"
"v_pk_mul_f32 v[44:45], v[46:47], v[46:47]\n"
"v_pk_mul_f32 v[46:47], v[66:67], v[66:67]\n"
"v_pk_fma_f32 v[44:45], v[64:65], v[64:65], v[44:45]\n"
"v_pk_fma_f32 v[46:47], v[56:57], v[56:57], v[46:47]\n"
"v_pk_add_f32 v[44:45], v[44:45], v[46:47]\n"
"buffer_load_dwordx4 a[92:95], v2, s[16:19], s37 offen offset:3072\n"
"v_add_f32 v0, v44, v45\n"
"v_mov_b32 v44, v0\n"
"s_nop 1\n"
"v_permlane16_swap_b32 v0, v44\n"
"v_add_f32 v40, v0, v44\n"
"v_mov_b32 v0, v40\n"
"s_nop 1\n"
"v_permlane32_swap_b32 v40, v0\n"
"v_add_f32 v43, v40, v0\n"
"buffer_store_dwordx2 v[42:43], v5, s[16:19], s0 offen sc1\n"
"buffer_load_dwordx4 a[100:103], v3, s[16:19], s37 offen offset:1024\n"
"buffer_load_dwordx4 a[108:111], v3, s[16:19], s37 offen offset:3072\n"
"buffer_load_dwordx4 a[116:119], v4, s[16:19], s37 offen offset:1024\n"
"buffer_load_dwordx4 a[124:127], v4, s[16:19], s37 offen offset:3072\n"
"s_add_u32 s20, s20, 0xc00000\n"
"s_addc_u32 s21, s21, 0\n"
"s_sub_u32 s24, s24, 0x1000000\n"
"s_subb_u32 s25, s25, 0\n"
"s_add_u32 s28, s4, 0x1a00000\n"
"s_addc_u32 s29, s5, 0\n"
"s_mov_b32 s80, s48\n"
"s_mov_b32 s81, s51\n"
"v_and_b32 v35, 0x7fffffff, v35\n"
"v_readfirstlane_b32 s69, v24\n"
"s_mov_b32 s68, 0\n"
"s_mov_b32 s18, 0\n"
"s_add_u32 s71, s71, 1\n"
"s_cmp_eq_u32 s71, 1\n"
"s_cbranch_scc1 LOOP%=\n"
"s_branch LEND%=\n"
"RkvA%=:\n"
"s_nop 7\n"
"s_cmp_eq_u32 s70, 0\n"
"s_cbranch_scc1 CkvA%=\n"
"s_sub_u32 s70, s70, 1\n"
"s_waitcnt vmcnt(0) lgkmcnt(0)\n"
"s_add_u32 s34, s48, 0x1400000\n"
"buffer_load_dwordx4 a[132:135], v1, s[24:27], s34 offen sc1\n"
"s_add_u32 s33, s48, 0x1408000\n"
"s_add_u32 s3, s51, 0x1500000\n"
"v_mov_b32 v44, -1\n"
"v_mov_b32 v45, -1\n"
"v_mov_b32 v46, -1\n"
"v_mov_b32 v47, -1\n"
"s_add_u32 s32, s52, 0x800000\n"
"s_add_u32 s2, s53, 0x900000\n"
"s_add_u32 s1, s52, 0xc00000\n"
"s_add_u32 s0, s53, 0xd00000\n"
"buffer_load_dwordx4 a[136:139], v1, s[24:27], s34 offen offset:1024 sc1\n"
"buffer_load_dwordx4 a[140:143], v1, s[24:27], s34 offen offset:2048 sc1\n"
"buffer_load_dwordx4 a[144:147], v1, s[24:27], s34 offen offset:3072 sc1\n"
"buffer_load_dwordx4 a[148:151], v1, s[24:27], s33 offen sc1\n"
"buffer_load_dwordx4 a[152:155], v1, s[24:27], s33 offen offset:1024 sc1\n"
"buffer_load_dwordx4 a[156:159], v1, s[24:27], s33 offen offset:2048 sc1\n"
"buffer_load_dwordx4 a[160:163], v1, s[24:27], s33 offen offset:3072 sc1\n"
"s_waitcnt vmcnt(7)\n"
"v_mfma_f32_16x16x32_bf16 v[72:75], a[0:3], a[132:135], 0\n"
"ds_write_b128 v9, a[132:135]\n"
"v_mfma_f32_16x16x32_bf16 v[68:71], a[4:7], a[132:135], 0\n"
"buffer_load_dwordx4 v[60:63], v1, s[24:27], s3 offen sc1\n"
"s_waitcnt vmcnt(7)\n"
"v_mfma_f32_16x16x32_bf16 v[72:75], a[8:11], a[136:139], v[72:75]\n"
"ds_write_b128 v9, a[136:139] offset:1024\n"
"v_mfma_f32_16x16x32_bf16 v[68:71], a[12:15], a[136:139], v[68:71]\n"
"buffer_store_dwordx4 v[44:47], v1, s[20:23], s32 offen sc1\n"
"s_waitcnt vmcnt(7)\n"
"v_mfma_f32_16x16x32_bf16 v[72:75], a[16:19], a[140:143], v[72:75]\n"
"ds_write_b128 v9, a[140:143] offset:2048\n"
"v_mfma_f32_16x16x32_bf16 v[68:71], a[20:23], a[140:143], v[68:71]\n"
"buffer_store_dwordx2 v[44:45], v5, s[20:23], s2 offen sc1\n"
"s_waitcnt vmcnt(7)\n"
"v_mfma_f32_16x16x32_bf16 v[72:75], a[24:27], a[144:147], v[72:75]\n"
"ds_write_b128 v9, a[144:147] offset:3072\n"
"v_mfma_f32_16x16x32_bf16 v[68:71], a[28:31], a[144:147], v[68:71]\n"
"buffer_store_dwordx4 v[44:47], v1, s[20:23], s1 offen sc1\n"
"s_waitcnt vmcnt(7)\n"
"v_mfma_f32_16x16x32_bf16 v[84:87], a[0:3], a[148:151], 0\n"
"ds_write_b128 v9, a[148:151] offset:16384\n"
"v_mfma_f32_16x16x32_bf16 v[76:79], a[4:7], a[148:151], 0\n"
"buffer_store_dwordx2 v[44:45], v5, s[20:23], s0 offen sc1\n"
"s_waitcnt vmcnt(7)\n"
"v_mfma_f32_16x16x32_bf16 v[84:87], a[8:11], a[152:155], v[84:87]\n"
"ds_write_b128 v9, a[152:155] offset:17408\n"
"v_mfma_f32_16x16x32_bf16 v[76:79], a[12:15], a[152:155], v[76:79]\n"
"s_waitcnt vmcnt(6)\n"
"v_mfma_f32_16x16x32_bf16 v[84:87], a[16:19], a[156:159], v[84:87]\n"
"ds_write_b128 v9, a[156:159] offset:18432\n"
"v_mfma_f32_16x16x32_bf16 v[76:79], a[20:23], a[156:159], v[76:79]\n"
"s_waitcnt vmcnt(5)\n"
"v_mfma_f32_16x16x32_bf16 v[84:87], a[24:27], a[160:163], v[84:87]\n"
"ds_write_b128 v9, a[160:163] offset:19456\n"
"v_mfma_f32_16x16x32_bf16 v[76:79], a[28:31], a[160:163], v[76:79]\n"
"s_waitcnt vmcnt(4)\n"
"v_add_f32 v40, v60, v62\n"
"v_mov_b32 v43, v40\n"
"v_add_f32 v0, v61, v63\n"
"s_nop 0\n"
"v_permlane16_swap_b32 v40, v43\n"
"v_add_f32 v42, v40, v43\n"
"v_mov_b32 v40, v42\n"
"v_cmp_u_f32 vcc, v72, v84\n"
"s_nop 0\n"
"v_permlane32_swap_b32 v42, v40\n"
"v_add_f32 v44, v42, v40\n"
"v_mul_f32 v42, 0x3e000000, v44\n"
"v_sub_f32 v40, v60, v42\n"
"v_sub_f32 v44, v62, v42\n"
"v_mul_f32 v45, v40, v40\n"
"v_fmac_f32 v45, v44, v44\n"
"v_fmamk_f32 v40, v45, 0x42000000, v0\n"
"v_mov_b32 v44, v40\n"
"s_nop 1\n"
"v_permlane16_swap_b32 v40, v44\n"
"v_add_f32 v0, v40, v44\n"
"v_mov_b32 v40, v0\n"
"s_nop 1\n"
"v_permlane32_swap_b32 v0, v40\n"
"v_add_f32 v43, v0, v40\n"
"v_cmp_u_f32 s[0:1], v43, v43\n"
"ds_write_b64 v17, v[42:43]\n"
"s_or_b64 vcc, vcc, s[0:1]\n"
"s_waitcnt lgkmcnt(0)\n"
"s_nop 7\n"
"s_cbranch_vccnz RkvA%=\n"
"s_branch CkvA%=\n"
"RkvB%=:\n"
"s_nop 7\n"
"s_cmp_eq_u32 s70, 0\n"
"s_cbranch_scc1 CkvB%=\n"
"s_sub_u32 s70, s70, 1\n"
"s_waitcnt vmcnt(0) lgkmcnt(0)\n"
"s_add_u32 s2, s80, 0x1800000\n"
"buffer_load_dwordx4 a[196:199], v1, s[24:27], s2 offen sc1\n"
"s_add_u32 s0, s80, 0x1808000\n"
"s_add_u32 s1, s81, 0x1900000\n"
"buffer_load_dwordx4 a[188:191], v1, s[24:27], s2 offen offset:1024 sc1\n"
"buffer_load_dwordx4 a[184:187], v1, s[24:27], s2 offen offset:2048 sc1\n"
"buffer_load_dwordx4 a[180:183], v1, s[24:27], s2 offen offset:3072 sc1\n"
"buffer_load_dwordx4 a[176:179], v1, s[24:27], s0 offen sc1\n"
"buffer_load_dwordx4 a[172:175], v1, s[24:27], s0 offen offset:1024 sc1\n"
"buffer_load_dwordx4 a[168:171], v1, s[24:27], s0 offen offset:2048 sc1\n"
"buffer_load_dwordx4 a[164:167], v1, s[24:27], s0 offen offset:3072 sc1\n"
"buffer_load_dwordx4 v[60:63], v1, s[24:27], s1 offen sc1\n"
"s_waitcnt vmcnt(8)\n"
"v_mfma_f32_16x16x32_bf16 v[64:67], a[0:3], a[196:199], 0\n"
"ds_write_b128 v9, a[196:199] offset:32768\n"
"v_mfma_f32_16x16x32_bf16 v[56:59], a[4:7], a[196:199], 0\n"
"s_waitcnt vmcnt(7)\n"
"v_mfma_f32_16x16x32_bf16 v[64:67], a[8:11], a[188:191], v[64:67]\n"
"ds_write_b128 v9, a[188:191] offset:33792\n"
"v_mfma_f32_16x16x32_bf16 v[56:59], a[12:15], a[188:191], v[56:59]\n"
"s_waitcnt vmcnt(6)\n"
"v_mfma_f32_16x16x32_bf16 v[64:67], a[16:19], a[184:187], v[64:67]\n"
"ds_write_b128 v9, a[184:187] offset:34816\n"
"v_mfma_f32_16x16x32_bf16 v[56:59], a[20:23], a[184:187], v[56:59]\n"
"s_waitcnt vmcnt(5)\n"
"v_mfma_f32_16x16x32_bf16 v[64:67], a[24:27], a[180:183], v[64:67]\n"
"ds_write_b128 v9, a[180:183] offset:35840\n"
"v_mfma_f32_16x16x32_bf16 v[56:59], a[28:31], a[180:183], v[56:59]\n"
"s_waitcnt vmcnt(4)\n"
"v_mfma_f32_16x16x32_bf16 v[72:75], a[0:3], a[176:179], 0\n"
"ds_write_b128 v9, a[176:179] offset:49152\n"
"v_mfma_f32_16x16x32_bf16 v[68:71], a[4:7], a[176:179], 0\n"
"s_waitcnt vmcnt(3)\n"
"v_mfma_f32_16x16x32_bf16 v[72:75], a[8:11], a[172:175], v[72:75]\n"
"ds_write_b128 v9, a[172:175] offset:50176\n"
"v_mfma_f32_16x16x32_bf16 v[68:71], a[12:15], a[172:175], v[68:71]\n"
"s_waitcnt vmcnt(2)\n"
"v_mfma_f32_16x16x32_bf16 v[72:75], a[16:19], a[168:171], v[72:75]\n"
"ds_write_b128 v9, a[168:171] offset:51200\n"
"v_mfma_f32_16x16x32_bf16 v[68:71], a[20:23], a[168:171], v[68:71]\n"
"s_waitcnt vmcnt(0)\n"
"v_add_f32 v44, v60, v62\n"
"v_mfma_f32_16x16x32_bf16 v[72:75], a[24:27], a[164:167], v[72:75]\n"
"v_mov_b32 v45, v44\n"
"v_add_f32 v46, v61, v63\n"
"v_mfma_f32_16x16x32_bf16 v[68:71], a[28:31], a[164:167], v[68:71]\n"
"v_permlane16_swap_b32 v44, v45\n"
"v_add_f32 v47, v44, v45\n"
"v_mov_b32 v44, v47\n"
"ds_write_b128 v9, a[164:167] offset:52224\n"
"s_nop 0\n"
"v_permlane32_swap_b32 v47, v44\n"
"v_add_f32 v76, v47, v44\n"
"v_mul_f32 v44, 0x3e000000, v76\n"
"v_sub_f32 v47, v60, v44\n"
"v_sub_f32 v76, v62, v44\n"
"v_mul_f32 v60, v47, v47\n"
"v_fmac_f32 v60, v76, v76\n"
"v_fmamk_f32 v47, v60, 0x42000000, v46\n"
"v_mov_b32 v46, v47\n"
"v_cmp_u_f32 vcc, v64, v72\n"
"s_nop 0\n"
"v_permlane16_swap_b32 v47, v46\n"
"v_add_f32 v60, v47, v46\n"
"v_mov_b32 v46, v60\n"
"s_nop 1\n"
"v_permlane32_swap_b32 v60, v46\n"
"v_add_f32 v45, v60, v46\n"
"v_cmp_u_f32 s[0:1], v45, v45\n"
"ds_write_b64 v17, v[44:45] offset:512\n"
"s_or_b64 vcc, vcc, s[0:1]\n"
"s_waitcnt lgkmcnt(0)\n"
"s_nop 7\n"
"s_cbranch_vccnz RkvB%=\n"
"s_branch CkvB%=\n"
"Rred%=:\n"
"s_nop 7\n"
"s_cmp_eq_u32 s70, 0\n"
"s_cbranch_scc1 Cred%=\n"
"s_sub_u32 s70, s70, 1\n"
"buffer_load_dwordx4 v[56:59], v30, s[28:31], s61 offen sc1\n"
"s_add_u32 s0, s61, 0x20000\n"
"buffer_load_dwordx4 v[60:63], v30, s[28:31], s0 offen sc1\n"
"buffer_load_dword v0, v31, s[20:23], s62 offen sc1\n"
"s_add_u32 s0, s62, 0x1000\n"
"buffer_load_dword v40, v31, s[20:23], s0 offen sc1\n"
"s_waitcnt vmcnt(0) lgkmcnt(0)\n"
"v_max3_u32 v42, v56, v57, v58\n"
"v_max3_u32 v43, v59, v60, v61\n"
"v_max3_u32 v64, v62, v63, v0\n"
"v_max3_u32 v65, v40, v42, v43\n"
"v_max_u32 v42, v64, v65\n"
"v_cmp_eq_u32 vcc, -1, v42\n"
"s_cbranch_vccnz Rred%=\n"
"s_nop 1\n"
"s_branch Bred%=\n"
"RO%=:\n"
"s_nop 7\n"
"s_cmp_eq_u32 s70, 0\n"
"s_cbranch_scc1 CO%=\n"
"s_sub_u32 s70, s70, 1\n"
"buffer_load_dwordx4 v[44:47], v1, s[20:23], s65 offen sc1\n"
"buffer_load_dwordx4 v[56:59], v34, s[20:23], s66 offen sc1\n"
"buffer_load_dwordx4 v[60:63], v1, s[20:23], s65 offen offset:1024 sc1\n"
"buffer_load_dwordx4 v[76:79], v34, s[20:23], s66 offen offset:64 sc1\n"
"buffer_load_dword v80, v41, s[20:23], 0 offen sc1\n"
"s_waitcnt vmcnt(0) lgkmcnt(0)\n"
"v_max3_u32 v0, v44, v45, v46\n"
"v_max3_u32 v40, v47, v56, v57\n"
"v_max3_u32 v42, v58, v59, v60\n"
"v_max3_u32 v43, v61, v62, v63\n"
"v_max3_u32 v64, v76, v77, v78\n"
"v_max3_u32 v65, v79, v80, v0\n"
"v_max3_u32 v0, v40, v42, v43\n"
"v_max3_u32 v40, v64, v65, v0\n"
"v_cmp_eq_u32 vcc, -1, v40\n"
"s_cbranch_vccnz RO%=\n"
"s_nop 1\n"
"s_branch BO%=\n"
"RwoA%=:\n"
"s_nop 7\n"
"s_cmp_eq_u32 s70, 0\n"
"s_cbranch_scc1 CwoA%=\n"
"s_sub_u32 s70, s70, 1\n"
"s_add_u32 s0, s48, 0x400000\n"
"buffer_load_dword v0, v37, s[20:23], s0 offen sc1\n"
"s_waitcnt vmcnt(0) lgkmcnt(0)\n"
"v_cmp_eq_u32 vcc, -1, v0\n"
"s_cbranch_vccnz RwoA%=\n"
"s_nop 1\n"
"s_branch BwoA%=\n"
"RwoB%=:\n"
"s_nop 7\n"
"s_cmp_eq_u32 s70, 0\n"
"s_cbranch_scc1 CwoB%=\n"
"s_sub_u32 s70, s70, 1\n"
"s_waitcnt vmcnt(0) lgkmcnt(0)\n"
"s_add_u32 s1, s50, 0x400000\n"
"buffer_load_dwordx4 v[68:71], v1, s[20:23], s1 offen sc1\n"
"s_add_u32 s0, s49, 0x400000\n"
"v_lshlrev_b32 v56, 16, v52\n"
"v_and_b32 v57, s72, v52\n"
"v_lshlrev_b32 v58, 16, v53\n"
"v_and_b32 v59, s72, v53\n"
"v_lshlrev_b32 v60, 16, v54\n"
"v_and_b32 v61, s72, v54\n"
"v_lshlrev_b32 v62, 16, v55\n"
"v_and_b32 v63, s72, v55\n"
"buffer_load_dwordx4 a[96:99], v1, s[20:23], s1 offen offset:1024 sc1\n"
"buffer_load_dwordx4 a[112:115], v1, s[20:23], s1 offen offset:2048 sc1\n"
"buffer_load_dwordx4 a[68:71], v1, s[20:23], s1 offen offset:3072 sc1\n"
"buffer_load_dwordx4 a[120:123], v1, s[20:23], s0 offen sc1\n"
"buffer_load_dwordx4 v[92:95], v1, s[20:23], s0 offen offset:1024 sc1\n"
"buffer_load_dwordx4 v[96:99], v1, s[20:23], s0 offen offset:2048 sc1\n"
"buffer_load_dwordx4 v[88:91], v1, s[20:23], s0 offen offset:3072 sc1\n"
"s_waitcnt vmcnt(7)\n"
"v_mfma_f32_16x16x32_bf16 v[44:47], a[200:203], v[68:71], 0\n"
"v_mfma_f32_16x16x32_bf16 a[88:91], a[204:207], v[68:71], 0\n"
"v_mfma_f32_16x16x32_bf16 v[76:79], a[136:139], v[68:71], 0\n"
"v_mfma_f32_16x16x32_bf16 v[80:83], a[132:135], v[68:71], 0\n"
"s_waitcnt vmcnt(6)\n"
"v_mfma_f32_16x16x32_bf16 v[44:47], v[172:175], a[96:99], v[44:47]\n"
"v_mfma_f32_16x16x32_bf16 a[88:91], v[168:171], a[96:99], a[88:91]\n"
"v_mfma_f32_16x16x32_bf16 v[76:79], a[208:211], a[96:99], v[76:79]\n"
"v_mfma_f32_16x16x32_bf16 v[80:83], v[176:179], a[96:99], v[80:83]\n"
"s_waitcnt vmcnt(5)\n"
"v_mfma_f32_16x16x32_bf16 v[44:47], a[80:83], a[112:115], v[44:47]\n"
"v_mfma_f32_16x16x32_bf16 a[88:91], a[44:47], a[112:115], a[88:91]\n"
"v_mfma_f32_16x16x32_bf16 v[76:79], v[164:167], a[112:115], v[76:79]\n"
"v_mfma_f32_16x16x32_bf16 v[80:83], a[64:67], a[112:115], v[80:83]\n"
"s_waitcnt vmcnt(4)\n"
"v_mfma_f32_16x16x32_bf16 v[44:47], a[60:63], a[68:71], v[44:47]\n"
"v_mfma_f32_16x16x32_bf16 a[88:91], a[76:79], a[68:71], a[88:91]\n"
"v_mfma_f32_16x16x32_bf16 v[76:79], a[104:107], a[68:71], v[76:79]\n"
"v_mfma_f32_16x16x32_bf16 v[80:83], a[52:55], a[68:71], v[80:83]\n"
"s_waitcnt vmcnt(3)\n"
"v_mfma_f32_16x16x32_bf16 v[64:67], a[200:203], a[120:123], 0\n"
"v_mfma_f32_16x16x32_bf16 a[68:71], a[204:207], a[120:123], 0\n"
"v_mfma_f32_16x16x32_bf16 v[56:59], a[136:139], a[120:123], v[56:59]\n"
"v_mfma_f32_16x16x32_bf16 v[60:63], a[132:135], a[120:123], v[60:63]\n"
"ds_write_b128 v15, v[44:47] offset:32768\n"
"s_waitcnt vmcnt(2)\n"
"v_mfma_f32_16x16x32_bf16 v[64:67], v[172:175], v[92:95], v[64:67]\n"
"ds_write_b128 v15, a[88:91] offset:33792\n"
"v_mfma_f32_16x16x32_bf16 a[68:71], v[168:171], v[92:95], a[68:71]\n"
"ds_write_b128 v14, v[76:79] offset:32768\n"
"v_mfma_f32_16x16x32_bf16 v[56:59], a[208:211], v[92:95], v[56:59]\n"
"ds_write_b128 v14, v[80:83] offset:33792\n"
"s_waitcnt vmcnt(1)\n"
"v_mfma_f32_16x16x32_bf16 v[64:67], a[80:83], v[96:99], v[64:67]\n"
"v_mfma_f32_16x16x32_bf16 a[68:71], a[44:47], v[96:99], a[68:71]\n"
"v_mfma_f32_16x16x32_bf16 v[60:63], v[176:179], v[92:95], v[60:63]\n"
"v_mfma_f32_16x16x32_bf16 v[56:59], v[164:167], v[96:99], v[56:59]\n"
"v_mfma_f32_16x16x32_bf16 v[60:63], a[64:67], v[96:99], v[60:63]\n"
"s_waitcnt vmcnt(0)\n"
"v_mfma_f32_16x16x32_bf16 v[64:67], a[60:63], v[88:91], v[64:67]\n"
"v_mfma_f32_16x16x32_bf16 a[68:71], a[76:79], v[88:91], a[68:71]\n"
"v_mfma_f32_16x16x32_bf16 v[56:59], a[104:107], v[88:91], v[56:59]\n"
"v_mfma_f32_16x16x32_bf16 v[60:63], a[52:55], v[88:91], v[60:63]\n"
"s_nop 4\n"
"ds_write_b128 v13, v[64:67] offset:32768\n"
"v_cmp_u_f32 vcc, v44, v64\n"
"ds_write_b128 v13, a[68:71] offset:33792\n"
"s_waitcnt lgkmcnt(0)\n"
"s_nop 7\n"
"s_cbranch_vccnz RwoB%=\n"
"s_branch CwoB%=\n"
"Rw1A%=:\n"
"s_nop 7\n"
"s_cmp_eq_u32 s70, 0\n"
"s_cbranch_scc1 Cw1A%=\n"
"s_sub_u32 s70, s70, 1\n"
"s_waitcnt vmcnt(0) lgkmcnt(0)\n"
"s_add_u32 s2, s50, 0x800000\n"
"buffer_load_dwordx4 a[208:211], v1, s[20:23], s2 offen sc1\n"
"s_add_u32 s1, s49, 0x800000\n"
"s_add_u32 s0, s51, 0x900000\n"
"buffer_load_dwordx4 a[204:207], v1, s[20:23], s2 offen offset:1024 sc1\n"
"buffer_load_dwordx4 a[200:203], v1, s[20:23], s2 offen offset:2048 sc1\n"
"buffer_load_dwordx4 v[76:79], v1, s[20:23], s2 offen offset:3072 sc1\n"
"buffer_load_dwordx4 a[104:107], v1, s[20:23], s1 offen sc1\n"
"buffer_load_dwordx4 a[60:63], v1, s[20:23], s1 offen offset:1024 sc1\n"
"buffer_load_dwordx4 v[80:83], v1, s[20:23], s1 offen offset:2048 sc1\n"
"buffer_load_dwordx4 v[72:75], v1, s[20:23], s1 offen offset:3072 sc1\n"
"s_waitcnt vmcnt(7)\n"
"v_mfma_f32_16x16x32_bf16 v[64:67], a[124:127], a[208:211], 0\n"
"buffer_load_dwordx4 v[60:63], v1, s[20:23], s0 offen sc1\n"
"v_mfma_f32_16x16x32_bf16 v[168:171], a[116:119], a[208:211], 0\n"
"v_mfma_f32_16x16x32_bf16 v[176:179], a[92:95], a[208:211], 0\n"
"v_mfma_f32_16x16x32_bf16 v[172:175], a[108:111], a[208:211], 0\n"
"s_waitcnt vmcnt(7)\n"
"v_mfma_f32_16x16x32_bf16 v[64:67], a[164:167], a[204:207], v[64:67]\n"
"v_mfma_f32_16x16x32_bf16 v[168:171], a[168:171], a[204:207], v[168:171]\n"
"v_mfma_f32_16x16x32_bf16 v[176:179], a[100:103], a[204:207], v[176:179]\n"
"v_mfma_f32_16x16x32_bf16 v[172:175], a[84:87], a[204:207], v[172:175]\n"
"s_waitcnt vmcnt(6)\n"
"v_mfma_f32_16x16x32_bf16 v[64:67], a[180:183], a[200:203], v[64:67]\n"
"v_mfma_f32_16x16x32_bf16 v[168:171], a[184:187], a[200:203], v[168:171]\n"
"v_mfma_f32_16x16x32_bf16 v[176:179], a[172:175], a[200:203], v[176:179]\n"
"v_mfma_f32_16x16x32_bf16 v[172:175], a[176:179], a[200:203], v[172:175]\n"
"s_waitcnt vmcnt(5)\n"
"v_mfma_f32_16x16x32_bf16 v[64:67], a[192:195], v[76:79], v[64:67]\n"
"v_mfma_f32_16x16x32_bf16 v[168:171], a[196:199], v[76:79], v[168:171]\n"
"v_mfma_f32_16x16x32_bf16 v[176:179], a[188:191], v[76:79], v[176:179]\n"
"v_mfma_f32_16x16x32_bf16 v[172:175], v[100:103], v[76:79], v[172:175]\n"
"s_waitcnt vmcnt(4)\n"
"v_mfma_f32_16x16x32_bf16 v[68:71], a[124:127], a[104:107], 0\n"
"v_mfma_f32_16x16x32_bf16 v[76:79], a[116:119], a[104:107], 0\n"
"v_mfma_f32_16x16x32_bf16 v[52:55], a[92:95], a[104:107], 0\n"
"v_mfma_f32_16x16x32_bf16 v[56:59], a[108:111], a[104:107], 0\n"
"ds_write_b128 v15, v[64:67]\n"
"s_waitcnt vmcnt(3)\n"
"v_mfma_f32_16x16x32_bf16 v[68:71], a[164:167], a[60:63], v[68:71]\n"
"ds_write_b128 v15, v[168:171] offset:1024\n"
"v_mfma_f32_16x16x32_bf16 v[76:79], a[168:171], a[60:63], v[76:79]\n"
"ds_write_b128 v14, v[176:179]\n"
"v_mfma_f32_16x16x32_bf16 v[52:55], a[100:103], a[60:63], v[52:55]\n"
"ds_write_b128 v14, v[172:175] offset:1024\n"
"s_waitcnt vmcnt(2)\n"
"v_mfma_f32_16x16x32_bf16 v[68:71], a[180:183], v[80:83], v[68:71]\n"
"v_mfma_f32_16x16x32_bf16 v[76:79], a[184:187], v[80:83], v[76:79]\n"
"v_mfma_f32_16x16x32_bf16 v[56:59], a[84:87], a[60:63], v[56:59]\n"
"v_mfma_f32_16x16x32_bf16 v[52:55], a[172:175], v[80:83], v[52:55]\n"
"v_mfma_f32_16x16x32_bf16 v[56:59], a[176:179], v[80:83], v[56:59]\n"
"s_waitcnt vmcnt(0)\n"
"v_add_f32 v40, v60, v62\n"
"v_mov_b32 v42, v40\n"
"v_mfma_f32_16x16x32_bf16 v[68:71], a[192:195], v[72:75], v[68:71]\n"
"v_add_f32 v0, v61, v63\n"
"v_permlane16_swap_b32 v40, v42\n"
"v_mfma_f32_16x16x32_bf16 v[76:79], a[196:199], v[72:75], v[76:79]\n"
"v_add_f32 v43, v40, v42\n"
"v_mov_b32 v40, v43\n"
"v_mfma_f32_16x16x32_bf16 v[52:55], a[188:191], v[72:75], v[52:55]\n"
"s_nop 0\n"
"v_permlane32_swap_b32 v43, v40\n"
"v_mfma_f32_16x16x32_bf16 v[56:59], v[100:103], v[72:75], v[56:59]\n"
"v_add_f32 v72, v43, v40\n"
"v_mul_f32 v42, 0x3e000000, v72\n"
"v_sub_f32 v40, v60, v42\n"
"v_sub_f32 v72, v62, v42\n"
"v_mul_f32 v60, v40, v40\n"
"v_fmac_f32 v60, v72, v72\n"
"v_fmamk_f32 v40, v60, 0x42000000, v0\n"
"v_mov_b32 v0, v40\n"
"v_cmp_u_f32 vcc, v64, v68\n"
"ds_write_b128 v13, v[68:71]\n"
"v_permlane16_swap_b32 v40, v0\n"
"v_add_f32 v60, v40, v0\n"
"v_mov_b32 v0, v60\n"
"ds_write_b128 v13, v[76:79] offset:1024\n"
"s_nop 0\n"
"v_permlane32_swap_b32 v60, v0\n"
"v_add_f32 v43, v60, v0\n"
"v_cmp_u_f32 s[0:1], v43, v43\n"
"ds_write_b64 v17, v[42:43]\n"
"s_or_b64 vcc, vcc, s[0:1]\n"
"s_waitcnt lgkmcnt(0)\n"
"s_nop 7\n"
"s_cbranch_vccnz Rw1A%=\n"
"s_branch Cw1A%=\n"
"Rw1B%=:\n"
"s_nop 7\n"
"s_cmp_eq_u32 s70, 0\n"
"s_cbranch_scc1 Cw1B%=\n"
"s_sub_u32 s70, s70, 1\n"
"s_waitcnt vmcnt(0) lgkmcnt(0)\n"
"s_add_u32 s2, s50, 0xc00000\n"
"buffer_load_dwordx4 v[168:171], v1, s[20:23], s2 offen sc1\n"
"s_add_u32 s1, s49, 0xc00000\n"
"s_add_u32 s0, s51, 0xd00000\n"
"buffer_load_dwordx4 v[172:175], v1, s[20:23], s2 offen offset:1024 sc1\n"
"buffer_load_dwordx4 v[176:179], v1, s[20:23], s2 offen offset:2048 sc1\n"
"buffer_load_dwordx4 a[204:207], v1, s[20:23], s2 offen offset:3072 sc1\n"
"buffer_load_dwordx4 a[208:211], v1, s[20:23], s1 offen sc1\n"
"buffer_load_dwordx4 a[200:203], v1, s[20:23], s1 offen offset:1024 sc1\n"
"buffer_load_dwordx4 v[76:79], v1, s[20:23], s1 offen offset:2048 sc1\n"
"buffer_load_dwordx4 v[84:87], v1, s[20:23], s1 offen offset:3072 sc1\n"
"s_waitcnt vmcnt(7)\n"
"v_mfma_f32_16x16x32_bf16 v[68:71], a[124:127], v[168:171], 0\n"
"buffer_load_dwordx4 v[64:67], v1, s[20:23], s0 offen sc1\n"
"v_mfma_f32_16x16x32_bf16 v[88:91], a[116:119], v[168:171], 0\n"
"v_mfma_f32_16x16x32_bf16 v[80:83], a[92:95], v[168:171], 0\n"
"v_mfma_f32_16x16x32_bf16 v[164:167], a[108:111], v[168:171], 0\n"
"s_waitcnt vmcnt(7)\n"
"v_mfma_f32_16x16x32_bf16 v[68:71], a[164:167], v[172:175], v[68:71]\n"
"v_mfma_f32_16x16x32_bf16 v[88:91], a[168:171], v[172:175], v[88:91]\n"
"v_mfma_f32_16x16x32_bf16 v[80:83], a[100:103], v[172:175], v[80:83]\n"
"v_mfma_f32_16x16x32_bf16 v[164:167], a[84:87], v[172:175], v[164:167]\n"
"s_waitcnt vmcnt(6)\n"
"v_mfma_f32_16x16x32_bf16 v[68:71], a[180:183], v[176:179], v[68:71]\n"
"v_mfma_f32_16x16x32_bf16 v[88:91], a[184:187], v[176:179], v[88:91]\n"
"v_mfma_f32_16x16x32_bf16 v[80:83], a[172:175], v[176:179], v[80:83]\n"
"v_mfma_f32_16x16x32_bf16 v[164:167], a[176:179], v[176:179], v[164:167]\n"
"s_waitcnt vmcnt(5)\n"
"v_mfma_f32_16x16x32_bf16 v[68:71], a[192:195], a[204:207], v[68:71]\n"
"v_mfma_f32_16x16x32_bf16 v[88:91], a[196:199], a[204:207], v[88:91]\n"
"v_mfma_f32_16x16x32_bf16 v[80:83], a[188:191], a[204:207], v[80:83]\n"
"v_mfma_f32_16x16x32_bf16 v[164:167], v[100:103], a[204:207], v[164:167]\n"
"s_waitcnt vmcnt(4)\n"
"v_mfma_f32_16x16x32_bf16 v[72:75], a[124:127], a[208:211], 0\n"
"v_mfma_f32_16x16x32_bf16 a[204:207], a[116:119], a[208:211], 0\n"
"v_mfma_f32_16x16x32_bf16 v[56:59], a[92:95], a[208:211], 0\n"
"v_mfma_f32_16x16x32_bf16 v[60:63], a[108:111], a[208:211], 0\n"
"ds_write_b128 v15, v[68:71] offset:32768\n"
"s_waitcnt vmcnt(3)\n"
"v_mfma_f32_16x16x32_bf16 v[72:75], a[164:167], a[200:203], v[72:75]\n"
"ds_write_b128 v15, v[88:91] offset:33792\n"
"v_mfma_f32_16x16x32_bf16 a[204:207], a[168:171], a[200:203], a[204:207]\n"
"ds_write_b128 v14, v[80:83] offset:32768\n"
"v_mfma_f32_16x16x32_bf16 v[56:59], a[100:103], a[200:203], v[56:59]\n"
"ds_write_b128 v14, v[164:167] offset:33792\n"
"s_waitcnt vmcnt(2)\n"
"v_mfma_f32_16x16x32_bf16 v[72:75], a[180:183], v[76:79], v[72:75]\n"
"v_mfma_f32_16x16x32_bf16 a[204:207], a[184:187], v[76:79], a[204:207]\n"
"v_mfma_f32_16x16x32_bf16 v[60:63], a[84:87], a[200:203], v[60:63]\n"
"v_mfma_f32_16x16x32_bf16 v[56:59], a[172:175], v[76:79], v[56:59]\n"
"v_mfma_f32_16x16x32_bf16 v[60:63], a[176:179], v[76:79], v[60:63]\n"
"s_waitcnt vmcnt(0)\n"
"v_add_f32 v40, v64, v66\n"
"v_mov_b32 v52, v40\n"
"v_mfma_f32_16x16x32_bf16 v[72:75], a[192:195], v[84:87], v[72:75]\n"
"v_add_f32 v0, v65, v67\n"
"v_permlane16_swap_b32 v40, v52\n"
"v_mfma_f32_16x16x32_bf16 a[204:207], a[196:199], v[84:87], a[204:207]\n"
"v_add_f32 v53, v40, v52\n"
"v_mov_b32 v40, v53\n"
"v_mfma_f32_16x16x32_bf16 v[56:59], a[188:191], v[84:87], v[56:59]\n"
"s_nop 0\n"
"v_permlane32_swap_b32 v53, v40\n"
"v_mfma_f32_16x16x32_bf16 v[60:63], v[100:103], v[84:87], v[60:63]\n"
"v_add_f32 v76, v53, v40\n"
"v_mul_f32 v52, 0x3e000000, v76\n"
"v_sub_f32 v40, v64, v52\n"
"v_sub_f32 v76, v66, v52\n"
"v_mul_f32 v64, v40, v40\n"
"v_fmac_f32 v64, v76, v76\n"
"v_fmamk_f32 v40, v64, 0x42000000, v0\n"
"v_mov_b32 v0, v40\n"
"v_cmp_u_f32 vcc, v68, v72\n"
"ds_write_b128 v13, v[72:75] offset:32768\n"
"v_permlane16_swap_b32 v40, v0\n"
"v_add_f32 v64, v40, v0\n"
"v_mov_b32 v0, v64\n"
"ds_write_b128 v13, a[204:207] offset:33792\n"
"s_nop 0\n"
"v_permlane32_swap_b32 v64, v0\n"
"v_add_f32 v53, v64, v0\n"
"v_cmp_u_f32 s[0:1], v53, v53\n"
"ds_write_b64 v17, v[52:53] offset:512\n"
"s_or_b64 vcc, vcc, s[0:1]\n"
"s_waitcnt lgkmcnt(0)\n"
"s_nop 7\n"
"s_cbranch_vccnz Rw1B%=\n"
"s_branch Cw1B%=\n"
"Rw2A%=:\n"
"s_nop 7\n"
"s_cmp_eq_u32 s70, 0\n"
"s_cbranch_scc1 Cw2A%=\n"
"s_sub_u32 s70, s70, 1\n"
"s_waitcnt vmcnt(0) lgkmcnt(0)\n"
"s_add_u32 s1, s50, 0xfb8000\n"
"buffer_load_dwordx4 a[188:191], v38, s[24:27], s1 offen sc1\n"
"s_add_u32 s0, s49, 0xfb8000\n"
"v_lshlrev_b32 v64, 16, v48\n"
"v_and_b32 v65, s72, v48\n"
"v_lshlrev_b32 v66, 16, v49\n"
"v_and_b32 v67, s72, v49\n"
"v_lshlrev_b32 v68, 16, v50\n"
"v_and_b32 v69, s72, v50\n"
"v_lshlrev_b32 v70, 16, v51\n"
"v_and_b32 v71, s72, v51\n"
"v_fma_f32 v64, v64, v42, v54\n"
"v_fma_f32 v65, v65, v42, v54\n"
"v_fma_f32 v66, v66, v42, v54\n"
"v_fma_f32 v67, v67, v42, v54\n"
"buffer_load_dwordx4 a[184:187], v38, s[24:27], s1 offen offset:1024 sc1\n"
"v_fma_f32 v68, v68, v42, v54\n"
"v_fma_f32 v69, v69, v42, v54\n"
"v_fma_f32 v70, v70, v42, v54\n"
"v_fma_f32 v71, v71, v42, v54\n"
"buffer_load_dwordx4 a[180:183], v38, s[24:27], s1 offen offset:2048 sc1\n"
"buffer_load_dwordx4 v[104:107], v38, s[24:27], s1 offen offset:3072 sc1\n"
"buffer_load_dwordx4 v[88:91], v8, s[24:27], s0 offen sc1\n"
"buffer_load_dwordx4 v[76:79], v8, s[24:27], s0 offen offset:1024 sc1\n"
"buffer_load_dwordx4 v[84:87], v8, s[24:27], s0 offen offset:2048 sc1\n"
"buffer_load_dwordx4 v[80:83], v8, s[24:27], s0 offen offset:3072 sc1\n"
"s_waitcnt vmcnt(7)\n"
"v_mfma_f32_16x16x32_bf16 v[56:59], a[212:215], a[188:191], 0\n"
"v_mfma_f32_16x16x32_bf16 v[100:103], a[216:219], a[188:191], 0\n"
"v_mfma_f32_16x16x32_bf16 a[192:195], a[144:147], a[188:191], 0\n"
"v_mfma_f32_16x16x32_bf16 a[196:199], a[140:143], a[188:191], 0\n"
"s_waitcnt vmcnt(6)\n"
"v_mfma_f32_16x16x32_bf16 v[56:59], a[228:231], a[184:187], v[56:59]\n"
"v_mfma_f32_16x16x32_bf16 v[100:103], a[232:235], a[184:187], v[100:103]\n"
"v_mfma_f32_16x16x32_bf16 a[192:195], a[220:223], a[184:187], a[192:195]\n"
"v_mfma_f32_16x16x32_bf16 a[196:199], a[224:227], a[184:187], a[196:199]\n"
"s_waitcnt vmcnt(5)\n"
"v_mfma_f32_16x16x32_bf16 v[56:59], v[200:203], a[180:183], v[56:59]\n"
"v_mfma_f32_16x16x32_bf16 v[100:103], v[196:199], a[180:183], v[100:103]\n"
"v_mfma_f32_16x16x32_bf16 a[192:195], a[236:239], a[180:183], a[192:195]\n"
"v_mfma_f32_16x16x32_bf16 a[196:199], v[204:207], a[180:183], a[196:199]\n"
"s_waitcnt vmcnt(4)\n"
"v_mfma_f32_16x16x32_bf16 v[56:59], v[184:187], v[104:107], v[56:59]\n"
"v_mfma_f32_16x16x32_bf16 v[100:103], v[180:183], v[104:107], v[100:103]\n"
"v_mfma_f32_16x16x32_bf16 a[192:195], v[192:195], v[104:107], a[192:195]\n"
"v_mfma_f32_16x16x32_bf16 a[196:199], v[188:191], v[104:107], a[196:199]\n"
"s_waitcnt vmcnt(3)\n"
"v_mfma_f32_16x16x32_bf16 v[72:75], a[212:215], v[88:91], 0\n"
"v_mfma_f32_16x16x32_bf16 v[104:107], a[216:219], v[88:91], 0\n"
"v_mfma_f32_16x16x32_bf16 v[64:67], a[144:147], v[88:91], v[64:67]\n"
"v_mfma_f32_16x16x32_bf16 v[68:71], a[140:143], v[88:91], v[68:71]\n"
"ds_write_b128 v15, v[56:59]\n"
"s_waitcnt vmcnt(2)\n"
"v_mfma_f32_16x16x32_bf16 v[72:75], a[228:231], v[76:79], v[72:75]\n"
"ds_write_b128 v15, v[100:103] offset:1024\n"
"v_mfma_f32_16x16x32_bf16 v[104:107], a[232:235], v[76:79], v[104:107]\n"
"ds_write_b128 v14, a[192:195]\n"
"v_mfma_f32_16x16x32_bf16 v[64:67], a[220:223], v[76:79], v[64:67]\n"
"ds_write_b128 v14, a[196:199] offset:1024\n"
"s_waitcnt vmcnt(1)\n"
"v_mfma_f32_16x16x32_bf16 v[72:75], v[200:203], v[84:87], v[72:75]\n"
"v_mfma_f32_16x16x32_bf16 v[104:107], v[196:199], v[84:87], v[104:107]\n"
"v_mfma_f32_16x16x32_bf16 v[68:71], a[224:227], v[76:79], v[68:71]\n"
"v_mfma_f32_16x16x32_bf16 v[64:67], a[236:239], v[84:87], v[64:67]\n"
"v_mfma_f32_16x16x32_bf16 v[68:71], v[204:207], v[84:87], v[68:71]\n"
"s_waitcnt vmcnt(0)\n"
"v_mfma_f32_16x16x32_bf16 v[72:75], v[184:187], v[80:83], v[72:75]\n"
"v_mfma_f32_16x16x32_bf16 v[104:107], v[180:183], v[80:83], v[104:107]\n"
"v_mfma_f32_16x16x32_bf16 v[64:67], v[192:195], v[80:83], v[64:67]\n"
"v_mfma_f32_16x16x32_bf16 v[68:71], v[188:191], v[80:83], v[68:71]\n"
"s_nop 4\n"
"ds_write_b128 v13, v[72:75]\n"
"v_cmp_u_f32 vcc, v56, v72\n"
"ds_write_b128 v13, v[104:107] offset:1024\n"
"s_waitcnt lgkmcnt(0)\n"
"s_nop 7\n"
"s_cbranch_vccnz Rw2A%=\n"
"s_branch Cw2A%=\n"
"Rw2B%=:\n"
"s_nop 7\n"
"s_cmp_eq_u32 s70, 0\n"
"s_cbranch_scc1 Cw2B%=\n"
"s_sub_u32 s70, s70, 1\n"
"s_waitcnt vmcnt(0) lgkmcnt(0)\n"
"s_add_u32 s1, s50, 0x13b8000\n"
"buffer_load_dwordx4 v[176:179], v38, s[24:27], s1 offen sc1\n"
"s_add_u32 s0, s49, 0x13b8000\n"
"v_lshlrev_b32 v72, 16, v44\n"
"v_and_b32 v73, s72, v44\n"
"v_lshlrev_b32 v74, 16, v45\n"
"v_and_b32 v75, s72, v45\n"
"v_lshlrev_b32 v76, 16, v46\n"
"v_and_b32 v77, s72, v46\n"
"v_lshlrev_b32 v78, 16, v47\n"
"v_and_b32 v79, s72, v47\n"
"v_fma_f32 v72, v72, v52, v60\n"
"v_fma_f32 v73, v73, v52, v60\n"
"v_fma_f32 v74, v74, v52, v60\n"
"v_fma_f32 v75, v75, v52, v60\n"
"buffer_load_dwordx4 v[112:115], v38, s[24:27], s1 offen offset:1024 sc1\n"
"v_fma_f32 v76, v76, v52, v60\n"
"v_fma_f32 v77, v77, v52, v60\n"
"v_fma_f32 v78, v78, v52, v60\n"
"v_fma_f32 v79, v79, v52, v60\n"
"buffer_load_dwordx4 v[104:107], v38, s[24:27], s1 offen offset:2048 sc1\n"
"buffer_load_dwordx4 v[108:111], v38, s[24:27], s1 offen offset:3072 sc1\n"
"buffer_load_dwordx4 v[92:95], v8, s[24:27], s0 offen sc1\n"
"buffer_load_dwordx4 v[100:103], v8, s[24:27], s0 offen offset:1024 sc1\n"
"buffer_load_dwordx4 v[88:91], v8, s[24:27], s0 offen offset:2048 sc1\n"
"buffer_load_dwordx4 v[84:87], v8, s[24:27], s0 offen offset:3072 sc1\n"
"s_waitcnt vmcnt(7)\n"
"v_mfma_f32_16x16x32_bf16 v[64:67], a[212:215], v[176:179], 0\n"
"v_mfma_f32_16x16x32_bf16 v[172:175], a[216:219], v[176:179], 0\n"
"v_mfma_f32_16x16x32_bf16 v[80:83], a[144:147], v[176:179], 0\n"
"v_mfma_f32_16x16x32_bf16 v[164:167], a[140:143], v[176:179], 0\n"
"s_waitcnt vmcnt(6)\n"
"v_mfma_f32_16x16x32_bf16 v[64:67], a[228:231], v[112:115], v[64:67]\n"
"v_mfma_f32_16x16x32_bf16 v[172:175], a[232:235], v[112:115], v[172:175]\n"
"v_mfma_f32_16x16x32_bf16 v[80:83], a[220:223], v[112:115], v[80:83]\n"
"v_mfma_f32_16x16x32_bf16 v[164:167], a[224:227], v[112:115], v[164:167]\n"
"s_waitcnt vmcnt(5)\n"
"v_mfma_f32_16x16x32_bf16 v[64:67], v[200:203], v[104:107], v[64:67]\n"
"v_mfma_f32_16x16x32_bf16 v[172:175], v[196:199], v[104:107], v[172:175]\n"
"v_mfma_f32_16x16x32_bf16 v[80:83], a[236:239], v[104:107], v[80:83]\n"
"v_mfma_f32_16x16x32_bf16 v[164:167], v[204:207], v[104:107], v[164:167]\n"
"s_waitcnt vmcnt(4)\n"
"v_mfma_f32_16x16x32_bf16 v[64:67], v[184:187], v[108:111], v[64:67]\n"
"v_mfma_f32_16x16x32_bf16 v[172:175], v[180:183], v[108:111], v[172:175]\n"
"v_mfma_f32_16x16x32_bf16 v[80:83], v[192:195], v[108:111], v[80:83]\n"
"v_mfma_f32_16x16x32_bf16 v[164:167], v[188:191], v[108:111], v[164:167]\n"
"s_waitcnt vmcnt(3)\n"
"v_mfma_f32_16x16x32_bf16 v[68:71], a[212:215], v[92:95], 0\n"
"v_mfma_f32_16x16x32_bf16 v[108:111], a[216:219], v[92:95], 0\n"
"v_mfma_f32_16x16x32_bf16 v[72:75], a[144:147], v[92:95], v[72:75]\n"
"v_mfma_f32_16x16x32_bf16 v[76:79], a[140:143], v[92:95], v[76:79]\n"
"ds_write_b128 v15, v[64:67] offset:32768\n"
"s_waitcnt vmcnt(2)\n"
"v_mfma_f32_16x16x32_bf16 v[68:71], a[228:231], v[100:103], v[68:71]\n"
"ds_write_b128 v15, v[172:175] offset:33792\n"
"v_mfma_f32_16x16x32_bf16 v[108:111], a[232:235], v[100:103], v[108:111]\n"
"ds_write_b128 v14, v[80:83] offset:32768\n"
"v_mfma_f32_16x16x32_bf16 v[72:75], a[220:223], v[100:103], v[72:75]\n"
"ds_write_b128 v14, v[164:167] offset:33792\n"
"s_waitcnt vmcnt(1)\n"
"v_mfma_f32_16x16x32_bf16 v[68:71], v[200:203], v[88:91], v[68:71]\n"
"v_mfma_f32_16x16x32_bf16 v[108:111], v[196:199], v[88:91], v[108:111]\n"
"v_mfma_f32_16x16x32_bf16 v[76:79], a[224:227], v[100:103], v[76:79]\n"
"v_mfma_f32_16x16x32_bf16 v[72:75], a[236:239], v[88:91], v[72:75]\n"
"v_mfma_f32_16x16x32_bf16 v[76:79], v[204:207], v[88:91], v[76:79]\n"
"s_waitcnt vmcnt(0)\n"
"v_mfma_f32_16x16x32_bf16 v[68:71], v[184:187], v[84:87], v[68:71]\n"
"v_mfma_f32_16x16x32_bf16 v[108:111], v[180:183], v[84:87], v[108:111]\n"
"v_mfma_f32_16x16x32_bf16 v[72:75], v[192:195], v[84:87], v[72:75]\n"
"v_mfma_f32_16x16x32_bf16 v[76:79], v[188:191], v[84:87], v[76:79]\n"
"s_nop 4\n"
"ds_write_b128 v13, v[68:71] offset:32768\n"
"v_cmp_u_f32 vcc, v64, v68\n"
"ds_write_b128 v13, v[108:111] offset:33792\n"
"s_waitcnt lgkmcnt(0)\n"
"s_nop 7\n"
"s_cbranch_vccnz Rw2B%=\n"
"s_branch Cw2B%=\n"
"Rw3A%=:\n"
"s_nop 7\n"
"s_cmp_eq_u32 s70, 0\n"
"s_cbranch_scc1 Cw3A%=\n"
"s_sub_u32 s70, s70, 1\n"
"s_waitcnt vmcnt(0) lgkmcnt(0)\n"
"s_add_u32 s2, s48, 0x1800000\n"
"buffer_load_dwordx4 v[188:191], v1, s[24:27], s2 offen sc1\n"
"s_add_u32 s1, s48, 0x1808000\n"
"s_add_u32 s0, s51, 0x1900000\n"
"buffer_load_dwordx4 v[192:195], v1, s[24:27], s2 offen offset:1024 sc1\n"
"buffer_load_dwordx4 v[196:199], v1, s[24:27], s2 offen offset:2048 sc1\n"
"buffer_load_dwordx4 v[200:203], v1, s[24:27], s2 offen offset:3072 sc1\n"
"buffer_load_dwordx4 v[204:207], v1, s[24:27], s1 offen sc1\n"
"buffer_load_dwordx4 a[144:147], v1, s[24:27], s1 offen offset:1024 sc1\n"
"buffer_load_dwordx4 a[140:143], v1, s[24:27], s1 offen offset:2048 sc1\n"
"buffer_load_dwordx4 v[88:91], v1, s[24:27], s1 offen offset:3072 sc1\n"
"s_waitcnt vmcnt(7)\n"
"v_mfma_f32_16x16x32_bf16 v[80:83], a[72:75], v[188:191], 0\n"
"ds_write_b128 v9, v[188:191]\n"
"v_mfma_f32_16x16x32_bf16 v[76:79], a[48:51], v[188:191], 0\n"
"buffer_load_dwordx4 v[60:63], v1, s[24:27], s0 offen sc1\n"
"v_mfma_f32_16x16x32_bf16 v[84:87], a[16:19], v[188:191], 0\n"
"s_waitcnt vmcnt(7)\n"
"v_mfma_f32_16x16x32_bf16 v[80:83], a[40:43], v[192:195], v[80:83]\n"
"ds_write_b128 v9, v[192:195] offset:1024\n"
"v_mfma_f32_16x16x32_bf16 v[76:79], a[32:35], v[192:195], v[76:79]\n"
"v_mfma_f32_16x16x32_bf16 v[84:87], a[20:23], v[192:195], v[84:87]\n"
"s_waitcnt vmcnt(6)\n"
"v_mfma_f32_16x16x32_bf16 v[80:83], a[0:3], v[196:199], v[80:83]\n"
"ds_write_b128 v9, v[196:199] offset:2048\n"
"v_mfma_f32_16x16x32_bf16 v[76:79], a[4:7], v[196:199], v[76:79]\n"
"v_mfma_f32_16x16x32_bf16 v[84:87], a[24:27], v[196:199], v[84:87]\n"
"s_waitcnt vmcnt(5)\n"
"v_mfma_f32_16x16x32_bf16 v[80:83], a[8:11], v[200:203], v[80:83]\n"
"ds_write_b128 v9, v[200:203] offset:3072\n"
"v_mfma_f32_16x16x32_bf16 v[76:79], a[12:15], v[200:203], v[76:79]\n"
"v_mfma_f32_16x16x32_bf16 v[84:87], a[28:31], v[200:203], v[84:87]\n"
"s_waitcnt vmcnt(4)\n"
"v_mfma_f32_16x16x32_bf16 v[64:67], a[72:75], v[204:207], 0\n"
"ds_write_b128 v9, v[204:207] offset:16384\n"
"v_mfma_f32_16x16x32_bf16 v[68:71], a[48:51], v[204:207], 0\n"
"v_mfma_f32_16x16x32_bf16 v[72:75], a[16:19], v[204:207], 0\n"
"s_waitcnt vmcnt(3)\n"
"v_mfma_f32_16x16x32_bf16 v[64:67], a[40:43], a[144:147], v[64:67]\n"
"ds_write_b128 v9, a[144:147] offset:17408\n"
"v_mfma_f32_16x16x32_bf16 v[68:71], a[32:35], a[144:147], v[68:71]\n"
"v_mfma_f32_16x16x32_bf16 v[72:75], a[20:23], a[144:147], v[72:75]\n"
"s_waitcnt vmcnt(2)\n"
"v_mfma_f32_16x16x32_bf16 v[64:67], a[0:3], a[140:143], v[64:67]\n"
"ds_write_b128 v9, a[140:143] offset:18432\n"
"v_mfma_f32_16x16x32_bf16 v[68:71], a[4:7], a[140:143], v[68:71]\n"
"v_mfma_f32_16x16x32_bf16 v[72:75], a[24:27], a[140:143], v[72:75]\n"
"s_waitcnt vmcnt(1)\n"
"v_mfma_f32_16x16x32_bf16 v[64:67], a[8:11], v[88:91], v[64:67]\n"
"ds_write_b128 v9, v[88:91] offset:19456\n"
"v_mfma_f32_16x16x32_bf16 v[68:71], a[12:15], v[88:91], v[68:71]\n"
"s_waitcnt vmcnt(0)\n"
"v_add_f32 v40, v60, v62\n"
"v_mov_b32 v42, v40\n"
"v_mfma_f32_16x16x32_bf16 v[72:75], a[28:31], v[88:91], v[72:75]\n"
"v_add_f32 v0, v61, v63\n"
"v_permlane16_swap_b32 v40, v42\n"
"v_add_f32 v43, v40, v42\n"
"v_mov_b32 v40, v43\n"
"v_cmp_u_f32 vcc, v80, v64\n"
"s_nop 0\n"
"v_permlane32_swap_b32 v43, v40\n"
"v_add_f32 v88, v43, v40\n"
"v_mul_f32 v42, 0x3e000000, v88\n"
"v_sub_f32 v40, v60, v42\n"
"v_sub_f32 v88, v62, v42\n"
"v_mul_f32 v60, v40, v40\n"
"v_fmac_f32 v60, v88, v88\n"
"v_fmamk_f32 v40, v60, 0x42000000, v0\n"
"v_mov_b32 v60, v40\n"
"s_nop 1\n"
"v_permlane16_swap_b32 v40, v60\n"
"v_add_f32 v0, v40, v60\n"
"v_mov_b32 v40, v0\n"
"s_nop 1\n"
"v_permlane32_swap_b32 v0, v40\n"
"v_add_f32 v43, v0, v40\n"
"v_cmp_u_f32 s[0:1], v43, v43\n"
"ds_write_b64 v17, v[42:43]\n"
"s_or_b64 vcc, vcc, s[0:1]\n"
"s_waitcnt lgkmcnt(0)\n"
"s_nop 7\n"
"s_cbranch_vccnz Rw3A%=\n"
"s_branch Cw3A%=\n"
"Rw3B%=:\n"
"s_nop 7\n"
"s_cmp_eq_u32 s70, 0\n"
"s_cbranch_scc1 Cw3B%=\n"
"s_sub_u32 s70, s70, 1\n"
"s_waitcnt vmcnt(0) lgkmcnt(0)\n"
"buffer_load_dwordx4 v[184:187], v1, s[20:23], s48 offen sc1\n"
"s_add_u32 s0, s48, 0x8000\n"
"s_add_u32 s1, s51, 0x100000\n"
"buffer_load_dwordx4 v[188:191], v1, s[20:23], s48 offen offset:1024 sc1\n"
"buffer_load_dwordx4 v[200:203], v1, s[20:23], s48 offen offset:2048 sc1\n"
"buffer_load_dwordx4 a[140:143], v1, s[20:23], s48 offen offset:3072 sc1\n"
"buffer_load_dwordx4 v[192:195], v1, s[20:23], s0 offen sc1\n"
"buffer_load_dwordx4 v[196:199], v1, s[20:23], s0 offen offset:1024 sc1\n"
"buffer_load_dwordx4 v[204:207], v1, s[20:23], s0 offen offset:2048 sc1\n"
"buffer_load_dwordx4 a[144:147], v1, s[20:23], s0 offen offset:3072 sc1\n"
"buffer_load_dwordx4 v[60:63], v1, s[20:23], s1 offen sc1\n"
"s_waitcnt vmcnt(8)\n"
"v_mfma_f32_16x16x32_bf16 v[88:91], a[72:75], v[184:187], 0\n"
"ds_write_b128 v9, v[184:187] offset:32768\n"
"v_mfma_f32_16x16x32_bf16 v[92:95], a[48:51], v[184:187], 0\n"
"v_mfma_f32_16x16x32_bf16 v[96:99], a[16:19], v[184:187], 0\n"
"s_waitcnt vmcnt(7)\n"
"v_mfma_f32_16x16x32_bf16 v[88:91], a[40:43], v[188:191], v[88:91]\n"
"ds_write_b128 v9, v[188:191] offset:33792\n"
"v_mfma_f32_16x16x32_bf16 v[92:95], a[32:35], v[188:191], v[92:95]\n"
"v_mfma_f32_16x16x32_bf16 v[96:99], a[20:23], v[188:191], v[96:99]\n"
"s_waitcnt vmcnt(6)\n"
"v_mfma_f32_16x16x32_bf16 v[88:91], a[0:3], v[200:203], v[88:91]\n"
"ds_write_b128 v9, v[200:203] offset:34816\n"
"v_mfma_f32_16x16x32_bf16 v[92:95], a[4:7], v[200:203], v[92:95]\n"
"v_mfma_f32_16x16x32_bf16 v[96:99], a[24:27], v[200:203], v[96:99]\n"
"s_waitcnt vmcnt(5)\n"
"v_mfma_f32_16x16x32_bf16 v[88:91], a[8:11], a[140:143], v[88:91]\n"
"ds_write_b128 v9, a[140:143] offset:35840\n"
"v_mfma_f32_16x16x32_bf16 v[92:95], a[12:15], a[140:143], v[92:95]\n"
"v_mfma_f32_16x16x32_bf16 v[96:99], a[28:31], a[140:143], v[96:99]\n"
"s_waitcnt vmcnt(4)\n"
"v_mfma_f32_16x16x32_bf16 v[76:79], a[72:75], v[192:195], 0\n"
"ds_write_b128 v9, v[192:195] offset:49152\n"
"v_mfma_f32_16x16x32_bf16 v[80:83], a[48:51], v[192:195], 0\n"
"v_mfma_f32_16x16x32_bf16 v[84:87], a[16:19], v[192:195], 0\n"
"s_waitcnt vmcnt(3)\n"
"v_mfma_f32_16x16x32_bf16 v[76:79], a[40:43], v[196:199], v[76:79]\n"
"ds_write_b128 v9, v[196:199] offset:50176\n"
"v_mfma_f32_16x16x32_bf16 v[80:83], a[32:35], v[196:199], v[80:83]\n"
"v_mfma_f32_16x16x32_bf16 v[84:87], a[20:23], v[196:199], v[84:87]\n"
"s_waitcnt vmcnt(2)\n"
"v_mfma_f32_16x16x32_bf16 v[76:79], a[0:3], v[204:207], v[76:79]\n"
"ds_write_b128 v9, v[204:207] offset:51200\n"
"v_mfma_f32_16x16x32_bf16 v[80:83], a[4:7], v[204:207], v[80:83]\n"
"v_mfma_f32_16x16x32_bf16 v[84:87], a[24:27], v[204:207], v[84:87]\n"
"s_waitcnt vmcnt(0)\n"
"v_add_f32 v40, v60, v62\n"
"v_mfma_f32_16x16x32_bf16 v[76:79], a[8:11], a[144:147], v[76:79]\n"
"v_mov_b32 v43, v40\n"
"v_add_f32 v0, v61, v63\n"
"v_mfma_f32_16x16x32_bf16 v[80:83], a[12:15], a[144:147], v[80:83]\n"
"v_permlane16_swap_b32 v40, v43\n"
"v_add_f32 v42, v40, v43\n"
"v_mfma_f32_16x16x32_bf16 v[84:87], a[28:31], a[144:147], v[84:87]\n"
"v_mov_b32 v40, v42\n"
"ds_write_b128 v9, a[144:147] offset:52224\n"
"v_cmp_u_f32 vcc, v88, v76\n"
"v_permlane32_swap_b32 v42, v40\n"
"v_add_f32 v64, v42, v40\n"
"v_mul_f32 v42, 0x3e000000, v64\n"
"v_sub_f32 v40, v60, v42\n"
"v_sub_f32 v64, v62, v42\n"
"v_mul_f32 v60, v40, v40\n"
"v_fmac_f32 v60, v64, v64\n"
"v_fmamk_f32 v40, v60, 0x42000000, v0\n"
"v_mov_b32 v60, v40\n"
"s_nop 1\n"
"v_permlane16_swap_b32 v40, v60\n"
"v_add_f32 v0, v40, v60\n"
"v_mov_b32 v40, v0\n"
"s_nop 1\n"
"v_permlane32_swap_b32 v0, v40\n"
"v_add_f32 v43, v0, v40\n"
"v_cmp_u_f32 s[0:1], v43, v43\n"
"ds_write_b64 v17, v[42:43] offset:512\n"
"s_or_b64 vcc, vcc, s[0:1]\n"
"s_waitcnt lgkmcnt(0)\n"
"s_nop 7\n"
"s_cbranch_vccnz Rw3B%=\n"
"s_branch Cw3B%=\n"
"Rw4A%=:\n"
"s_nop 7\n"
"s_cmp_eq_u32 s70, 0\n"
"s_cbranch_scc1 Cw4A%=\n"
"s_sub_u32 s70, s70, 1\n"
"s_waitcnt vmcnt(0) lgkmcnt(0)\n"
"buffer_load_dwordx4 a[152:155], v1, s[4:7], s57 offen sc1\n"
"s_add_u32 s1, s57, 0x400000\n"
"s_add_u32 s0, s56, 0x400000\n"
"ds_read_b128 a[60:63], v28 offset:2048\n"
"v_lshlrev_b32 v60, 16, v48\n"
"v_and_b32 v61, s72, v48\n"
"v_lshlrev_b32 v62, 16, v49\n"
"ds_read_b128 a[120:123], v28\n"
"v_and_b32 v63, s72, v49\n"
"v_lshlrev_b32 v64, 16, v50\n"
"v_and_b32 v65, s72, v50\n"
"ds_read_b128 a[244:247], v28 offset:1024\n"
"v_lshlrev_b32 v66, 16, v51\n"
"v_and_b32 v67, s72, v51\n"
"buffer_load_dwordx4 a[148:151], v1, s[4:7], s56 offen sc1\n"
"ds_read_b128 a[68:71], v28 offset:3072\n"
"v_lshlrev_b32 v72, 16, v56\n"
"v_and_b32 v73, s72, v56\n"
"v_lshlrev_b32 v74, 16, v57\n"
"v_and_b32 v75, s72, v57\n"
"v_lshlrev_b32 v68, 16, v58\n"
"v_and_b32 v69, s72, v58\n"
"v_lshlrev_b32 v70, 16, v59\n"
"v_and_b32 v71, s72, v59\n"
"v_add_f32 v60, v60, v72\n"
"v_add_f32 v61, v61, v73\n"
"v_add_f32 v62, v62, v74\n"
"v_add_f32 v63, v63, v75\n"
"buffer_load_dwordx4 a[160:163], v1, s[4:7], s57 offen offset:1024 sc1\n"
"v_add_f32 v64, v64, v68\n"
"v_add_f32 v65, v65, v69\n"
"v_add_f32 v66, v66, v70\n"
"v_add_f32 v67, v67, v71\n"
"ds_read_b128 a[248:251], v28 offset:6144\n"
"ds_read_b128 a[252:255], v28 offset:7168\n"
"ds_read_b128 v[252:255], v28 offset:4096\n"
"buffer_load_dwordx4 a[156:159], v1, s[4:7], s56 offen offset:1024 sc1\n"
"ds_read_b128 v[248:251], v28 offset:5120\n"
"buffer_load_dwordx4 a[36:39], v1, s[4:7], s57 offen offset:2048 sc1\n"
"buffer_load_dwordx4 a[76:79], v1, s[4:7], s56 offen offset:2048 sc1\n"
"buffer_load_dwordx4 a[80:83], v1, s[4:7], s57 offen offset:3072 sc1\n"
"buffer_load_dwordx4 a[96:99], v1, s[4:7], s56 offen offset:3072 sc1\n"
"buffer_load_dwordx4 a[88:91], v2, s[4:7], s57 offen sc1\n"
"buffer_load_dwordx4 a[112:115], v2, s[4:7], s56 offen sc1\n"
"buffer_load_dwordx4 a[44:47], v2, s[4:7], s57 offen offset:1024 sc1\n"
"buffer_load_dwordx4 a[104:107], v2, s[4:7], s57 offen offset:2048 sc1\n"
"buffer_load_dwordx4 a[52:55], v2, s[4:7], s57 offen offset:3072 sc1\n"
"buffer_load_dwordx4 v[76:79], v1, s[4:7], s58 offen sc1\n"
"buffer_load_dwordx4 v[84:87], v1, s[4:7], s58 offen offset:1024 sc1\n"
"buffer_load_dwordx4 v[80:83], v1, s[4:7], s58 offen offset:2048 sc1\n"
"s_waitcnt vmcnt(15) lgkmcnt(7)\n"
"v_mfma_f32_16x16x32_bf16 v[68:71], a[60:63], a[152:155], 0\n"
"s_waitcnt lgkmcnt(4)\n"
"v_mfma_f32_16x16x32_bf16 a[240:243], a[68:71], a[152:155], 0\n"
"buffer_load_dwordx4 v[88:91], v1, s[4:7], s58 offen offset:3072 sc1\n"
"v_mfma_f32_16x16x32_bf16 a[132:135], a[120:123], a[152:155], 0\n"
"v_mfma_f32_16x16x32_bf16 a[136:139], a[244:247], a[152:155], 0\n"
"s_waitcnt vmcnt(15)\n"
"v_mfma_f32_16x16x32_bf16 v[72:75], a[60:63], a[148:151], 0\n"
"ds_read_b128 v[244:247], v28 offset:10240\n"
"buffer_load_dwordx4 v[92:95], v7, s[4:7], s58 offen sc1\n"
"v_mfma_f32_16x16x32_bf16 v[60:63], a[120:123], a[148:151], v[60:63]\n"
"ds_read_b128 v[240:243], v28 offset:8192\n"
"v_mfma_f32_16x16x32_bf16 v[64:67], a[244:247], a[148:151], v[64:67]\n"
"ds_read_b128 a[244:247], v28 offset:9216\n"
"v_mfma_f32_16x16x32_bf16 a[152:155], a[68:71], a[148:151], 0\n"
"ds_read_b128 a[148:151], v28 offset:11264\n"
"s_waitcnt vmcnt(15) lgkmcnt(7)\n"
"v_mfma_f32_16x16x32_bf16 v[68:71], a[248:251], a[160:163], v[68:71]\n"
"buffer_load_dwordx4 v[96:99], v7, s[4:7], s58 offen offset:1024 sc1\n"
"s_waitcnt lgkmcnt(6)\n"
"v_mfma_f32_16x16x32_bf16 a[240:243], a[252:255], a[160:163], a[240:243]\n"
"s_waitcnt lgkmcnt(5)\n"
"v_mfma_f32_16x16x32_bf16 a[132:135], v[252:255], a[160:163], a[132:135]\n"
"s_waitcnt lgkmcnt(4)\n"
"v_mfma_f32_16x16x32_bf16 a[136:139], v[248:251], a[160:163], a[136:139]\n"
"buffer_load_dwordx4 a[120:123], v2, s[4:7], s56 offen offset:1024 sc1\n"
"s_waitcnt vmcnt(16)\n"
"v_mfma_f32_16x16x32_bf16 v[72:75], a[248:251], a[156:159], v[72:75]\n"
"ds_read_b128 a[248:251], v28 offset:14336\n"
"v_mfma_f32_16x16x32_bf16 a[152:155], a[252:255], a[156:159], a[152:155]\n"
"ds_read_b128 a[252:255], v28 offset:15360\n"
"v_mfma_f32_16x16x32_bf16 v[60:63], v[252:255], a[156:159], v[60:63]\n"
"ds_read_b128 v[252:255], v28 offset:12288\n"
"v_mfma_f32_16x16x32_bf16 v[64:67], v[248:251], a[156:159], v[64:67]\n"
"buffer_load_dwordx4 a[60:63], v2, s[4:7], s56 offen offset:2048 sc1\n"
"s_waitcnt vmcnt(16) lgkmcnt(6)\n"
"v_mfma_f32_16x16x32_bf16 v[68:71], v[244:247], a[36:39], v[68:71]\n"
"ds_read_b128 a[156:159], v28 offset:13312\n"
"s_waitcnt lgkmcnt(4)\n"
"v_mfma_f32_16x16x32_bf16 a[240:243], a[148:151], a[36:39], a[240:243]\n"
"v_mfma_f32_16x16x32_bf16 a[132:135], v[240:243], a[36:39], a[132:135]\n"
"v_mfma_f32_16x16x32_bf16 a[136:139], a[244:247], a[36:39], a[136:139]\n"
"buffer_load_dwordx4 a[68:71], v2, s[4:7], s56 offen offset:3072 sc1\n"
"s_waitcnt vmcnt(16)\n"
"v_mfma_f32_16x16x32_bf16 v[72:75], v[244:247], a[76:79], v[72:75]\n"
"ds_read_b128 v[244:247], v28 offset:18432\n"
"v_mfma_f32_16x16x32_bf16 v[60:63], v[240:243], a[76:79], v[60:63]\n"
"ds_read_b128 v[240:243], v28 offset:16384\n"
"v_mfma_f32_16x16x32_bf16 v[64:67], a[244:247], a[76:79], v[64:67]\n"
"ds_read_b128 a[244:247], v28 offset:17408\n"
"v_mfma_f32_16x16x32_bf16 a[152:155], a[148:151], a[76:79], a[152:155]\n"
"buffer_load_dwordx4 v[104:107], v1, s[4:7], s1 offen sc1\n"
"s_waitcnt vmcnt(16) lgkmcnt(6)\n"
"v_mfma_f32_16x16x32_bf16 v[68:71], a[248:251], a[80:83], v[68:71]\n"
"s_waitcnt lgkmcnt(5)\n"
"v_mfma_f32_16x16x32_bf16 a[240:243], a[252:255], a[80:83], a[240:243]\n"
"s_waitcnt lgkmcnt(4)\n"
"v_mfma_f32_16x16x32_bf16 a[132:135], v[252:255], a[80:83], a[132:135]\n"
"buffer_load_dwordx4 a[76:79], v1, s[4:7], s0 offen sc1\n"
"s_waitcnt lgkmcnt(3)\n"
"v_mfma_f32_16x16x32_bf16 a[136:139], a[156:159], a[80:83], a[136:139]\n"
"s_waitcnt vmcnt(16)\n"
"v_mfma_f32_16x16x32_bf16 v[72:75], a[248:251], a[96:99], v[72:75]\n"
"v_mfma_f32_16x16x32_bf16 a[152:155], a[252:255], a[96:99], a[152:155]\n"
"v_mfma_f32_16x16x32_bf16 v[60:63], v[252:255], a[96:99], v[60:63]\n"
"buffer_load_dwordx4 v[112:115], v1, s[4:7], s1 offen offset:1024 sc1\n"
"v_mfma_f32_16x16x32_bf16 v[64:67], a[156:159], a[96:99], v[64:67]\n"
"s_waitcnt vmcnt(16) lgkmcnt(2)\n"
"v_mfma_f32_16x16x32_bf16 v[68:71], v[244:247], a[88:91], v[68:71]\n"
"v_mfma_f32_16x16x32_bf16 a[240:243], a[124:127], a[88:91], a[240:243]\n"
"buffer_load_dwordx4 v[108:111], v1, s[4:7], s0 offen offset:1024 sc1\n"
"s_waitcnt lgkmcnt(1)\n"
"v_mfma_f32_16x16x32_bf16 a[132:135], v[240:243], a[88:91], a[132:135]\n"
"s_waitcnt lgkmcnt(0)\n"
"v_mfma_f32_16x16x32_bf16 a[136:139], a[244:247], a[88:91], a[136:139]\n"
"s_waitcnt vmcnt(16)\n"
"v_mfma_f32_16x16x32_bf16 v[72:75], v[244:247], a[112:115], v[72:75]\n"
"v_mfma_f32_16x16x32_bf16 a[152:155], a[124:127], a[112:115], a[152:155]\n"
"buffer_load_dwordx4 v[100:103], v1, s[4:7], s1 offen offset:2048 sc1\n"
"v_mfma_f32_16x16x32_bf16 v[60:63], v[240:243], a[112:115], v[60:63]\n"
"v_mfma_f32_16x16x32_bf16 v[64:67], a[244:247], a[112:115], v[64:67]\n"
"s_waitcnt vmcnt(16)\n"
"v_mfma_f32_16x16x32_bf16 v[68:71], a[100:103], a[44:47], v[68:71]\n"
"buffer_load_dwordx4 v[152:155], v1, s[4:7], s0 offen offset:2048 sc1\n"
"v_mfma_f32_16x16x32_bf16 a[240:243], a[92:95], a[44:47], a[240:243]\n"
"v_mfma_f32_16x16x32_bf16 a[132:135], a[116:119], a[44:47], a[132:135]\n"
"v_mfma_f32_16x16x32_bf16 a[136:139], a[108:111], a[44:47], a[136:139]\n"
"s_waitcnt vmcnt(16)\n"
"v_mfma_f32_16x16x32_bf16 v[68:71], a[168:171], a[104:107], v[68:71]\n"
"buffer_load_dwordx4 v[124:127], v1, s[4:7], s1 offen offset:3072 sc1\n"
"v_mfma_f32_16x16x32_bf16 a[240:243], a[172:175], a[104:107], a[240:243]\n"
"v_mfma_f32_16x16x32_bf16 a[132:135], a[84:87], a[104:107], a[132:135]\n"
"v_mfma_f32_16x16x32_bf16 a[136:139], a[164:167], a[104:107], a[136:139]\n"
"buffer_load_dwordx4 v[148:151], v1, s[4:7], s0 offen offset:3072 sc1\n"
"s_waitcnt vmcnt(17)\n"
"v_mfma_f32_16x16x32_bf16 v[68:71], a[184:187], a[52:55], v[68:71]\n"
"v_mfma_f32_16x16x32_bf16 a[240:243], a[188:191], a[52:55], a[240:243]\n"
"v_mfma_f32_16x16x32_bf16 a[132:135], a[176:179], a[52:55], a[132:135]\n"
"v_mfma_f32_16x16x32_bf16 a[136:139], a[180:183], a[52:55], a[136:139]\n"
"s_waitcnt vmcnt(15)\n"
"v_swap_b32 v78, v84\n"
"v_swap_b32 v79, v85\n"
"s_nop 1\n"
"v_mfma_f32_16x16x32_bf16 v[72:75], a[200:203], v[76:79], v[72:75]\n"
"v_mfma_f32_16x16x32_bf16 a[152:155], a[204:207], v[76:79], a[152:155]\n"
"v_mfma_f32_16x16x32_bf16 v[60:63], a[192:195], v[76:79], v[60:63]\n"
"v_mfma_f32_16x16x32_bf16 v[64:67], a[196:199], v[76:79], v[64:67]\n"
"v_mfma_f32_16x16x32_bf16 v[68:71], a[200:203], v[84:87], v[68:71]\n"
"v_mfma_f32_16x16x32_bf16 a[240:243], a[204:207], v[84:87], a[240:243]\n"
"v_mfma_f32_16x16x32_bf16 a[132:135], a[192:195], v[84:87], a[132:135]\n"
"v_mfma_f32_16x16x32_bf16 a[136:139], a[196:199], v[84:87], a[136:139]\n"
"s_waitcnt vmcnt(13)\n"
"v_swap_b32 v82, v88\n"
"v_swap_b32 v83, v89\n"
"s_nop 1\n"
"v_mfma_f32_16x16x32_bf16 v[72:75], a[216:219], v[80:83], v[72:75]\n"
"v_mfma_f32_16x16x32_bf16 a[152:155], a[220:223], v[80:83], a[152:155]\n"
"v_mfma_f32_16x16x32_bf16 v[60:63], a[208:211], v[80:83], v[60:63]\n"
"v_mfma_f32_16x16x32_bf16 v[64:67], a[212:215], v[80:83], v[64:67]\n"
"v_mfma_f32_16x16x32_bf16 v[68:71], a[216:219], v[88:91], v[68:71]\n"
"v_mfma_f32_16x16x32_bf16 a[240:243], a[220:223], v[88:91], a[240:243]\n"
"v_mfma_f32_16x16x32_bf16 a[132:135], a[208:211], v[88:91], a[132:135]\n"
"v_mfma_f32_16x16x32_bf16 a[136:139], a[212:215], v[88:91], a[136:139]\n"
"s_waitcnt vmcnt(11)\n"
"v_swap_b32 v94, v96\n"
"v_swap_b32 v95, v97\n"
"s_nop 1\n"
"v_mfma_f32_16x16x32_bf16 v[72:75], a[232:235], v[92:95], v[72:75]\n"
"v_mfma_f32_16x16x32_bf16 a[152:155], a[236:239], v[92:95], a[152:155]\n"
"v_mfma_f32_16x16x32_bf16 v[60:63], a[224:227], v[92:95], v[60:63]\n"
"v_mfma_f32_16x16x32_bf16 v[64:67], a[228:231], v[92:95], v[64:67]\n"
"s_waitcnt vmcnt(10)\n"
"v_mfma_f32_16x16x32_bf16 v[72:75], a[100:103], a[120:123], v[72:75]\n"
"v_mfma_f32_16x16x32_bf16 a[152:155], a[92:95], a[120:123], a[152:155]\n"
"v_mfma_f32_16x16x32_bf16 v[68:71], a[232:235], v[96:99], v[68:71]\n"
"v_mfma_f32_16x16x32_bf16 a[240:243], a[236:239], v[96:99], a[240:243]\n"
"v_mfma_f32_16x16x32_bf16 a[132:135], a[224:227], v[96:99], a[132:135]\n"
"s_waitcnt vmcnt(9)\n"
"v_mfma_f32_16x16x32_bf16 v[72:75], a[168:171], a[60:63], v[72:75]\n"
"v_mfma_f32_16x16x32_bf16 a[152:155], a[172:175], a[60:63], a[152:155]\n"
"v_mfma_f32_16x16x32_bf16 a[136:139], a[228:231], v[96:99], a[136:139]\n"
"v_mfma_f32_16x16x32_bf16 v[60:63], a[116:119], a[120:123], v[60:63]\n"
"v_mfma_f32_16x16x32_bf16 v[64:67], a[108:111], a[120:123], v[64:67]\n"
"ds_write_b128 v15, v[68:71]\n"
"s_waitcnt vmcnt(8)\n"
"v_mfma_f32_16x16x32_bf16 v[72:75], a[184:187], a[68:71], v[72:75]\n"
"ds_write_b128 v15, a[240:243] offset:1024\n"
"v_mfma_f32_16x16x32_bf16 a[152:155], a[188:191], a[68:71], a[152:155]\n"
"ds_write_b128 v14, a[132:135]\n"
"v_mfma_f32_16x16x32_bf16 v[60:63], a[84:87], a[60:63], v[60:63]\n"
"ds_write_b128 v14, a[136:139] offset:1024\n"
"v_mfma_f32_16x16x32_bf16 v[64:67], a[164:167], a[60:63], v[64:67]\n"
"v_mfma_f32_16x16x32_bf16 v[60:63], a[176:179], a[68:71], v[60:63]\n"
"s_nop 0\n"
"ds_write_b128 v13, v[72:75]\n"
"v_mfma_f32_16x16x32_bf16 v[64:67], a[180:183], a[68:71], v[64:67]\n"
"v_cmp_u_f32 vcc, v68, v72\n"
"ds_write_b128 v13, a[152:155] offset:1024\n"
"s_waitcnt lgkmcnt(0)\n"
"s_nop 7\n"
"s_cbranch_vccnz Rw4A%=\n"
"s_branch Cw4A%=\n"
"Rw4B%=:\n"
"s_nop 7\n"
"s_cmp_eq_u32 s70, 0\n"
"s_cbranch_scc1 Cw4B%=\n"
"s_sub_u32 s70, s70, 1\n"
"s_waitcnt vmcnt(0) lgkmcnt(0)\n"
"s_add_u32 s33, s57, 0x400000\n"
"buffer_load_dwordx4 v[104:107], v1, s[4:7], s33 offen sc1\n"
"s_add_u32 s3, s56, 0x400000\n"
"s_add_u32 s32, s58, 0x100000\n"
"s_add_u32 s2, s48, 0x1000000\n"
"s_add_u32 s1, s48, 0x1008000\n"
"s_add_u32 s0, s51, 0x1100000\n"
"ds_read_b128 a[152:155], v28 offset:2048\n"
"v_lshlrev_b32 v56, 16, v44\n"
"v_and_b32 v57, s72, v44\n"
"v_lshlrev_b32 v58, 16, v45\n"
"ds_read_b128 a[132:135], v28\n"
"v_and_b32 v59, s72, v45\n"
"v_lshlrev_b32 v68, 16, v46\n"
"v_and_b32 v69, s72, v46\n"
"buffer_load_dwordx4 a[76:79], v1, s[4:7], s3 offen sc1\n"
"ds_read_b128 a[120:123], v28 offset:1024\n"
"v_lshlrev_b32 v70, 16, v47\n"
"v_and_b32 v71, s72, v47\n"
"v_lshlrev_b32 v60, 16, v52\n"
"ds_read_b128 a[136:139], v28 offset:3072\n"
"v_and_b32 v61, s72, v52\n"
"v_lshlrev_b32 v62, 16, v53\n"
"v_and_b32 v63, s72, v53\n"
"v_lshlrev_b32 v64, 16, v54\n"
"v_and_b32 v65, s72, v54\n"
"v_lshlrev_b32 v66, 16, v55\n"
"v_and_b32 v67, s72, v55\n"
"v_add_f32 v56, v56, v60\n"
"buffer_load_dwordx4 v[112:115], v1, s[4:7], s33 offen offset:1024 sc1\n"
"v_add_f32 v57, v57, v61\n"
"v_add_f32 v58, v58, v62\n"
"v_add_f32 v59, v59, v63\n"
"v_add_f32 v68, v68, v64\n"
"v_add_f32 v69, v69, v65\n"
"v_add_f32 v70, v70, v66\n"
"v_add_f32 v71, v71, v67\n"
"ds_read_b128 v[60:63], v28 offset:6144\n"
"ds_read_b128 a[52:55], v28 offset:7168\n"
"buffer_load_dwordx4 v[108:111], v1, s[4:7], s3 offen offset:1024 sc1\n"
"ds_read_b128 a[104:107], v28 offset:4096\n"
"ds_read_b128 a[44:47], v28 offset:5120\n"
"buffer_load_dwordx4 v[100:103], v1, s[4:7], s33 offen offset:2048 sc1\n"
"buffer_load_dwordx4 v[152:155], v1, s[4:7], s3 offen offset:2048 sc1\n"
"buffer_load_dwordx4 v[124:127], v1, s[4:7], s33 offen offset:3072 sc1\n"
"buffer_load_dwordx4 v[148:151], v1, s[4:7], s3 offen offset:3072 sc1\n"
"buffer_load_dwordx4 v[132:135], v2, s[4:7], s33 offen sc1\n"
"buffer_load_dwordx4 v[136:139], v2, s[4:7], s3 offen sc1\n"
"buffer_load_dwordx4 v[144:147], v2, s[4:7], s33 offen offset:1024 sc1\n"
"buffer_load_dwordx4 v[120:123], v2, s[4:7], s33 offen offset:2048 sc1\n"
"buffer_load_dwordx4 v[140:143], v2, s[4:7], s33 offen offset:3072 sc1\n"
"buffer_load_dwordx4 v[84:87], v1, s[4:7], s32 offen sc1\n"
"buffer_load_dwordx4 v[88:91], v1, s[4:7], s32 offen offset:1024 sc1\n"
"buffer_load_dwordx4 v[80:83], v1, s[4:7], s32 offen offset:2048 sc1\n"
"s_waitcnt vmcnt(15) lgkmcnt(7)\n"
"v_mfma_f32_16x16x32_bf16 v[92:95], a[152:155], v[104:107], 0\n"
"s_waitcnt lgkmcnt(4)\n"
"v_mfma_f32_16x16x32_bf16 a[60:63], a[136:139], v[104:107], 0\n"
"buffer_load_dwordx4 v[76:79], v1, s[4:7], s32 offen offset:3072 sc1\n"
"v_mfma_f32_16x16x32_bf16 a[68:71], a[132:135], v[104:107], 0\n"
"v_mfma_f32_16x16x32_bf16 a[240:243], a[120:123], v[104:107], 0\n"
"s_waitcnt vmcnt(15)\n"
"v_mfma_f32_16x16x32_bf16 v[96:99], a[152:155], a[76:79], 0\n"
"ds_read_b128 a[152:155], v28 offset:10240\n"
"buffer_load_dwordx4 v[72:75], v7, s[4:7], s32 offen sc1\n"
"v_mfma_f32_16x16x32_bf16 v[56:59], a[132:135], a[76:79], v[56:59]\n"
"ds_read_b128 a[132:135], v28 offset:8192\n"
"v_mfma_f32_16x16x32_bf16 v[68:71], a[120:123], a[76:79], v[68:71]\n"
"ds_read_b128 a[120:123], v28 offset:9216\n"
"v_mfma_f32_16x16x32_bf16 v[104:107], a[136:139], a[76:79], 0\n"
"ds_read_b128 a[76:79], v28 offset:11264\n"
"s_waitcnt vmcnt(15) lgkmcnt(7)\n"
"v_mfma_f32_16x16x32_bf16 v[92:95], v[60:63], v[112:115], v[92:95]\n"
"buffer_load_dwordx4 v[64:67], v7, s[4:7], s32 offen offset:1024 sc1\n"
"s_waitcnt lgkmcnt(6)\n"
"v_mfma_f32_16x16x32_bf16 a[60:63], a[52:55], v[112:115], a[60:63]\n"
"s_waitcnt lgkmcnt(5)\n"
"v_mfma_f32_16x16x32_bf16 a[68:71], a[104:107], v[112:115], a[68:71]\n"
"s_waitcnt lgkmcnt(4)\n"
"v_mfma_f32_16x16x32_bf16 a[240:243], a[44:47], v[112:115], a[240:243]\n"
"buffer_load_dwordx4 v[112:115], v2, s[4:7], s3 offen offset:1024 sc1\n"
"s_waitcnt vmcnt(16)\n"
"v_mfma_f32_16x16x32_bf16 v[96:99], v[60:63], v[108:111], v[96:99]\n"
"ds_read_b128 v[60:63], v28 offset:14336\n"
"v_mfma_f32_16x16x32_bf16 v[104:107], a[52:55], v[108:111], v[104:107]\n"
"ds_read_b128 a[52:55], v28 offset:15360\n"
"v_mfma_f32_16x16x32_bf16 v[56:59], a[104:107], v[108:111], v[56:59]\n"
"ds_read_b128 a[104:107], v28 offset:12288\n"
"v_mfma_f32_16x16x32_bf16 v[68:71], a[44:47], v[108:111], v[68:71]\n"
"buffer_load_dwordx4 v[108:111], v2, s[4:7], s3 offen offset:2048 sc1\n"
"s_waitcnt vmcnt(16) lgkmcnt(6)\n"
"v_mfma_f32_16x16x32_bf16 v[92:95], a[152:155], v[100:103], v[92:95]\n"
"ds_read_b128 a[44:47], v28 offset:13312\n"
"s_waitcnt lgkmcnt(4)\n"
"v_mfma_f32_16x16x32_bf16 a[60:63], a[76:79], v[100:103], a[60:63]\n"
"v_mfma_f32_16x16x32_bf16 a[68:71], a[132:135], v[100:103], a[68:71]\n"
"v_mfma_f32_16x16x32_bf16 a[240:243], a[120:123], v[100:103], a[240:243]\n"
"buffer_load_dwordx4 v[100:103], v2, s[4:7], s3 offen offset:3072 sc1\n"
"s_waitcnt vmcnt(16)\n"
"v_mfma_f32_16x16x32_bf16 v[96:99], a[152:155], v[152:155], v[96:99]\n"
"ds_read_b128 a[152:155], v28 offset:18432\n"
"v_mfma_f32_16x16x32_bf16 v[56:59], a[132:135], v[152:155], v[56:59]\n"
"ds_read_b128 a[112:115], v28 offset:16384\n"
"v_mfma_f32_16x16x32_bf16 v[68:71], a[120:123], v[152:155], v[68:71]\n"
"ds_read_b128 a[120:123], v28 offset:17408\n"
"v_mfma_f32_16x16x32_bf16 v[104:107], a[76:79], v[152:155], v[104:107]\n"
"buffer_load_dwordx4 a[132:135], v1, s[16:19], s2 offen sc1\n"
"s_waitcnt vmcnt(16) lgkmcnt(6)\n"
"v_mfma_f32_16x16x32_bf16 v[92:95], v[60:63], v[124:127], v[92:95]\n"
"s_waitcnt lgkmcnt(5)\n"
"v_mfma_f32_16x16x32_bf16 a[60:63], a[52:55], v[124:127], a[60:63]\n"
"s_waitcnt lgkmcnt(4)\n"
"v_mfma_f32_16x16x32_bf16 a[68:71], a[104:107], v[124:127], a[68:71]\n"
"buffer_load_dwordx4 a[136:139], v1, s[16:19], s2 offen offset:1024 sc1\n"
"s_waitcnt lgkmcnt(3)\n"
"v_mfma_f32_16x16x32_bf16 a[240:243], a[44:47], v[124:127], a[240:243]\n"
"s_waitcnt vmcnt(16)\n"
"v_mfma_f32_16x16x32_bf16 v[96:99], v[60:63], v[148:151], v[96:99]\n"
"v_mfma_f32_16x16x32_bf16 v[104:107], a[52:55], v[148:151], v[104:107]\n"
"v_mfma_f32_16x16x32_bf16 v[56:59], a[104:107], v[148:151], v[56:59]\n"
"buffer_load_dwordx4 a[140:143], v1, s[16:19], s2 offen offset:2048 sc1\n"
"v_mfma_f32_16x16x32_bf16 v[68:71], a[44:47], v[148:151], v[68:71]\n"
"s_waitcnt vmcnt(16) lgkmcnt(2)\n"
"v_mfma_f32_16x16x32_bf16 v[92:95], a[152:155], v[132:135], v[92:95]\n"
"v_mfma_f32_16x16x32_bf16 a[60:63], a[124:127], v[132:135], a[60:63]\n"
"buffer_load_dwordx4 a[144:147], v1, s[16:19], s2 offen offset:3072 sc1\n"
"s_waitcnt lgkmcnt(1)\n"
"v_mfma_f32_16x16x32_bf16 a[68:71], a[112:115], v[132:135], a[68:71]\n"
"s_waitcnt lgkmcnt(0)\n"
"v_mfma_f32_16x16x32_bf16 a[240:243], a[120:123], v[132:135], a[240:243]\n"
"s_waitcnt vmcnt(16)\n"
"v_mfma_f32_16x16x32_bf16 v[96:99], a[152:155], v[136:139], v[96:99]\n"
"v_mfma_f32_16x16x32_bf16 v[104:107], a[124:127], v[136:139], v[104:107]\n"
"buffer_load_dwordx4 a[148:151], v1, s[16:19], s1 offen sc1\n"
"v_mfma_f32_16x16x32_bf16 v[56:59], a[112:115], v[136:139], v[56:59]\n"
"v_mfma_f32_16x16x32_bf16 v[68:71], a[120:123], v[136:139], v[68:71]\n"
"s_waitcnt vmcnt(16)\n"
"v_mfma_f32_16x16x32_bf16 v[92:95], a[100:103], v[144:147], v[92:95]\n"
"buffer_load_dwordx4 a[152:155], v1, s[16:19], s1 offen offset:1024 sc1\n"
"v_mfma_f32_16x16x32_bf16 a[60:63], a[92:95], v[144:147], a[60:63]\n"
"v_mfma_f32_16x16x32_bf16 a[68:71], a[116:119], v[144:147], a[68:71]\n"
"v_mfma_f32_16x16x32_bf16 a[240:243], a[108:111], v[144:147], a[240:243]\n"
"s_waitcnt vmcnt(16)\n"
"v_mfma_f32_16x16x32_bf16 v[92:95], a[168:171], v[120:123], v[92:95]\n"
"buffer_load_dwordx4 a[156:159], v1, s[16:19], s1 offen offset:2048 sc1\n"
"v_mfma_f32_16x16x32_bf16 a[60:63], a[172:175], v[120:123], a[60:63]\n"
"v_mfma_f32_16x16x32_bf16 a[68:71], a[84:87], v[120:123], a[68:71]\n"
"v_mfma_f32_16x16x32_bf16 a[240:243], a[164:167], v[120:123], a[240:243]\n"
"buffer_load_dwordx4 a[160:163], v1, s[16:19], s1 offen offset:3072 sc1\n"
"s_waitcnt vmcnt(17)\n"
"v_mfma_f32_16x16x32_bf16 v[92:95], a[184:187], v[140:143], v[92:95]\n"
"v_mfma_f32_16x16x32_bf16 a[60:63], a[188:191], v[140:143], a[60:63]\n"
"v_mfma_f32_16x16x32_bf16 a[68:71], a[176:179], v[140:143], a[68:71]\n"
"v_mfma_f32_16x16x32_bf16 a[240:243], a[180:183], v[140:143], a[240:243]\n"
"buffer_load_dwordx4 v[60:63], v1, s[16:19], s0 offen sc1\n"
"buffer_load_dwordx4 a[80:83], v2, s[16:19], s37 offen\n"
"s_waitcnt vmcnt(17)\n"
"v_swap_b32 v86, v88\n"
"v_swap_b32 v87, v89\n"
"s_nop 1\n"
"v_mfma_f32_16x16x32_bf16 v[96:99], a[200:203], v[84:87], v[96:99]\n"
"v_mfma_f32_16x16x32_bf16 v[104:107], a[204:207], v[84:87], v[104:107]\n"
"v_mfma_f32_16x16x32_bf16 v[56:59], a[192:195], v[84:87], v[56:59]\n"
"buffer_load_dwordx4 a[88:91], v2, s[16:19], s37 offen offset:2048\n"
"v_mfma_f32_16x16x32_bf16 v[68:71], a[196:199], v[84:87], v[68:71]\n"
"v_mfma_f32_16x16x32_bf16 v[92:95], a[200:203], v[88:91], v[92:95]\n"
"v_mfma_f32_16x16x32_bf16 a[60:63], a[204:207], v[88:91], a[60:63]\n"
"v_mfma_f32_16x16x32_bf16 a[68:71], a[192:195], v[88:91], a[68:71]\n"
"buffer_load_dwordx4 a[96:99], v3, s[16:19], s37 offen\n"
"v_mfma_f32_16x16x32_bf16 a[240:243], a[196:199], v[88:91], a[240:243]\n"
"s_waitcnt vmcnt(17)\n"
"v_swap_b32 v82, v76\n"
"v_swap_b32 v83, v77\n"
"s_nop 1\n"
"v_mfma_f32_16x16x32_bf16 v[96:99], a[216:219], v[80:83], v[96:99]\n"
"v_mfma_f32_16x16x32_bf16 v[104:107], a[220:223], v[80:83], v[104:107]\n"
"buffer_load_dwordx4 a[104:107], v3, s[16:19], s37 offen offset:2048\n"
"v_mfma_f32_16x16x32_bf16 v[56:59], a[208:211], v[80:83], v[56:59]\n"
"v_mfma_f32_16x16x32_bf16 v[68:71], a[212:215], v[80:83], v[68:71]\n"
"v_mfma_f32_16x16x32_bf16 v[92:95], a[216:219], v[76:79], v[92:95]\n"
"v_mfma_f32_16x16x32_bf16 a[60:63], a[220:223], v[76:79], a[60:63]\n"
"buffer_load_dwordx4 a[112:115], v4, s[16:19], s37 offen\n"
"v_mfma_f32_16x16x32_bf16 a[68:71], a[208:211], v[76:79], a[68:71]\n"
"v_mfma_f32_16x16x32_bf16 a[240:243], a[212:215], v[76:79], a[240:243]\n"
"s_waitcnt vmcnt(17)\n"
"v_swap_b32 v74, v64\n"
"v_swap_b32 v75, v65\n"
"s_nop 1\n"
"v_mfma_f32_16x16x32_bf16 v[96:99], a[232:235], v[72:75], v[96:99]\n"
"buffer_load_dwordx4 a[120:123], v4, s[16:19], s37 offen offset:2048\n"
"v_mfma_f32_16x16x32_bf16 v[104:107], a[236:239], v[72:75], v[104:107]\n"
"v_mfma_f32_16x16x32_bf16 v[56:59], a[224:227], v[72:75], v[56:59]\n"
"v_mfma_f32_16x16x32_bf16 v[68:71], a[228:231], v[72:75], v[68:71]\n"
"s_waitcnt vmcnt(17)\n"
"v_mfma_f32_16x16x32_bf16 v[96:99], a[100:103], v[112:115], v[96:99]\n"
"buffer_load_dwordx4 a[36:39], v3, s[16:19], s36 offen offset:1024\n"
"v_mfma_f32_16x16x32_bf16 v[104:107], a[92:95], v[112:115], v[104:107]\n"
"v_mfma_f32_16x16x32_bf16 v[92:95], a[232:235], v[64:67], v[92:95]\n"
"v_mfma_f32_16x16x32_bf16 a[60:63], a[236:239], v[64:67], a[60:63]\n"
"s_waitcnt vmcnt(17)\n"
"v_mfma_f32_16x16x32_bf16 v[96:99], a[168:171], v[108:111], v[96:99]\n"
"buffer_load_dwordx4 a[44:47], v3, s[16:19], s36 offen offset:3072\n"
"v_mfma_f32_16x16x32_bf16 v[104:107], a[172:175], v[108:111], v[104:107]\n"
"v_mfma_f32_16x16x32_bf16 a[68:71], a[224:227], v[64:67], a[68:71]\n"
"v_mfma_f32_16x16x32_bf16 a[240:243], a[228:231], v[64:67], a[240:243]\n"
"v_mfma_f32_16x16x32_bf16 v[56:59], a[116:119], v[112:115], v[56:59]\n"
"buffer_load_dwordx4 a[52:55], v4, s[16:19], s36 offen offset:1024\n"
"s_waitcnt vmcnt(18)\n"
"v_mfma_f32_16x16x32_bf16 v[96:99], a[184:187], v[100:103], v[96:99]\n"
"ds_write_b128 v15, v[92:95] offset:32768\n"
"v_mfma_f32_16x16x32_bf16 v[104:107], a[188:191], v[100:103], v[104:107]\n"
"ds_write_b128 v15, a[60:63] offset:33792\n"
"v_mfma_f32_16x16x32_bf16 v[68:71], a[108:111], v[112:115], v[68:71]\n"
"buffer_load_dwordx4 a[60:63], v4, s[16:19], s36 offen offset:3072\n"
"v_mfma_f32_16x16x32_bf16 v[56:59], a[84:87], v[108:111], v[56:59]\n"
"ds_write_b128 v14, a[68:71] offset:32768\n"
"v_mfma_f32_16x16x32_bf16 v[68:71], a[164:167], v[108:111], v[68:71]\n"
"ds_write_b128 v14, a[240:243] offset:33792\n"
"v_mfma_f32_16x16x32_bf16 v[56:59], a[176:179], v[100:103], v[56:59]\n"
"buffer_load_dwordx4 a[68:71], v1, s[16:19], s37 offen offset:1024\n"
"v_mfma_f32_16x16x32_bf16 v[68:71], a[180:183], v[100:103], v[68:71]\n"
"ds_write_b128 v13, v[96:99] offset:32768\n"
"v_cmp_u_f32 vcc, v92, v96\n"
"ds_write_b128 v13, v[104:107] offset:33792\n"
"buffer_load_dwordx4 a[76:79], v1, s[16:19], s37 offen offset:3072\n"
"s_waitcnt lgkmcnt(0)\n"
"s_nop 7\n"
"s_cbranch_vccnz Rw4B%=\n"
"s_branch Cw4B%=\n"
"LEND%=:\n"
"kp_code_z:\n"
               :
               : "{v0}"(threadIdx.x), "{s[0:1]}"(ka), "{s2}"(blockIdx.x), "{s3}"(lb)
               : "v1", "v2", "v3", "v4", "v5", "v6", "v7", "v8", "v9", "v10", "v11", "v12", "v13", "v14", "v15", "v16", "v17", "v18", "v19", "v20", "v21", "v22", "v23", "v24",
"v25", "v26", "v27", "v28", "v29", "v30", "v31", "v32", "v33", "v34", "v35", "v36", "v37", "v38", "v39", "v40", "v41", "v42", "v43", "v44", "v45", "v46", "v47", "v48",
"v49", "v50", "v51", "v52", "v53", "v54", "v55", "v56", "v57", "v58", "v59", "v60", "v61", "v62", "v63", "v64", "v65", "v66", "v67", "v68", "v69", "v70", "v71", "v72",
"v73", "v74", "v75", "v76", "v77", "v78", "v79", "v80", "v81", "v82", "v83", "v84", "v85", "v86", "v87", "v88", "v89", "v90", "v91", "v92", "v93", "v94", "v95", "v96",
"v97", "v98", "v99", "v100", "v101", "v102", "v103", "v104", "v105", "v106", "v107", "v108", "v109", "v110", "v111", "v112", "v113", "v114", "v115", "v116", "v117", "v118", "v119", "v120",
"v121", "v122", "v123", "v124", "v125", "v126", "v127", "v128", "v129", "v130", "v131", "v132", "v133", "v134", "v135", "v136", "v137", "v138", "v139", "v140", "v141", "v142", "v143", "v144",
"v145", "v146", "v147", "v148", "v149", "v150", "v151", "v152", "v153", "v154", "v155", "v156", "v157", "v158", "v159", "v160", "v161", "v162", "v163", "v164", "v165", "v166", "v167", "v168",
"v169", "v170", "v171", "v172", "v173", "v174", "v175", "v176", "v177", "v178", "v179", "v180", "v181", "v182", "v183", "v184", "v185", "v186", "v187", "v188", "v189", "v190", "v191", "v192",
"v193", "v194", "v195", "v196", "v197", "v198", "v199", "v200", "v201", "v202", "v203", "v204", "v205", "v206", "v207", "v208", "v209", "v210", "v211", "v212", "v213", "v214", "v215", "v216",
"v217", "v218", "v219", "v220", "v221", "v222", "v223", "v224", "v225", "v226", "v227", "v228", "v229", "v230", "v231", "v232", "v233", "v234", "v235", "v236", "v237", "v238", "v239", "v240",
"v241", "v242", "v243", "v244", "v245", "v246", "v247", "v248", "v249", "v250", "v251", "v252", "v253", "v254", "v255", "a0", "a1", "a2", "a3", "a4", "a5", "a6", "a7", "a8",
"a9", "a10", "a11", "a12", "a13", "a14", "a15", "a16", "a17", "a18", "a19", "a20", "a21", "a22", "a23", "a24", "a25", "a26", "a27", "a28", "a29", "a30", "a31", "a32",
"a33", "a34", "a35", "a36", "a37", "a38", "a39", "a40", "a41", "a42", "a43", "a44", "a45", "a46", "a47", "a48", "a49", "a50", "a51", "a52", "a53", "a54", "a55", "a56",
"a57", "a58", "a59", "a60", "a61", "a62", "a63", "a64", "a65", "a66", "a67", "a68", "a69", "a70", "a71", "a72", "a73", "a74", "a75", "a76", "a77", "a78", "a79", "a80",
"a81", "a82", "a83", "a84", "a85", "a86", "a87", "a88", "a89", "a90", "a91", "a92", "a93", "a94", "a95", "a96", "a97", "a98", "a99", "a100", "a101", "a102", "a103", "a104",
"a105", "a106", "a107", "a108", "a109", "a110", "a111", "a112", "a113", "a114", "a115", "a116", "a117", "a118", "a119", "a120", "a121", "a122", "a123", "a124", "a125", "a126", "a127", "a128",
"a129", "a130", "a131", "a132", "a133", "a134", "a135", "a136", "a137", "a138", "a139", "a140", "a141", "a142", "a143", "a144", "a145", "a146", "a147", "a148", "a149", "a150", "a151", "a152",
"a153", "a154", "a155", "a156", "a157", "a158", "a159", "a160", "a161", "a162", "a163", "a164", "a165", "a166", "a167", "a168", "a169", "a170", "a171", "a172", "a173", "a174", "a175", "a176",
"a177", "a178", "a179", "a180", "a181", "a182", "a183", "a184", "a185", "a186", "a187", "a188", "a189", "a190", "a191", "a192", "a193", "a194", "a195", "a196", "a197", "a198", "a199", "a200",
"a201", "a202", "a203", "a204", "a205", "a206", "a207", "a208", "a209", "a210", "a211", "a212", "a213", "a214", "a215", "a216", "a217", "a218", "a219", "a220", "a221", "a222", "a223", "a224",
"a225", "a226", "a227", "a228", "a229", "a230", "a231", "a232", "a233", "a234", "a235", "a236", "a237", "a238", "a239", "a240", "a241", "a242", "a243", "a244", "a245", "a246", "a247", "a248",
"a249", "a250", "a251", "a252", "a253", "a254", "a255", "s4", "s5", "s6", "s7", "s8", "s9", "s10", "s11", "s12", "s13", "s14", "s15", "s16", "s17", "s18", "s19", "s20",
"s21", "s22", "s23", "s24", "s25", "s26", "s27", "s28", "s29", "s30", "s31", "s32", "s33", "s34", "s35", "s36", "s37", "s38", "s39", "s40", "s41", "s42", "s43", "s44",
"s45", "s46", "s47", "s48", "s49", "s50", "s51", "s52", "s53", "s54", "s55", "s56", "s57", "s58", "s59", "s60", "s61", "s62", "s63", "s64", "s65", "s66", "s67", "s68",
"s69", "s70", "s71", "s72", "s73", "s74", "s75", "s76", "s77", "s78", "s79", "s80", "s81", "s82", "s83", "s84", "s85", "s86", "s87", "s88", "s89", "s90", "s91", "s92",
"s93", "s94", "s95", "s96", "s97", "s98", "s99", "s100", "s101", "vcc", "scc", "memory");
}

extern "C" void kernel_launch(void* const* d_in, const int* in_sizes, int n_in, void* d_out, int out_size,
                              void* d_ws, size_t ws_size, hipStream_t stream) {
  if (ws_size < WS_BYTES) return;
  k0_prep<<<256 + NCAL, 512, 0, stream>>>((const u16*)d_in[1], (const u16*)d_in[5], (const u16*)d_in[7], (const u16*)d_in[3],
                                   (const u16*)d_in[6], (const u16*)d_in[8], (const u16*)d_in[0], (const u16*)d_in[2],
                                   (char*)d_ws);
  kp_main<<<256, 256, 0, stream>>>((char*)d_ws, (u16*)d_out);
}
```
